```python
import torch

def _softmax(x, axis=-1):
    return torch.softmax(x, dim=axis)
def _gelu(x):
    return torch.nn.functional.gelu(x, approximate="tanh")
def _silu(x):
    return torch.nn.functional.silu(x)
def _sigmoid(x):
    return torch.sigmoid(x)
def _layernorm(x, axis=-1, eps=1e-5):
    m = x.mean(dim=axis, keepdim=True)
    v = x.var(dim=axis, keepdim=True, unbiased=False)
    return (x - m) / torch.sqrt(v + eps)
def _scatter_add(tgt, idx, src, axis=0):
    return tgt.clone().index_add_(axis, idx, src)
def _segment_sum(src, seg, n):
    out = torch.zeros((n,) + src.shape[1:], dtype=src.dtype)
    return out.index_add_(0, seg, src)
def _pad(x, pw):
    flat = tuple(v for pair in reversed(pw) for v in pair)
    return torch.nn.functional.pad(x, flat)

kernel_name = 'parallel_sum__sliding_local_dilated__glu_gated_swiglu__prompt'

def setup_inputs():

    _w = (lambda a: (lambda t: t.to(torch.bfloat16) if t.is_floating_point() else t)(torch.as_tensor(a)))
    def _A_setup():
        B, S, D = 1, 2048, 512
        return {
            "x": _w((lambda *s: torch.randn(*s, dtype=torch.bfloat16) * 0.05)(B, S, D)),
            "wqkv": _w((lambda *s: torch.randn(*s, dtype=torch.bfloat16) * 0.05)(D, 3 * D)),
            "wo": _w((lambda *s: torch.randn(*s, dtype=torch.bfloat16) * 0.05)(D, D)),
        }
    _A_in = _A_setup()

    def _B_setup():
        B, S, D, F = 1, 2048, 512, 1376
        return {
            "x": _w((lambda *s: torch.randn(*s, dtype=torch.bfloat16) * 0.05)(B, S, D)),
            "w1": _w((lambda *s: torch.randn(*s, dtype=torch.bfloat16) * 0.8)(D, F) * (2.0 / D) ** 0.5),
            "w3": _w((lambda *s: torch.randn(*s, dtype=torch.bfloat16) * 0.8)(D, F) * (2.0 / D) ** 0.5),
            "w2": _w((lambda *s: torch.randn(*s, dtype=torch.bfloat16) * 0.8)(F, D) * (2.0 / F) ** 0.5),
        }
    _B_in = _B_setup()

    return {
        "A_x": _A_in["x"],
        "A_wqkv": _A_in["wqkv"],
        "A_wo": _A_in["wo"],
        "B_x": _B_in["x"],
        "B_w1": _B_in["w1"],
        "B_w3": _B_in["w3"],
        "B_w2": _B_in["w2"],
    }

def reference(A_x, A_wqkv, A_wo, B_x, B_w1, B_w3, B_w2):
    def _A_ref(x, wqkv, wo):
        B, S, D = x.shape
        H = 32; Dh = D // H
        W, dil = 8, 2
        qkv = (lambda x, *d: x.permute(*d))((x @ wqkv).reshape(B, S, 3, H, Dh), 2, 0, 3, 1, 4)
        q, k, v = qkv[0], qkv[1], qkv[2]
        scale = Dh ** -0.5
        pos = torch.arange(S)
        off = torch.arange(W) * dil
        idx = pos[:, None] - off[None, :]
        mask = idx >= 0
        idx_c = torch.where(mask, idx, 0).reshape(-1)
        kg = k[:, :, idx_c, :].reshape(B, H, S, W, Dh)
        vg = v[:, :, idx_c, :].reshape(B, H, S, W, Dh)
        logits = torch.einsum("bhsd,bhswd->bhsw", q, kg) * scale
        logits = torch.where(mask[None, None, :, :], logits, -1e9)
        attn = _softmax(logits, axis=-1)
        o = torch.einsum("bhsw,bhswd->bhsd", attn, vg)
        return (lambda x, *d: x.permute(*d))(o, 0, 2, 1, 3).reshape(B, S, D) @ wo

    def _B_ref(x, w1, w3, w2):
        gate = torch.einsum("bsd,df->bsf", x, w1)
        up = torch.einsum("bsd,df->bsf", x, w3)
        h = _silu(gate) * up
        out = torch.einsum("bsf,fd->bsd", h, w2)
        return out

    x = A_x
    xn = _layernorm(x, axis=-1)
    return x + _A_ref(xn, A_wqkv, A_wo) + _B_ref(xn, B_w1, B_w3, B_w2)
```

```cpp
#include <hip/hip_runtime.h>
#include <stdint.h>

#define LAS __attribute__((address_space(3)))
#define DI __device__ __forceinline__
typedef __bf16 bf16x8 __attribute__((ext_vector_type(8)));
typedef __bf16 bf16x4 __attribute__((ext_vector_type(4)));
typedef __bf16 bf16x2 __attribute__((ext_vector_type(2)));
typedef short s16x4 __attribute__((ext_vector_type(4)));
typedef float f32x4 __attribute__((ext_vector_type(4)));
typedef float f32x2 __attribute__((ext_vector_type(2)));
typedef unsigned u32x2 __attribute__((ext_vector_type(2)));
typedef unsigned u32x4 __attribute__((ext_vector_type(4)));

constexpr int NKS = 59;
constexpr int SLOT = 53248;
constexpr int WOFF = 16384, EOFF = 49152;
constexpr int P1_LDS = 3 * SLOT;
constexpr int P2_SLOT = 32768, P2_NSLOT = 4;
constexpr int P2_LDS = P2_SLOT * P2_NSLOT;

DI int rfl(int v) { return __builtin_amdgcn_readfirstlane(v); }
DI void glds16(const void* g, unsigned lds_dst) {
  unsigned keep;
  asm volatile("s_mov_b32 %0, m0\n\ts_mov_b32 m0, %2\n\ts_nop 0\n\tglobal_load_lds_dwordx4 %1, off\n\ts_mov_b32 m0, %0"
               : "=&s"(keep) : "v"(g), "s"(lds_dst) : "memory");
}
#define WAITV(n) asm volatile("s_waitcnt vmcnt(" #n ")" ::: "memory")
#define WAITL0() asm volatile("s_waitcnt lgkmcnt(0)" ::: "memory")
#define BAR() do { asm volatile("" ::: "memory"); __builtin_amdgcn_s_barrier(); asm volatile("" ::: "memory"); } while (0)

DI bf16x8 ldsA(LAS char* p) { return *(LAS bf16x8*)p; }
DI bf16x8 ldsB(LAS char* p, int toff) {
  s16x4 lo = __builtin_amdgcn_ds_read_tr16_b64_v4i16((LAS s16x4*)p);
  s16x4 hi = __builtin_amdgcn_ds_read_tr16_b64_v4i16((LAS s16x4*)(p + toff));
  return __builtin_bit_cast(bf16x8, __builtin_shufflevector(lo, hi, 0, 1, 2, 3, 4, 5, 6, 7));
}
DI f32x4 mfma32(bf16x8 a, bf16x8 b, f32x4 c) { return __builtin_amdgcn_mfma_f32_16x16x32_bf16(a, b, c, 0, 0, 0); }
DI f32x4 mfma16(s16x4 a, s16x4 b, f32x4 c) { return __builtin_amdgcn_mfma_f32_16x16x16bf16_1k(a, b, c, 0, 0, 0); }
DI float dot2(bf16x2 a, bf16x2 b, float c) { return __builtin_amdgcn_fdot2_f32_bf16(a, b, c, false); }
DI float dot_ones(bf16x8 v, float c) {
  const bf16x2 o = {(__bf16)1.0f, (__bf16)1.0f};
  c = dot2(__builtin_shufflevector(v, v, 0, 1), o, c); c = dot2(__builtin_shufflevector(v, v, 2, 3), o, c);
  c = dot2(__builtin_shufflevector(v, v, 4, 5), o, c); c = dot2(__builtin_shufflevector(v, v, 6, 7), o, c);
  return c;
}
DI float dot_self(bf16x8 v, float c) {
  bf16x2 a = __builtin_shufflevector(v, v, 0, 1), b = __builtin_shufflevector(v, v, 2, 3), d = __builtin_shufflevector(v, v, 4, 5), e = __builtin_shufflevector(v, v, 6, 7);
  c = dot2(a, a, c); c = dot2(b, b, c); c = dot2(d, d, c); c = dot2(e, e, c);
  return c;
}
DI s16x4 pack4(float a, float b, float c, float d) {
  bf16x4 v = __builtin_convertvector((f32x4){a, b, c, d}, bf16x4);
  return __builtin_bit_cast(s16x4, v);
}
DI float sumg(float v) { v += __shfl_xor(v, 16); v += __shfl_xor(v, 32); return v; }
DI float maxg(float v) { v = fmaxf(v, __shfl_xor(v, 16)); v = fmaxf(v, __shfl_xor(v, 32)); return v; }
DI float silu_mul(float g, float u) {
  float e = __builtin_amdgcn_exp2f(g * -1.44269504089f);
  return g * u * __builtin_amdgcn_rcpf(1.0f + e);
}

struct Params {
  const char* x; const char* wqkv; const char* wo; const char* w1; const char* w3; const char* w2;
  char* oh; char* out;
};

constexpr int SC_CSP = 0;
constexpr int SC_ST = 20480;
constexpr int SC_CS = 21760;
constexpr int SC_MR = 23040;
constexpr int SC_KV = 24576;

template <bool TA>
DI void phase1(const Params& p, LAS char* lds, int rg, int cg) {
  const int tid = threadIdx.x, lane = tid & 63;
  const int w = rfl(tid >> 6), wi = w & 3, wj = w >> 2;
  const int r16 = lane & 15, g = lane >> 4, q = r16 >> 2, pp = lane & 3;
  const unsigned ldsb = (unsigned)(uintptr_t)lds;
  const int row0 = rg * 128;
  const int t0 = TA ? 2 * cg : 16 + 9 * cg;
  const char* px[2];
#pragma unroll
  for (int m = 0; m < 2; ++m) {
    const int row = 16 * w + 8 * m + (lane >> 3);
    const int ch = (lane & 7) ^ ((row >> 1) & 7);
    px[m] = p.x + (size_t)(row0 + row) * 1024 + ch * 16;
  }
  const char* pw[4]; unsigned adv[4];
#pragma unroll
  for (int m = 0; m < 4; ++m) {
    const int kr = 8 * w + 2 * m + (lane >> 5), pc = lane & 31, u = pc >> 1;
    const int f = (kr & 3) | (((kr >> 3) & 1) << 2);
    const int c = (u & 8) | ((u & 7) ^ f);
    const char* base; int ld, colb;
    if (TA) {
      if (c < 12) { base = p.wqkv; ld = 3072; colb = ((c >> 2) * 512 + (4 * cg + (c & 3)) * 16) * 2; }
      else { base = (c < 14) ? p.w1 : p.w3; ld = 2752; colb = (t0 + (c & 1)) * 32; }
    } else {
      base = (c < 8) ? p.w1 : p.w3; ld = 2752;
      int t = t0 + (c & 7); t = t > 85 ? 85 : t; colb = t * 32;
    }
    pw[m] = base + (size_t)kr * ld + colb + (pc & 1) * 16;
    adv[m] = 64u * ld;
  }
  const bool has_e = TA ? (w < 2) : (w < 4);
  const char* pe; unsigned adve;
  if (TA) {
    const int row = 8 * (w & 1) + (lane >> 3);
    const int ch = (lane & 7) ^ ((row >> 1) & 7);
    int gr = row0 - 16 + row; gr = gr < 0 ? 0 : gr;
    pe = p.x + (size_t)gr * 1024 + ch * 16; adve = 128;
  } else {
    const int kr = 16 * (w & 3) + (lane >> 2), pc = lane & 3;
    const int e = (pc >> 1) ^ ((kr >> 3) & 1);
    int t = t0 + 8; t = t > 85 ? 85 : t;
    pe = (e ? p.w3 : p.w1) + (size_t)kr * 2752 + t * 32 + (pc & 1) * 16; adve = 64u * 2752;
  }
  auto issue = [&](int so) {
    if (has_e) { glds16(pe, ldsb + so + EOFF + w * 1024); pe += adve; }
    glds16(px[0], ldsb + so + (2 * w) * 1024); glds16(px[1], ldsb + so + (2 * w + 1) * 1024);
    px[0] += 128; px[1] += 128;
#pragma unroll
    for (int m = 0; m < 4; ++m) { glds16(pw[m], ldsb + so + WOFF + (4 * w + m) * 1024); pw[m] += adv[m]; }
  };
  const int xa0 = r16 * 128 + ((g ^ (r16 >> 1)) << 4);
  const int fl = q | ((g & 1) << 2);
  const int wl = (8 * g + q) * 512 + (fl << 5) + pp * 8;
  const int el = (8 * g + q) * 64 + ((g & 1) << 5) + pp * 8;
  constexpr int NCT = TA ? 8 : 10;
  f32x4 acc[NCT][2];
  float cs[NCT];
#pragma unroll
  for (int c = 0; c < NCT; ++c) { acc[c][0] = (f32x4){0, 0, 0, 0}; acc[c][1] = (f32x4){0, 0, 0, 0}; cs[c] = 0.f; }
  f32x4 acch = {0, 0, 0, 0};
  float st0 = 0.f, st1 = 0.f, sth = 0.f;
  const int wq = TA ? (wl ^ ((2 * wj) << 5)) : (wl ^ ((4 * wj) << 5));
  const int wf = wl ^ (wj << 5);
  const int hunit = (wi & 1 ? 8 : 4) + 2 * wj + (wi >> 1);
  const int wh = (wl ^ ((hunit & 7) << 5)) + (hunit & 8) * 32;

  issue(0); issue(SLOT);
  int so = 0, so2 = 2 * SLOT;
  for (int s = 0; s < 8; ++s) {
    if (s < 7) WAITV(6); else WAITV(0);
    WAITL0();
    BAR();
    if (s < 6) issue(so2);
    LAS char* sb = lds + so;
#pragma unroll
    for (int ks = 0; ks < 2; ++ks) {
      const int xa = (xa0 ^ (ks * 64)) + wi * 4096;
      const bf16x8 a0 = ldsA(sb + xa), a1 = ldsA(sb + xa + 2048);
      bf16x8 b[NCT];
      LAS char* wb = sb + WOFF + ks * 16384;
      if (TA) {
        b[0] = ldsB(wb + wq, 2048); b[1] = ldsB(wb + (wq ^ 32), 2048);
        b[2] = ldsB(wb + (wq ^ 128), 2048); b[3] = ldsB(wb + (wq ^ 160), 2048);
        b[4] = ldsB(wb + wq + 256, 2048); b[5] = ldsB(wb + (wq ^ 32) + 256, 2048);
        b[6] = ldsB(wb + (wf ^ 128) + 256, 2048); b[7] = ldsB(wb + (wf ^ 192) + 256, 2048);
      } else {
#pragma unroll
        for (int c = 0; c < 4; ++c) { b[c] = ldsB(wb + (wq ^ (c << 5)), 2048); b[4 + c] = ldsB(wb + (wq ^ (c << 5)) + 256, 2048); }
      }
#pragma unroll
      for (int c = 0; c < 8; ++c) {
        if (TA && (c == 4 || c == 5)) { acc[c][0] = mfma32(a0, b[c], acc[c][0]); acc[c][1] = mfma32(a1, b[c], acc[c][1]); }
        else { acc[c][0] = mfma32(b[c], a0, acc[c][0]); acc[c][1] = mfma32(b[c], a1, acc[c][1]); }
      }
      if (TA) {
        const bf16x8 ah = ldsA(sb + EOFF + (xa0 ^ (ks * 64)));
        const bf16x8 bh = ldsB(wb + wh, 2048);
        acch = mfma32(bh, ah, acch);
        if (wi == 0) sth = wj ? dot_self(ah, sth) : dot_ones(ah, sth);
      } else if (wj == 0) {
        LAS char* eb = sb + EOFF + ks * 2048;
        b[8] = ldsB(eb + el, 256); b[9] = ldsB(eb + (el ^ 32), 256);
        acc[8][0] = mfma32(b[8], a0, acc[8][0]); acc[8][1] = mfma32(b[8], a1, acc[8][1]);
        acc[9][0] = mfma32(b[9], a0, acc[9][0]); acc[9][1] = mfma32(b[9], a1, acc[9][1]);
        if (((2 * s + ks) & 3) == wi) { cs[8] = dot_ones(b[8], cs[8]); cs[9] = dot_ones(b[9], cs[9]); }
      }
      if (wj) { st0 = dot_self(a0, st0); st1 = dot_self(a1, st1); }
      else { st0 = dot_ones(a0, st0); st1 = dot_ones(a1, st1); }
      if (((2 * s + ks) & 3) == wi) {
#pragma unroll
        for (int c = 0; c < 8; ++c) cs[c] = dot_ones(b[c], cs[c]);
      }
    }
    so2 = so; so = (so == 2 * SLOT) ? 0 : so + SLOT;
  }
  {
    LAS float* csp = (LAS float*)(lds + SC_CSP) + w * 640 + lane;
#pragma unroll
    for (int c = 0; c < NCT; ++c) csp[c * 64] = cs[c];
    st0 = sumg(st0); st1 = sumg(st1);
    LAS float* st = (LAS float*)(lds + SC_ST) + wj * 144;
    if (lane < 16) { st[32 * wi + lane] = st0; st[32 * wi + 16 + lane] = st1; }
    if (TA && wi == 0) { sth = sumg(sth); if (lane < 16) st[128 + lane] = sth; }
  }
  WAITL0(); BAR();
  if (tid < 2 * NCT * 16) {
    const int j = tid / (NCT * 16), c = (tid / 16) % NCT, n = tid & 15;
    LAS float* csp = (LAS float*)(lds + SC_CSP) + (4 * j) * 640 + c * 64 + n;
    float sum = 0.f;
#pragma unroll
    for (int i = 0; i < 4; ++i)
#pragma unroll
      for (int gg = 0; gg < 4; ++gg) sum += csp[i * 640 + gg * 16];
    ((LAS float*)(lds + SC_CS))[(j * 10 + c) * 16 + n] = sum;
  } else if (tid >= 320 && tid < 320 + (TA ? 144 : 128)) {
    const int r = tid - 320;
    LAS float* st = (LAS float*)(lds + SC_ST);
    const float m = st[r] * (1.0f / 512.0f);
    const float var = fmaxf(st[144 + r] * (1.0f / 512.0f) - m * m, 0.f);
    ((LAS f32x2*)(lds + SC_MR))[r] = (f32x2){m, __builtin_amdgcn_rsqf(var + 1e-5f)};
  }
  WAITL0(); BAR();
  LAS f32x4* CS = (LAS f32x4*)(lds + SC_CS) + wj * 40;
  LAS f32x2* MR = (LAS f32x2*)(lds + SC_MR);
  const int RT0 = rg * 8 + 2 * wi;
  char* ohl = p.oh + (size_t)lane * 16;
  f32x2 mr[2]; mr[0] = MR[32 * wi + r16]; mr[1] = MR[32 * wi + 16 + r16];
  auto ffn_tile = [&](int cgt, int cup, int tile) {
    const f32x4 cg4 = CS[cgt * 4 + g], cu4 = CS[cup * 4 + g];
#pragma unroll
    for (int rt = 0; rt < 2; ++rt) {
      float h[4];
#pragma unroll
      for (int r = 0; r < 4; ++r) {
        const float gv = mr[rt][1] * (acc[cgt][rt][r] - mr[rt][0] * cg4[r]);
        const float uv = mr[rt][1] * (acc[cup][rt][r] - mr[rt][0] * cu4[r]);
        h[r] = silu_mul(gv, uv);
      }
      if (tile <= 85)
        *(s16x4*)(ohl + ((size_t)(RT0 + rt) * NKS + 16 + (tile >> 1)) * 1024 + (tile & 1) * 8) = pack4(h[0], h[1], h[2], h[3]);
    }
  };
  if (!TA) {
#pragma unroll
    for (int c = 0; c < 4; ++c) ffn_tile(c, 4 + c, t0 + 4 * wj + c);
    if (wj == 0) ffn_tile(8, 9, t0 + 8);
  } else {
    ffn_tile(6, 7, t0 + wj);
    s16x4 Q[2][2], K[2][2], V[2][2];
#pragma unroll
    for (int hl = 0; hl < 2; ++hl) {
      const f32x4 cq = CS[hl * 4 + g], ck = CS[(2 + hl) * 4 + g];
      const float cv = ((LAS float*)CS)[(4 + hl) * 16 + r16];
#pragma unroll
      for (int rt = 0; rt < 2; ++rt) {
        float a[4], bb[4], cc[4];
#pragma unroll
        for (int r = 0; r < 4; ++r) {
          a[r] = mr[rt][1] * (acc[hl][rt][r] - mr[rt][0] * cq[r]);
          bb[r] = mr[rt][1] * (acc[2 + hl][rt][r] - mr[rt][0] * ck[r]);
          const f32x2 t = MR[32 * wi + 16 * rt + 4 * g + r];
          cc[r] = t[1] * (acc[4 + hl][rt][r] - t[0] * cv);
        }
        Q[hl][rt] = pack4(a[0], a[1], a[2], a[3]); K[hl][rt] = pack4(bb[0], bb[1], bb[2], bb[3]); V[hl][rt] = pack4(cc[0], cc[1], cc[2], cc[3]);
      }
    }
    LAS char* kvb = lds + SC_KV + wj * 10240;
#pragma unroll
    for (int hl = 0; hl < 2; ++hl) {
      *(LAS s16x4*)(kvb + (wi + 1) * 2048 + hl * 1024 + lane * 8) = K[hl][1];
      *(LAS s16x4*)(kvb + (wi + 1) * 2048 + hl * 1024 + 512 + lane * 8) = V[hl][1];
    }
    {
      const int hl = wi >> 1, kind = wi & 1;
      const f32x4 ch = CS[((kind ? 4 : 2) + hl) * 4 + g];
      const f32x2 t = MR[128 + r16];
      const s16x4 hv = pack4(t[1] * (acch[0] - t[0] * ch[0]), t[1] * (acch[1] - t[0] * ch[1]), t[1] * (acch[2] - t[0] * ch[2]), t[1] * (acch[3] - t[0] * ch[3]));
      *(LAS s16x4*)(kvb + hl * 1024 + (kind ? 512 + r16 * 32 + g * 8 : lane * 8)) = hv;
    }
    WAITL0(); BAR();
    unsigned mk = 0;
#pragma unroll
    for (int r = 0; r < 4; ++r) {
      const int dp = r16 + 16 - (4 * g + r), dc = r16 - (4 * g + r);
      if (!(dp & 1) && dp <= 14) mk |= 1u << r;
      if (!(dc & 1) && dc >= 0 && dc <= 14) mk |= 16u << r;
    }
    u32x2 ost[2][2];
#pragma unroll
    for (int hl = 0; hl < 2; ++hl) {
      const s16x4 Kp = *(LAS s16x4*)(kvb + wi * 2048 + hl * 1024 + lane * 8);
      s16x4 Vp;
      if (wi == 0) Vp = __builtin_amdgcn_ds_read_tr16_b64_v4i16((LAS s16x4*)(kvb + hl * 1024 + 512 + (4 * g + q) * 32 + pp * 8));
      else Vp = *(LAS s16x4*)(kvb + wi * 2048 + hl * 1024 + 512 + lane * 8);
#pragma unroll
      for (int rt = 0; rt < 2; ++rt) {
        const s16x4 kp = rt ? K[hl][0] : Kp, vp = rt ? V[hl][0] : Vp;
        const f32x4 z = {0, 0, 0, 0};
        const f32x4 sp = mfma16(kp, Q[hl][rt], z), sc = mfma16(K[hl][rt], Q[hl][rt], z);
        unsigned m = mk; if (RT0 + rt == 0) m &= 0xF0u;
        float l[8];
#pragma unroll
        for (int r = 0; r < 4; ++r) { l[r] = (m >> r) & 1 ? sp[r] : -3.0e38f; l[4 + r] = (m >> (4 + r)) & 1 ? sc[r] : -3.0e38f; }
        float mx = fmaxf(fmaxf(fmaxf(l[0], l[1]), fmaxf(l[2], l[3])), fmaxf(fmaxf(l[4], l[5]), fmaxf(l[6], l[7])));
        mx = maxg(mx);
        const float sc2 = 0.25f * 1.44269504089f;
        float sum = 0.f;
#pragma unroll
        for (int r = 0; r < 8; ++r) { l[r] = (m >> r) & 1 ? __builtin_amdgcn_exp2f((l[r] - mx) * sc2) : 0.f; sum += l[r]; }
        sum = sumg(sum);
        const float inv = __builtin_amdgcn_rcpf(sum);
        const s16x4 p0 = pack4(l[0] * inv, l[1] * inv, l[2] * inv, l[3] * inv), p1 = pack4(l[4] * inv, l[5] * inv, l[6] * inv, l[7] * inv);
        const bf16x8 pf = __builtin_bit_cast(bf16x8, __builtin_shufflevector(p0, p1, 0, 1, 2, 3, 4, 5, 6, 7));
        const bf16x8 vf = __builtin_bit_cast(bf16x8, __builtin_shufflevector(vp, V[hl][rt], 0, 1, 2, 3, 4, 5, 6, 7));
        const f32x4 o = mfma32(vf, pf, z);
        ost[hl][rt] = __builtin_bit_cast(u32x2, pack4(o[0], o[1], o[2], o[3]));
      }
    }
#pragma unroll
    for (int rt = 0; rt < 2; ++rt)
      *(u32x4*)(ohl + ((size_t)(RT0 + rt) * NKS + 2 * cg + wj) * 1024) = (u32x4){ost[0][rt][0], ost[0][rt][1], ost[1][rt][0], ost[1][rt][1]};
  }
}

__global__ __launch_bounds__(512, 2) void k_phase1(Params p) {
  extern __shared__ __attribute__((aligned(16))) char shm[];
  LAS char* lds = (LAS char*)shm;
  const int b = blockIdx.x, xcd = b & 7, idx = b >> 3;
  const int rg = (xcd & 1) * 8 + (idx & 7), cg = (xcd >> 1) * 4 + (idx >> 3);
  if (cg < 8) phase1<true>(p, lds, rg, cg); else phase1<false>(p, lds, rg, cg - 8);
}

__global__ __launch_bounds__(512, 2) void k_phase2(Params p) {
  extern __shared__ __attribute__((aligned(16))) char shm[];
  LAS char* lds = (LAS char*)shm;
  const int tid = threadIdx.x, lane = tid & 63;
  const int w = rfl(tid >> 6), mi = w & 1, nj = w >> 1;
  const int r16 = lane & 15, g = lane >> 4, q = r16 >> 2, pp = lane & 3;
  const unsigned ldsb = (unsigned)(uintptr_t)lds;
  const int b = blockIdx.x, xcd = b & 7, idx = b >> 3;
  const int rt64 = xcd * 4 + (idx & 3), ct64 = idx >> 2;
  const int dks = w >> 1;
  const char* pa = p.oh + ((size_t)(rt64 * 4 + 2 * (w & 1)) * NKS) * 1024 + lane * 16;
  int boff[2];
#pragma unroll
  for (int m = 0; m < 2; ++m) {
    const int row = 16 * (w & 1) + 8 * m + (lane >> 3), pc = lane & 7;
    const int c = (pc >> 1) ^ ((row >> 1) & 3);
    boff[m] = row * 1024 + ct64 * 128 + (2 * c + (pc & 1)) * 16;
  }
  auto issue = [&](int st, int so) {
    int ks = 4 * st + dks; ks = ks > NKS - 1 ? NKS - 1 : ks;
    const char* a = pa + (size_t)ks * 1024;
    glds16(a, ldsb + so + (2 * w) * 1024);
    glds16(a + NKS * 1024, ldsb + so + (2 * w + 1) * 1024);
    const char* bb = (ks < 16) ? p.wo + (size_t)ks * 32768 : p.w2 + (size_t)(ks - 16) * 32768;
    glds16(bb + boff[0], ldsb + so + 16384 + (2 * w) * 1024);
    glds16(bb + boff[1], ldsb + so + 16384 + (2 * w + 1) * 1024);
  };
  const int bl = (4 * g + q) * 128 + ((nj ^ ((2 * g + (q >> 1)) & 3)) << 5) + pp * 8;
  f32x4 acc[2] = {{0, 0, 0, 0}, {0, 0, 0, 0}};
  issue(0, 0); issue(1, P2_SLOT); issue(2, 2 * P2_SLOT);
  constexpr int NST = (NKS + 3) / 4;
  for (int s = 0; s < NST; ++s) {
    if (s < NST - 2) WAITV(8); else if (s == NST - 2) WAITV(4); else WAITV(0);
    WAITL0();
    BAR();
    if (s + 3 < NST) issue(s + 3, ((s + 3) & 3) * P2_SLOT);
    LAS char* sb = lds + (s & 3) * P2_SLOT;
    const int nk = (s == NST - 1) ? NKS - 4 * (NST - 1) : 4;
#pragma unroll
    for (int ks = 0; ks < 4; ++ks) {
      if (ks < nk) {
        const bf16x8 a0 = ldsA(sb + ks * 4096 + (2 * mi) * 1024 + lane * 16), a1 = ldsA(sb + ks * 4096 + (2 * mi + 1) * 1024 + lane * 16);
        const bf16x8 bf = ldsB(sb + 16384 + ks * 4096 + bl, 2048);
        acc[0] = mfma32(bf, a0, acc[0]); acc[1] = mfma32(bf, a1, acc[1]);
      }
    }
  }
#pragma unroll
  for (int rt = 0; rt < 2; ++rt) {
    const size_t off = (size_t)(rt64 * 64 + 32 * mi + 16 * rt + r16) * 1024 + (ct64 * 64 + 16 * nj + 4 * g) * 2;
    const bf16x4 xv = *(const bf16x4*)(p.x + off);
    const f32x4 xf = __builtin_convertvector(xv, f32x4);
    *(s16x4*)(p.out + off) = pack4(acc[rt][0] + xf[0], acc[rt][1] + xf[1], acc[rt][2] + xf[2], acc[rt][3] + xf[3]);
  }
}

extern "C" void kernel_launch(void* const* d_in, const int* in_sizes, int n_in, void* d_out, int out_size, void* d_ws, size_t ws_size,
                              hipStream_t stream) {
  Params p;
  p.x = (const char*)d_in[0]; p.wqkv = (const char*)d_in[1]; p.wo = (const char*)d_in[2];
  p.w1 = (const char*)d_in[4]; p.w3 = (const char*)d_in[5]; p.w2 = (const char*)d_in[6];
  p.oh = (char*)d_ws; p.out = (char*)d_out;
  hipFuncSetAttribute((const void*)k_phase1, hipFuncAttributeMaxDynamicSharedMemorySize, P1_LDS);
  hipFuncSetAttribute((const void*)k_phase2, hipFuncAttributeMaxDynamicSharedMemorySize, P2_LDS);
  k_phase1<<<256, 512, P1_LDS, stream>>>(p);
  k_phase2<<<256, 512, P2_LDS, stream>>>(p);
}
```

```cpp
#include <hip/hip_runtime.h>
#include <stdint.h>

#define LAS __attribute__((address_space(3)))
#define DI __device__ __forceinline__
typedef __bf16 bf16x8 __attribute__((ext_vector_type(8)));
typedef __bf16 bf16x4 __attribute__((ext_vector_type(4)));
typedef short s16x4 __attribute__((ext_vector_type(4)));
typedef float f32x4 __attribute__((ext_vector_type(4)));
typedef float f32x2 __attribute__((ext_vector_type(2)));
typedef unsigned u32x2 __attribute__((ext_vector_type(2)));
typedef unsigned u32x4 __attribute__((ext_vector_type(4)));

constexpr int NKS = 59;
constexpr int SLOT = 53248;
constexpr int WOFF = 16384, EOFF = 49152;
constexpr int DUMMY = 3 * SLOT;
constexpr int SC_CS = DUMMY + 1024;
constexpr int SC_MR = SC_CS + 1152;
constexpr int P1_LDS = 163840;
constexpr int SC_HS = 0;
constexpr int SC_KV = 1024;
constexpr int SC_FX = 8192;
constexpr int P2_LDS = 7 * 16384 + 16384;

#define STAMP(i) do {} while (0)

DI int rfl(int v) { return __builtin_amdgcn_readfirstlane(v); }
DI void glds16(const void* g, unsigned lds_dst) {
  unsigned keep;
  asm volatile("s_mov_b32 %0, m0\n\ts_mov_b32 m0, %2\n\ts_nop 0\n\tglobal_load_lds_dwordx4 %1, off\n\ts_mov_b32 m0, %0"
               : "=&s"(keep) : "v"(g), "s"(lds_dst) : "memory");
}
DI void glds16s(const char* sbase, unsigned voff, unsigned lds_dst) {
  asm volatile("s_mov_b32 m0, %2\n\ts_nop 0\n\tglobal_load_lds_dwordx4 %1, %0" :: "s"(sbase), "v"(voff), "s"(lds_dst) : "memory", "m0");
}
#define WAITV(n) asm volatile("s_waitcnt vmcnt(" #n ")" ::: "memory")
#define WAITL0() do { asm volatile("" ::: "memory"); __builtin_amdgcn_s_waitcnt(0xC07F); asm volatile("" ::: "memory"); } while (0)
#define BAR() do { asm volatile("" ::: "memory"); __builtin_amdgcn_s_barrier(); asm volatile("" ::: "memory"); } while (0)
#define SB0() __builtin_amdgcn_sched_barrier(0)

DI bf16x8 ldsA(LAS char* p) { return *(LAS bf16x8*)p; }
DI bf16x8 ldsB(LAS char* p, int toff) {
  s16x4 lo = __builtin_amdgcn_ds_read_tr16_b64_v4i16((LAS s16x4*)p);
  s16x4 hi = __builtin_amdgcn_ds_read_tr16_b64_v4i16((LAS s16x4*)(p + toff));
  return __builtin_bit_cast(bf16x8, __builtin_shufflevector(lo, hi, 0, 1, 2, 3, 4, 5, 6, 7));
}
DI f32x4 mfma32(bf16x8 a, bf16x8 b, f32x4 c) { return __builtin_amdgcn_mfma_f32_16x16x32_bf16(a, b, c, 0, 0, 0); }
DI f32x4 mfma16(s16x4 a, s16x4 b, f32x4 c) { return __builtin_amdgcn_mfma_f32_16x16x16bf16_1k(a, b, c, 0, 0, 0); }
DI s16x4 pack4(f32x4 v) { return __builtin_bit_cast(s16x4, __builtin_convertvector(v, bf16x4)); }
DI f32x4 unpack4(s16x4 v) { return __builtin_convertvector(__builtin_bit_cast(bf16x4, v), f32x4); }
DI float xg_sum(float v) {
  auto r = __builtin_amdgcn_permlane16_swap(__float_as_uint(v), __float_as_uint(v), false, false);
  v = __uint_as_float(r[0]) + __uint_as_float(r[1]);
  auto s = __builtin_amdgcn_permlane32_swap(__float_as_uint(v), __float_as_uint(v), false, false);
  return __uint_as_float(s[0]) + __uint_as_float(s[1]);
}
DI float xg_max(float v) {
  auto r = __builtin_amdgcn_permlane16_swap(__float_as_uint(v), __float_as_uint(v), false, false);
  v = fmaxf(__uint_as_float(r[0]), __uint_as_float(r[1]));
  auto s = __builtin_amdgcn_permlane32_swap(__float_as_uint(v), __float_as_uint(v), false, false);
  return fmaxf(__uint_as_float(s[0]), __uint_as_float(s[1]));
}
DI f32x4 lnc(f32x4 a, f32x2 mr, f32x4 c) { return (a - c * mr[0]) * mr[1]; }
DI f32x4 swiglu4(f32x4 g, f32x4 u) {
  const f32x4 t = g * -1.44269504089f;
  f32x4 d;
#pragma unroll
  for (int r = 0; r < 4; ++r) d[r] = __builtin_amdgcn_rcpf(1.0f + __builtin_amdgcn_exp2f(t[r]));
  return g * u * d;
}

struct Params {
  const char* x; const char* wqkv; const char* wo; const char* w1; const char* w3; const char* w2;
  char* oh; char* out; unsigned long long* stamps;
};

struct Frags { bf16x8 a[4], as, ah, b[4], bx, be, e0; };

template <bool TA>
DI void phase1(const Params& p, LAS char* lds, int rg, int cg, int rot) {
  const int tid = threadIdx.x, lane = tid & 63;
  const int w = rfl(tid >> 6), mi = w >> 2, nj = w & 3;
  const int r16 = lane & 15, g = lane >> 4, q = r16 >> 2, pp = lane & 3;
  constexpr int PH = 0; STAMP(0);
  const unsigned ldsb = (unsigned)(uintptr_t)lds;
  const int row0 = rg * 128;
  const int tm = TA ? 2 * cg : 16 + 18 * (cg >> 1) + ((cg & 1) ? 10 : 0);
  const int te = 16 + 18 * (cg >> 1) + 8 + (cg & 1);
  unsigned vx[2], vw[4], ve;
#pragma unroll
  for (int m = 0; m < 2; ++m) {
    const int row = 16 * w + 8 * m + (lane >> 3);
    vx[m] = (unsigned)((row0 + row) * 1024 + (((lane & 7) ^ ((row >> 1) & 7)) << 4));
  }
  const char* bw[4]; unsigned ldw[4], dw[4];
#pragma unroll
  for (int m = 0; m < 4; ++m) {
    if (!TA || m < 2) {
      const int pi = TA ? (2 * w + m) : (2 * w + (m & 1));
      const int kr = 4 * pi + (lane >> 4), pc = lane & 15;
      const int c = (pc >> 1) ^ ((kr & 3) | (((kr >> 3) & 1) << 2));
      if (TA) { bw[m] = p.wqkv; ldw[m] = 3072; vw[m] = kr * 3072 + ((c >> 2) * 512 + (4 * cg + (c & 3)) * 16) * 2 + (pc & 1) * 16; dw[m] = WOFF + pi * 1024; }
      else { bw[m] = (m >> 1) ? p.w3 : p.w1; ldw[m] = 2752; int t = tm + c; t = t > 85 ? 85 : t; vw[m] = kr * 2752 + t * 32 + (pc & 1) * 16; dw[m] = WOFF + (m >> 1) * 16384 + pi * 1024; }
    } else if (m == 2) {
      const int kr = 8 * w + (lane >> 3), pc = lane & 7;
      const int c = (pc >> 1) ^ (((kr >> 1) & 1) | (((kr >> 3) & 1) << 1));
      bw[m] = p.wqkv; ldw[m] = 3072; vw[m] = kr * 3072 + (1024 + (4 * cg + c) * 16) * 2 + (pc & 1) * 16; dw[m] = WOFF + 16384 + w * 1024;
    } else {
      const int kr = 16 * (w & 3) + (lane >> 2), pc = lane & 3;
      const int e = (pc >> 1) ^ ((kr >> 3) & 1);
      bw[m] = mi ? p.w3 : p.w1; ldw[m] = 2752; vw[m] = kr * 2752 + (tm + e) * 32 + (pc & 1) * 16; dw[m] = WOFF + 24576 + mi * 4096 + (w & 3) * 1024;
    }
  }
  const bool has_e = TA ? (w < 2) : (w < 4);
  const char* be_; unsigned lde, de;
  if (TA) {
    const int row = 8 * (w & 1) + (lane >> 3);
    int gr = row0 - 16 + row; gr = gr < 0 ? 0 : gr;
    be_ = p.x; lde = 2; ve = gr * 1024 + (((lane & 7) ^ ((row >> 1) & 7)) << 4); de = EOFF + w * 1024;
  } else {
    const int pos = 32 * (w & 1) + (lane >> 1), kr = pos ^ (((pos >> 3) & 1) << 2);
    be_ = (w & 2) ? p.w3 : p.w1; lde = 2752; ve = kr * 2752 + te * 32 + (lane & 1) * 16; de = EOFF + (w & 3) * 1024;
  }
  if (!has_e) { be_ = p.x; lde = 0; ve = 0; }
  auto dma = [&](int i, int st, int so) {
    const bool live = st <= 7;
    const unsigned stc = ((live ? st : 7) + rot) & 7;
    const unsigned sink = ldsb + DUMMY;
    if (i == 0) glds16s(be_ + (size_t)(stc * 64u * lde), ve, (live && has_e) ? ldsb + so + de : sink);
    else if (i <= 2) glds16s(p.x + stc * 128u, vx[i - 1], live ? ldsb + so + (2 * w + i - 1) * 1024 : sink);
    else glds16s(bw[i - 3] + (size_t)(stc * 64u * ldw[i - 3]), vw[i - 3], live ? ldsb + so + dw[i - 3] : sink);
  };
#pragma unroll
  for (int st = 0; st < 3; ++st)
#pragma unroll
    for (int i = 0; i < 7; ++i) dma(i, st, st * SLOT);
  STAMP(1);
  const int xa0 = r16 * 128 + ((g ^ (r16 >> 1)) << 4);
  const int fl = q | ((g & 1) << 2);
  const int w256 = (8 * g + q) * 256 + (fl << 5) + pp * 8;
  const int w128 = (8 * g + q) * 128 + ((((q >> 1) & 1) | ((g & 1) << 1)) << 5) + pp * 8;
  const int w64 = (8 * g + q) * 64 + ((g & 1) << 5) + pp * 8;
  const int w32 = (8 * g + ((g & 1) << 2) + q) * 32 + pp * 8;
  const int dt32 = (g & 1) ? -128 : 128;
  const int tb0 = (w256 ^ (nj << 6)) + (mi ? 16384 : 0), tb2 = (w256 ^ (nj << 6)) + (mi ? 0 : 16384);
  const int tbx = EOFF + (nj & 1) * 2048 + w32;
  const int taq = w256 ^ (nj << 5), tak = w256 ^ ((4 + nj) << 5), tav = 16384 + (w128 ^ (nj << 5)), taf = 24576 + (nj & 1) * 4096 + (w64 ^ ((nj >> 1) << 5));
  const int fu = 4 + ((nj >> 1) | ((nj & 1) << 1));
  int xr[4];
#pragma unroll
  for (int i = 0; i < 4; ++i) xr[i] = mi * 8192 + (TA ? i : ((i + nj) & 3)) * 2048;
  const int bea = mi ? tav : tak, bes = mi ? 4096 : 8192, bet = mi ? 512 : 1024;
  const int e0a = mi ? taf : taq, e0s = mi ? 2048 : 8192, e0t = mi ? 256 : 1024;
  constexpr int NRD = TA ? 12 : 9;
  auto ldsB2 = [&](LAS char* p0, LAS char* p1) {
    s16x4 lo = __builtin_amdgcn_ds_read_tr16_b64_v4i16((LAS s16x4*)p0), hi = __builtin_amdgcn_ds_read_tr16_b64_v4i16((LAS s16x4*)p1);
    return __builtin_bit_cast(bf16x8, __builtin_shufflevector(lo, hi, 0, 1, 2, 3, 4, 5, 6, 7));
  };
  auto rdf = [&](Frags& F, int i, int so, int ks) {
    LAS char* sb = lds + so;
    const int xa = xa0 ^ (ks * 64);
    LAS char* wb = sb + WOFF;
    if (TA) {
      switch (i) {
        case 0: F.a[0] = ldsA(sb + xa + xr[0]); break;
        case 1: F.b[0] = ldsB(wb + taq + ks * 8192, 1024); break;
        case 2: F.b[1] = ldsB(wb + tak + ks * 8192, 1024); break;
        case 3: F.b[2] = ldsB(wb + tav + ks * 4096, 512); break;
        case 4: F.b[3] = ldsB(wb + taf + ks * 2048, 256); break;
        case 5: F.a[1] = ldsA(sb + xa + xr[1]); break;
        case 6: F.a[2] = ldsA(sb + xa + xr[2]); break;
        case 7: F.a[3] = ldsA(sb + xa + xr[3]); break;
        case 8: F.as = ldsA(sb + xa + w * 2048); break;
        case 9: F.be = ldsB2(wb + bea + ks * bes, wb + bea + ks * bes + bet); break;
        case 10: F.ah = ldsA(sb + EOFF + xa); break;
        default: F.e0 = ldsB2(wb + e0a + ks * e0s, wb + e0a + ks * e0s + e0t); break;
      }
    } else {
      switch (i) {
        case 0: F.a[0] = ldsA(sb + xa + xr[0]); break;
        case 1: F.b[0] = ldsB(wb + tb0 + ks * 8192, 1024); break;
        case 2: F.b[1] = ldsB(wb + (tb0 ^ 32) + ks * 8192, 1024); break;
        case 3: F.b[2] = ldsB(wb + tb2 + ks * 8192, 1024); break;
        case 4: F.b[3] = ldsB(wb + (tb2 ^ 32) + ks * 8192, 1024); break;
        case 5: F.a[1] = ldsA(sb + xa + xr[1]); break;
        case 6: F.a[2] = ldsA(sb + xa + xr[2]); break;
        case 7: F.a[3] = ldsA(sb + xa + xr[3]); break;
        default: F.bx = ldsB2(sb + tbx + ks * 1024, sb + tbx + ks * 1024 + dt32); break;
      }
    }
  };
  f32x4 acc[4][4], accx[2], acch = {0, 0, 0, 0}, cs0 = {0, 0, 0, 0}, cs1 = {0, 0, 0, 0}, csx = {0, 0, 0, 0};
  accx[0] = (f32x4){0, 0, 0, 0}; accx[1] = (f32x4){0, 0, 0, 0};
#pragma unroll
  for (int c = 0; c < 4; ++c)
#pragma unroll
    for (int rt = 0; rt < 4; ++rt) acc[c][rt] = (f32x4){0, 0, 0, 0};
  float ss0 = 0.f, ss1 = 0.f, sq0 = 0.f, sq1 = 0.f, hs_s = 0.f, hs_q = 0.f;
  const bf16x8 ones = {(__bf16)1.0f, (__bf16)1.0f, (__bf16)1.0f, (__bf16)1.0f, (__bf16)1.0f, (__bf16)1.0f, (__bf16)1.0f, (__bf16)1.0f};
  auto mm = [&](const Frags& F, int gi) {
    if (gi < 4) {
      acc[0][gi] = mfma32(F.b[0], F.a[gi], acc[0][gi]);
      acc[1][gi] = mfma32(F.b[1], F.a[gi], acc[1][gi]);
      acc[2][gi] = TA ? mfma32(F.a[gi], F.b[2], acc[2][gi]) : mfma32(F.b[2], F.a[gi], acc[2][gi]);
      acc[3][gi] = mfma32(F.b[3], F.a[gi], acc[3][gi]);
    } else if (TA) {
      acch = mfma32(F.be, F.ah, acch);
      cs0 = mfma32(F.e0, ones, cs0); cs1 = mfma32(F.be, ones, cs1);
    } else {
      accx[0] = mfma32(F.bx, F.a[0], accx[0]); accx[1] = mfma32(F.bx, F.a[1], accx[1]);
      cs0 = mfma32(F.b[0], ones, cs0); cs1 = mfma32(F.b[1], ones, cs1); csx = mfma32(F.bx, ones, csx);
    }
  };
  auto st2 = [&](bf16x8 v, int i, float& s0, float& s1, float& q0, float& q1) {
    const unsigned u = __builtin_bit_cast(u32x4, v)[i];
    const float lo = __uint_as_float(u << 16), hi = __uint_as_float(u & 0xffff0000u);
    s0 += lo; q0 = fmaf(lo, lo, q0); s1 += hi; q1 = fmaf(hi, hi, q1);
  };
  constexpr int RPG = (NRD + 4) / 5;
  auto half = [&](const Frags& Fc, Frags& Fn, int so_n, int ks_n, int d0, int nd, int st_d, int so_d) {
#pragma unroll
    for (int gi = 0; gi < 5; ++gi) {
#pragma unroll
      for (int i = 0; i < RPG; ++i) { const int idx = gi * RPG + i; if (idx < NRD) rdf(Fn, idx, so_n, ks_n); }
      mm(Fc, gi);
      if (gi < 4) st2(TA ? Fc.as : Fc.a[0], gi, ss0, ss1, sq0, sq1);
#pragma unroll
      for (int k = 0; k < 5; ++k) {
        __builtin_amdgcn_sched_group_barrier(0x8, 1, 0);
        __builtin_amdgcn_sched_group_barrier(0x100, 1, 0);
        __builtin_amdgcn_sched_group_barrier(0x2, 2, 0);
      }
      __builtin_amdgcn_sched_group_barrier(0x100, 8, 0);
      asm volatile("" : "+v"(ss0), "+v"(ss1), "+v"(sq0), "+v"(sq1));
      SB0();
      if (gi < nd) { dma(d0 + gi, st_d, so_d); SB0(); }
    }
  };

  Frags F0, F1;
  WAITV(14); BAR();
  STAMP(2);
#pragma unroll
  for (int i = 0; i < NRD; ++i) rdf(F0, i, 0, 0);
  int so = 0, sop = 2 * SLOT;
#pragma unroll 1
  for (int s = 0; s < 8; ++s) {
    half(F0, F1, so, 1, 4, 3, s == 0 ? 8 : s + 2, sop);
    if (TA && s == w) {
#pragma unroll
      for (int i = 0; i < 4; ++i) st2(F0.ah, i, hs_s, hs_s, hs_q, hs_q);
    }
    const int son = (so == 2 * SLOT) ? 0 : so + SLOT;
    WAITV(7);
    WAITL0();
    BAR();
    half(F1, F0, son, 0, 0, 4, s + 3, so);
    if (TA && s == w) {
#pragma unroll
      for (int i = 0; i < 4; ++i) st2(F1.ah, i, hs_s, hs_s, hs_q, hs_q);
    }
    sop = so; so = son;
  }
  float st_s = ss0 + ss1, st_q = sq0 + sq1;
  STAMP(4);
  {
    LAS f32x4* CSw = (LAS f32x4*)(lds + SC_CS);
    if (r16 == 0) {
      const int u0 = TA ? (mi ? 8 + fu : nj) : (mi * 8 + 2 * nj), u1 = TA ? (mi ? 8 + nj : 4 + nj) : (mi * 8 + 2 * nj + 1);
      CSw[u0 * 4 + g] = cs0; CSw[u1 * 4 + g] = cs1;
    }
    st_s = xg_sum(st_s); st_q = xg_sum(st_q);
    const float m = st_s * (1.0f / 512.0f), var = fmaxf(st_q * (1.0f / 512.0f) - m * m, 0.f);
    if (lane < 16) ((LAS f32x2*)(lds + SC_MR))[16 * w + lane] = (f32x2){m, __builtin_amdgcn_rsqf(var + 1e-5f)};
  }
  WAITV(0);
  if (TA) { hs_s = xg_sum(hs_s); hs_q = xg_sum(hs_q); }
  WAITL0(); BAR();
  STAMP(5);
  LAS f32x4* CS = (LAS f32x4*)(lds + SC_CS);
  LAS f32x2* MR = (LAS f32x2*)(lds + SC_MR);
  char* ohl = p.oh + (size_t)lane * 16;
  if (!TA) {
    const f32x4 c0 = CS[(2 * nj) * 4 + g], c1 = CS[(2 * nj + 1) * 4 + g], c2 = CS[(8 + 2 * nj) * 4 + g], c3 = CS[(9 + 2 * nj) * 4 + g];
    const int tp = tm + 2 * nj;
    LAS char* fx = lds + SC_FX;
    { const f32x2 m0 = MR[64 * mi + 16 * nj + r16], m1 = MR[64 * mi + 16 * ((nj + 1) & 3) + r16];
      *(LAS s16x4*)(fx + ((mi * 2 + (nj & 1)) * 4 + nj) * 512 + lane * 8) = pack4(lnc(accx[0], m0, csx));
      *(LAS s16x4*)(fx + ((mi * 2 + (nj & 1)) * 4 + ((nj + 1) & 3)) * 512 + lane * 8) = pack4(lnc(accx[1], m1, csx)); }
#pragma unroll
    for (int i = 0; i < 4; ++i) {
      const int rt = (i + nj) & 3;
      const f32x2 mr = MR[64 * mi + 16 * rt + r16];
      const f32x4 g0 = lnc(mi ? acc[2][i] : acc[0][i], mr, c0), u0 = lnc(mi ? acc[0][i] : acc[2][i], mr, c2);
      const f32x4 g1 = lnc(mi ? acc[3][i] : acc[1][i], mr, c1), u1 = lnc(mi ? acc[1][i] : acc[3][i], mr, c3);
      const u32x2 h0 = __builtin_bit_cast(u32x2, pack4(swiglu4(g0, u0))), h1 = __builtin_bit_cast(u32x2, pack4(swiglu4(g1, u1)));
      if (tp <= 85) *(u32x4*)(ohl + ((size_t)(rg * 8 + 4 * mi + rt) * NKS + 16 + (tp >> 1)) * 1024) = (u32x4){h0[0], h0[1], h1[0], h1[1]};
    }
    WAITL0(); BAR();
    const f32x4 gx = unpack4(*(LAS s16x4*)(fx + ((mi * 2 + 0) * 4 + nj) * 512 + lane * 8)), ux = unpack4(*(LAS s16x4*)(fx + ((mi * 2 + 1) * 4 + nj) * 512 + lane * 8));
    *(s16x4*)(ohl + ((size_t)(rg * 8 + w) * NKS + 16 + (te >> 1)) * 1024 + (te & 1) * 8) = pack4(swiglu4(gx, ux));
  } else {
    if (lane < 16) ((LAS f32x2*)(lds + SC_HS))[lane * 8 + w] = (f32x2){hs_s, hs_q};
    s16x4 Q[4], K[4], V[4], Fv[4];
    const f32x4 cq = CS[nj * 4 + g], ck = CS[(4 + nj) * 4 + g], cf = CS[(8 + fu) * 4 + g];
    const float cv = ((LAS float*)CS)[(8 + nj) * 16 + r16];
#pragma unroll
    for (int rt = 0; rt < 4; ++rt) {
      const f32x2 mr = MR[64 * mi + 16 * rt + r16];
      Q[rt] = pack4(lnc(acc[0][rt], mr, cq)); K[rt] = pack4(lnc(acc[1][rt], mr, ck)); Fv[rt] = pack4(lnc(acc[3][rt], mr, cf));
      const f32x4 t01 = *(LAS f32x4*)(MR + 64 * mi + 16 * rt + 4 * g), t23 = *(LAS f32x4*)(MR + 64 * mi + 16 * rt + 4 * g + 2);
      const f32x4 mm4 = {t01[0], t01[2], t23[0], t23[2]}, rr4 = {t01[1], t01[3], t23[1], t23[3]};
      V[rt] = pack4((acc[2][rt] - mm4 * cv) * rr4);
    }
    LAS char* kvb = lds + SC_KV + nj * 1536;
    LAS char* fx = lds + SC_FX + w * 1024;
    if (mi == 0) { *(LAS s16x4*)(kvb + 512 + lane * 8) = K[3]; *(LAS s16x4*)(kvb + 1024 + lane * 8) = V[3]; }
    *(LAS s16x4*)(fx + lane * 8) = (nj & 1) ? Fv[0] : Fv[2]; *(LAS s16x4*)(fx + 512 + lane * 8) = (nj & 1) ? Fv[1] : Fv[3];
    float mka[8];
#pragma unroll
    for (int r = 0; r < 4; ++r) {
      const int dp = r16 + 16 - (4 * g + r), dc = r16 - (4 * g + r);
      mka[r] = (!(dp & 1) && dp <= 14) ? 0.f : -__builtin_inff();
      mka[4 + r] = (!(dc & 1) && dc >= 0 && dc <= 14) ? 0.f : -__builtin_inff();
    }
    const int RT0 = rg * 8 + 4 * mi;
    char* od = ohl + ((size_t)RT0 * NKS + 2 * cg + (nj >> 1)) * 1024 + (nj & 1) * 8;
    auto attn = [&](s16x4 kp, s16x4 vp, s16x4 kc, s16x4 vc, s16x4 qq, bool noprev, int rt) {
      const f32x4 z = {0, 0, 0, 0};
      const f32x4 sp = mfma16(kp, qq, z), sc = mfma16(kc, qq, z);
      const float sc2 = 0.25f * 1.44269504089f;
      float l[8];
#pragma unroll
      for (int r = 0; r < 4; ++r) { l[r] = fmaf(sp[r], sc2, noprev ? -__builtin_inff() : mka[r]); l[4 + r] = fmaf(sc[r], sc2, mka[4 + r]); }
      float mx = fmaxf(fmaxf(fmaxf(l[0], l[1]), fmaxf(l[2], l[3])), fmaxf(fmaxf(l[4], l[5]), fmaxf(l[6], l[7])));
      mx = xg_max(mx);
      float sum = 0.f;
#pragma unroll
      for (int r = 0; r < 8; ++r) { l[r] = __builtin_amdgcn_exp2f(l[r] - mx); sum += l[r]; }
      sum = xg_sum(sum);
      const float inv = __builtin_amdgcn_rcpf(sum);
      const s16x4 p0 = pack4((f32x4){l[0], l[1], l[2], l[3]} * inv), p1 = pack4((f32x4){l[4], l[5], l[6], l[7]} * inv);
      const bf16x8 pf = __builtin_bit_cast(bf16x8, __builtin_shufflevector(p0, p1, 0, 1, 2, 3, 4, 5, 6, 7));
      const bf16x8 vf = __builtin_bit_cast(bf16x8, __builtin_shufflevector(vp, vc, 0, 1, 2, 3, 4, 5, 6, 7));
      *(s16x4*)(od + (size_t)rt * NKS * 1024) = pack4(mfma32(vf, pf, z));
    };
#pragma unroll
    for (int rt = 1; rt < 4; ++rt) attn(K[rt - 1], V[rt - 1], K[rt], V[rt], Q[rt], false, rt);
    WAITL0(); BAR();
    float hsum = 0.f, hsq = 0.f;
    { LAS f32x4* HS = (LAS f32x4*)(lds + SC_HS) + r16 * 4;
#pragma unroll
      for (int i = 0; i < 4; ++i) { const f32x4 t = HS[i]; hsum += t[0] + t[2]; hsq += t[1] + t[3]; } }
    const float hm = hsum * (1.0f / 512.0f), hr = __builtin_amdgcn_rsqf(fmaxf(hsq * (1.0f / 512.0f) - hm * hm, 0.f) + 1e-5f);
    const s16x4 hv = pack4(lnc(acch, (f32x2){hm, hr}, CS[((mi ? 8 : 4) + nj) * 4 + g]));
    if (mi) *(LAS s16x4*)(kvb + r16 * 32 + g * 8) = hv;
    {
      LAS char* fo = lds + SC_FX + (w ^ 1) * 1024;
      const s16x4 o0 = *(LAS s16x4*)(fo + lane * 8), o1 = *(LAS s16x4*)(fo + 512 + lane * 8);
      const int rb = (nj & 1) * 2;
      const f32x4 a0 = unpack4((nj & 1) ? Fv[2] : Fv[0]), a1 = unpack4((nj & 1) ? Fv[3] : Fv[1]), b0 = unpack4(o0), b1 = unpack4(o1);
      char* hd = ohl + ((size_t)(RT0 + rb) * NKS + 16 + cg) * 1024 + (nj >> 1) * 8;
      *(s16x4*)hd = pack4((nj & 1) ? swiglu4(b0, a0) : swiglu4(a0, b0));
      *(s16x4*)(hd + NKS * 1024) = pack4((nj & 1) ? swiglu4(b1, a1) : swiglu4(a1, b1));
    }
    WAITL0(); BAR();
    s16x4 Kp, Vp;
    if (mi == 0) { Kp = hv; Vp = __builtin_amdgcn_ds_read_tr16_b64_v4i16((LAS s16x4*)(kvb + (4 * g + q) * 32 + pp * 8)); }
    else { Kp = *(LAS s16x4*)(kvb + 512 + lane * 8); Vp = *(LAS s16x4*)(kvb + 1024 + lane * 8); }
    attn(Kp, Vp, K[0], V[0], Q[0], RT0 == 0, 0);
  }
  STAMP(6);
  STAMP(7);
}

__global__ __launch_bounds__(512, 2) void k_phase1(Params p) {
  extern __shared__ __attribute__((aligned(16))) char shm[];
  LAS char* lds = (LAS char*)shm;
  const int b = blockIdx.x, xcd = b & 7, idx = b >> 3;
  const int rg = (xcd & 1) * 8 + (idx & 7), cg = (xcd >> 1) * 4 + (idx >> 3);
#define ROTF 0
  const int rot = ROTF;
  if (cg < 8) phase1<true>(p, lds, rg, cg, rot); else phase1<false>(p, lds, rg, cg - 8, rot);
}

constexpr int P2D = 5;
constexpr int P2NA = P2D + 1;
constexpr int P2NS = P2D + 2;
DI void gload16(bf16x8& dst, const void* p) { asm volatile("global_load_dwordx4 %0, %1, off" : "=v"(dst) : "v"(p) : "memory"); }
template <int N> DI void waitA(bf16x8& a, bf16x8& b) { asm volatile("s_waitcnt vmcnt(%2)" : "+v"(a), "+v"(b) : "n"(N) : "memory"); }
template <int N> DI void waitVn() { asm volatile("s_waitcnt vmcnt(%0)" :: "n"(N) : "memory"); }
__global__ __launch_bounds__(512, 2) void k_phase2(Params p) {
  extern __shared__ __attribute__((aligned(16))) char shm[];
  LAS char* lds = (LAS char*)shm;
  const int tid = threadIdx.x, lane = tid & 63;
  const int w = rfl(tid >> 6), kh = w >> 2, m4 = w & 3;
  const int r16 = lane & 15, g = lane >> 4, q = r16 >> 2, pp = lane & 3;
  const unsigned ldsb = (unsigned)(uintptr_t)lds;
  const int b = blockIdx.x, xcd = b & 7, idx = b >> 3;
  const int rt64 = xcd * 4 + (idx & 3), ct64 = idx >> 2;
  constexpr int PH = 1; STAMP(0);
  constexpr int NR = (NKS + 3) / 4;
  const char* pa = p.oh + ((size_t)(rt64 * 4 + m4) * NKS + kh) * 1024 + lane * 16;
  int boff;
  { const int row = 8 * m4 + (lane >> 3), pc = lane & 7; const int c = (pc >> 1) ^ ((row >> 1) & 3);
    boff = row * 1024 + ct64 * 128 + (2 * c + (pc & 1)) * 16; }
  const char* pbo = p.wo + (size_t)kh * 32768 + boff;
  const char* pb2 = p.w2 + (long)(kh - 16) * 32768 + boff;
  bf16x8 A[P2NA][2];
  auto issue = [&](int r) {
#pragma unroll
    for (int j = 0; j < 2; ++j) {
      int ke = 4 * r + 2 * j;
      if (ke + 1 > NKS - 1) ke -= 2 * (ke + 1 > NKS - 1 ? 1 : 0) * 0;
      const bool over = (ke + 1 > NKS - 1);
      const long ko = over ? (long)ke - 2 * kh : (long)ke;
      gload16(A[r % P2NA][j], pa + ko * 1024);
      glds16((ke < 16 ? pbo : pb2) + ko * 32768, ldsb + (r % P2NS) * 16384 + (2 * j) * 4096 + kh * 4096 + m4 * 1024);
    }
  };
#pragma unroll
  for (int r = 0; r < P2D; ++r) issue(r);
  STAMP(1);
  int bl[4];
#pragma unroll
  for (int c = 0; c < 4; ++c) bl[c] = (4 * g + q) * 128 + ((c ^ ((2 * g + (q >> 1)) & 3)) << 5) + pp * 8 + kh * 4096;
  f32x4 acc[4] = {{0, 0, 0, 0}, {0, 0, 0, 0}, {0, 0, 0, 0}, {0, 0, 0, 0}};
  const size_t ooff = (size_t)(rt64 * 64 + 16 * m4 + r16) * 1024 + (ct64 * 64 + 32 * kh + 4 * g) * 2;
  bf16x4 xv0, xv1;
  bf16x8 bf[2][2][4];
  auto readB = [&](int r) {
    LAS char* sb = lds + (r % P2NS) * 16384;
#pragma unroll
    for (int j = 0; j < 2; ++j)
#pragma unroll
      for (int c = 0; c < 4; ++c) bf[r & 1][j][c] = ldsB(sb + j * 8192 + bl[c], 2048);
  };
  waitVn<4 * (P2D - 1)>(); BAR();
  STAMP(2);
  readB(0);
#pragma unroll
  for (int r = 0; r < NR; ++r) {
    if (r + 1 < NR) {
      const int after = (NR - 1 - (r + 1)) < (P2D - 2) ? (NR - 1 - (r + 1)) : (P2D - 2);
      if (after == 3) waitVn<12>(); else if (after == 2) waitVn<8>(); else if (after == 1) waitVn<4>(); else waitVn<0>();
      BAR();
      if (r == 8) STAMP(3);
      if (r + P2D < NR) issue(r + P2D);
      readB(r + 1);
    } else { xv0 = *(const bf16x4*)(p.x + ooff); xv1 = *(const bf16x4*)(p.x + ooff + 32); }
    asm volatile("" : "+v"(A[r % P2NA][0]), "+v"(A[r % P2NA][1]));
#pragma unroll
    for (int j = 0; j < 2; ++j) {
      if (4 * r + 2 * j + 1 > NKS - 1 && kh) continue;
#pragma unroll
      for (int c = 0; c < 4; ++c) acc[c] = mfma32(bf[r & 1][j][c], A[r % P2NA][j], acc[c]);
    }
  }
  STAMP(4);
  {
    LAS f32x4* red = (LAS f32x4*)(lds + 7 * 16384);
    red[(w * 2 + 0) * 64 + lane] = kh ? acc[0] : acc[2];
    red[(w * 2 + 1) * 64 + lane] = kh ? acc[1] : acc[3];
    WAITL0(); BAR();
    const f32x4 o0 = red[((w ^ 4) * 2 + 0) * 64 + lane], o1 = red[((w ^ 4) * 2 + 1) * 64 + lane];
    const f32x4 s0 = (kh ? acc[2] : acc[0]) + o0, s1 = (kh ? acc[3] : acc[1]) + o1;
    const f32x4 x0 = __builtin_convertvector(xv0, f32x4), x1 = __builtin_convertvector(xv1, f32x4);
    *(s16x4*)(p.out + ooff) = pack4(s0 + x0);
    *(s16x4*)(p.out + ooff + 32) = pack4(s1 + x1);
  }
  STAMP(5);
  STAMP(6); STAMP(7);
}

extern "C" void kernel_launch(void* const* d_in, const int* in_sizes, int n_in, void* d_out, int out_size, void* d_ws, size_t ws_size,
                              hipStream_t stream) {
  Params p;
  p.x = (const char*)d_in[0]; p.wqkv = (const char*)d_in[1]; p.wo = (const char*)d_in[2];
  p.w1 = (const char*)d_in[4]; p.w3 = (const char*)d_in[5]; p.w2 = (const char*)d_in[6];
  p.oh = (char*)d_ws; p.out = (char*)d_out; p.stamps = (unsigned long long*)((char*)d_ws + (128u << 20));
  (void)hipFuncSetAttribute((const void*)k_phase1, hipFuncAttributeMaxDynamicSharedMemorySize, P1_LDS);
  (void)hipFuncSetAttribute((const void*)k_phase2, hipFuncAttributeMaxDynamicSharedMemorySize, P2_LDS);
  k_phase1<<<256, 512, P1_LDS, stream>>>(p);
  k_phase2<<<256, 512, P2_LDS, stream>>>(p);
}
```

```cpp
#include <hip/hip_runtime.h>
#include <stdint.h>

#define LAS __attribute__((address_space(3)))
#define DI __device__ __forceinline__
typedef __bf16 bf16x8 __attribute__((ext_vector_type(8)));
typedef __bf16 bf16x4 __attribute__((ext_vector_type(4)));
typedef short s16x4 __attribute__((ext_vector_type(4)));
typedef float f32x4 __attribute__((ext_vector_type(4)));
typedef float f32x2 __attribute__((ext_vector_type(2)));
typedef unsigned u32x2 __attribute__((ext_vector_type(2)));
typedef unsigned u32x4 __attribute__((ext_vector_type(4)));

constexpr int NKS = 59;
constexpr int SLOT = 53248;
constexpr int WOFF = 16384, EOFF = 49152;
constexpr int DUMMY = 3 * SLOT;
constexpr int SC_CS = DUMMY + 1024;
constexpr int SC_MR = SC_CS + 1152;
constexpr int P1_LDS = 163840;
constexpr int SC_HS = 0;
constexpr int SC_KV = 1024;
constexpr int SC_FX = 8192;
constexpr int P2_LDS = 7 * 16384 + 16384;

#define STAMP(i) do {} while (0)

DI int rfl(int v) { return __builtin_amdgcn_readfirstlane(v); }
DI void glds16(const void* g, unsigned lds_dst) {
  unsigned keep;
  asm volatile("s_mov_b32 %0, m0\n\ts_mov_b32 m0, %2\n\ts_nop 0\n\tglobal_load_lds_dwordx4 %1, off\n\ts_mov_b32 m0, %0"
               : "=&s"(keep) : "v"(g), "s"(lds_dst) : "memory");
}
#define WAITV(n) asm volatile("s_waitcnt vmcnt(" #n ")" ::: "memory")
#define WAITL0() do { asm volatile("" ::: "memory"); __builtin_amdgcn_s_waitcnt(0xC07F); asm volatile("" ::: "memory"); } while (0)
#define BAR() do { asm volatile("" ::: "memory"); __builtin_amdgcn_s_barrier(); asm volatile("" ::: "memory"); } while (0)
#define SB0() __builtin_amdgcn_sched_barrier(0)

DI bf16x8 ldsA(LAS char* p) { return *(LAS bf16x8*)p; }
DI bf16x8 ldsB(LAS char* p, int toff) {
  s16x4 lo = __builtin_amdgcn_ds_read_tr16_b64_v4i16((LAS s16x4*)p);
  s16x4 hi = __builtin_amdgcn_ds_read_tr16_b64_v4i16((LAS s16x4*)(p + toff));
  return __builtin_bit_cast(bf16x8, __builtin_shufflevector(lo, hi, 0, 1, 2, 3, 4, 5, 6, 7));
}
DI f32x4 mfma32(bf16x8 a, bf16x8 b, f32x4 c) { return __builtin_amdgcn_mfma_f32_16x16x32_bf16(a, b, c, 0, 0, 0); }
DI f32x4 mfma16(s16x4 a, s16x4 b, f32x4 c) { return __builtin_amdgcn_mfma_f32_16x16x16bf16_1k(a, b, c, 0, 0, 0); }
DI s16x4 pack4(f32x4 v) { return __builtin_bit_cast(s16x4, __builtin_convertvector(v, bf16x4)); }
DI f32x4 unpack4(s16x4 v) { return __builtin_convertvector(__builtin_bit_cast(bf16x4, v), f32x4); }
DI float xg_sum(float v) {
  auto r = __builtin_amdgcn_permlane16_swap(__float_as_uint(v), __float_as_uint(v), false, false);
  v = __uint_as_float(r[0]) + __uint_as_float(r[1]);
  auto s = __builtin_amdgcn_permlane32_swap(__float_as_uint(v), __float_as_uint(v), false, false);
  return __uint_as_float(s[0]) + __uint_as_float(s[1]);
}
DI float xg_max(float v) {
  auto r = __builtin_amdgcn_permlane16_swap(__float_as_uint(v), __float_as_uint(v), false, false);
  v = fmaxf(__uint_as_float(r[0]), __uint_as_float(r[1]));
  auto s = __builtin_amdgcn_permlane32_swap(__float_as_uint(v), __float_as_uint(v), false, false);
  return fmaxf(__uint_as_float(s[0]), __uint_as_float(s[1]));
}
DI f32x4 lnc(f32x4 a, f32x2 mr, f32x4 c) { return (a - c * mr[0]) * mr[1]; }
DI f32x4 swiglu4(f32x4 g, f32x4 u) {
  const f32x4 t = g * -1.44269504089f;
  f32x4 d;
#pragma unroll
  for (int r = 0; r < 4; ++r) d[r] = __builtin_amdgcn_rcpf(1.0f + __builtin_amdgcn_exp2f(t[r]));
  return g * u * d;
}

struct Params {
  const char* x; const char* wqkv; const char* wo; const char* w1; const char* w3; const char* w2;
  char* oh; char* out; unsigned long long* stamps;
};

struct Frags { bf16x8 a[4], as, ah, b[4], bx, be, e0; };

template <bool TA>
DI void phase1(const Params& p, LAS char* lds, int rg, int cg, int rot) {
  const int tid = threadIdx.x, lane = tid & 63;
  const int w = rfl(tid >> 6), mi = w >> 2, nj = w & 3;
  const int r16 = lane & 15, g = lane >> 4, q = r16 >> 2, pp = lane & 3;
  constexpr int PH = 0; STAMP(0);
  const unsigned ldsb = (unsigned)(uintptr_t)lds;
  const int row0 = rg * 128;
  const int tm = TA ? 2 * cg : 16 + 18 * (cg >> 1) + ((cg & 1) ? 10 : 0);
  const int te = 16 + 18 * (cg >> 1) + 8 + (cg & 1);
  const char* px[2];
#pragma unroll
  for (int m = 0; m < 2; ++m) {
    const int row = 16 * w + 8 * m + (lane >> 3);
    const int ch = (lane & 7) ^ ((row >> 1) & 7);
    px[m] = p.x + (size_t)(row0 + row) * 1024 + ch * 16;
  }
  const char* pw[4]; unsigned adv[4];
#pragma unroll
  for (int m = 0; m < 4; ++m) {
    const int kr = 8 * w + 2 * m + (lane >> 5), pc = lane & 31, u = pc >> 1;
    const int f = (kr & 3) | (((kr >> 3) & 1) << 2);
    const int c = (u & 8) | ((u & 7) ^ f);
    const char* base; int ld, colb;
    if (TA) {
      if (c < 12) { base = p.wqkv; ld = 3072; colb = ((c >> 2) * 512 + (4 * cg + (c & 3)) * 16) * 2; }
      else { base = (c < 14) ? p.w1 : p.w3; ld = 2752; colb = (tm + (c & 1)) * 32; }
    } else {
      base = (c < 8) ? p.w1 : p.w3; ld = 2752;
      int t = tm + (c & 7); t = t > 85 ? 85 : t; colb = t * 32;
    }
    pw[m] = base + (size_t)kr * ld + colb + (pc & 1) * 16;
    adv[m] = 64u * ld;
  }
  const bool has_e = TA ? (w < 2) : (w < 4);
  const char* pe; unsigned adve;
  if (TA) {
    const int row = 8 * (w & 1) + (lane >> 3);
    const int ch = (lane & 7) ^ ((row >> 1) & 7);
    int gr = row0 - 16 + row; gr = gr < 0 ? 0 : gr;
    pe = p.x + (size_t)gr * 1024 + ch * 16; adve = 128;
  } else {
    const int kr = 16 * (w & 3) + (lane >> 2), pc = lane & 3;
    const int e = (pc >> 1) ^ ((kr >> 3) & 1);
    pe = (e ? p.w3 : p.w1) + (size_t)kr * 2752 + te * 32 + (pc & 1) * 16; adve = 64u * 2752;
  }
  if (!has_e) { pe = p.x; adve = 0; }
  const unsigned edst = has_e ? EOFF + w * 1024 : 0xffffffffu;
  auto dma = [&](int i, int st, int so) {
    const bool live = st <= 7;
    const unsigned stc = ((live ? st : 7) + rot) & 7;
    const unsigned sink = ldsb + DUMMY;
    if (i == 0) glds16(pe + (size_t)stc * adve, (live && has_e) ? ldsb + so + edst : sink);
    else if (i <= 2) glds16(px[i - 1] + (size_t)stc * 128, live ? ldsb + so + (2 * w + i - 1) * 1024 : sink);
    else glds16(pw[i - 3] + (size_t)stc * adv[i - 3], live ? ldsb + so + WOFF + (4 * w + i - 3) * 1024 : sink);
  };
#pragma unroll
  for (int st = 0; st < 3; ++st)
#pragma unroll
    for (int i = 0; i < 7; ++i) dma(i, st, st * SLOT);
  STAMP(1);
  const int xa0 = r16 * 128 + ((g ^ (r16 >> 1)) << 4);
  const int fl = q | ((g & 1) << 2);
  const int wl = (8 * g + q) * 512 + (fl << 5) + pp * 8;
  const int el = ((8 * g + q) * 64 + ((g & 1) << 5) + pp * 8) ^ ((nj & 1) << 5);
  const int wn = TA ? (wl ^ (nj << 5)) : (wl ^ (nj << 6));
  const int wg_ = wn + (mi ? 256 : 0), wu_ = wn + (mi ? 0 : 256);
  const int fu = 4 + ((nj >> 1) | ((nj & 1) << 1));
  const int wf = (wl ^ (fu << 5)) + 256;
  const int we0 = mi ? wf : wn;
  const int weh = mi ? (wn + 256) : (wn ^ 128);
  int xr[4];
#pragma unroll
  for (int i = 0; i < 4; ++i) xr[i] = mi * 8192 + (TA ? i : ((i + nj) & 3)) * 2048;
  constexpr int NRD = TA ? 12 : 9;
  auto rdf = [&](Frags& F, int i, int so, int ks) {
    LAS char* sb = lds + so;
    const int xa = xa0 ^ (ks * 64);
    LAS char* wb = sb + WOFF + ks * 16384;
    if (TA) {
      switch (i) {
        case 0: F.a[0] = ldsA(sb + xa + xr[0]); break;
        case 1: F.b[0] = ldsB(wb + wn, 2048); break;
        case 2: F.b[1] = ldsB(wb + (wn ^ 128), 2048); break;
        case 3: F.b[2] = ldsB(wb + wn + 256, 2048); break;
        case 4: F.b[3] = ldsB(wb + wf, 2048); break;
        case 5: F.a[1] = ldsA(sb + xa + xr[1]); break;
        case 6: F.a[2] = ldsA(sb + xa + xr[2]); break;
        case 7: F.a[3] = ldsA(sb + xa + xr[3]); break;
        case 8: F.as = ldsA(sb + xa + w * 2048); break;
        case 9: F.be = ldsB(wb + weh, 2048); break;
        case 10: F.ah = ldsA(sb + EOFF + xa); break;
        default: F.e0 = ldsB(wb + we0, 2048); break;
      }
    } else {
      LAS char* eb = sb + EOFF + ks * 2048;
      switch (i) {
        case 0: F.a[0] = ldsA(sb + xa + xr[0]); break;
        case 1: F.b[0] = ldsB(wb + wg_, 2048); break;
        case 2: F.b[1] = ldsB(wb + (wg_ ^ 32), 2048); break;
        case 3: F.b[2] = ldsB(wb + wu_, 2048); break;
        case 4: F.b[3] = ldsB(wb + (wu_ ^ 32), 2048); break;
        case 5: F.a[1] = ldsA(sb + xa + xr[1]); break;
        case 6: F.a[2] = ldsA(sb + xa + xr[2]); break;
        case 7: F.a[3] = ldsA(sb + xa + xr[3]); break;
        default: F.bx = ldsB(eb + el, 256); break;
      }
    }
  };
  f32x4 acc[4][4], accx[2], acch = {0, 0, 0, 0}, cs0 = {0, 0, 0, 0}, cs1 = {0, 0, 0, 0}, csx = {0, 0, 0, 0};
  accx[0] = (f32x4){0, 0, 0, 0}; accx[1] = (f32x4){0, 0, 0, 0};
#pragma unroll
  for (int c = 0; c < 4; ++c)
#pragma unroll
    for (int rt = 0; rt < 4; ++rt) acc[c][rt] = (f32x4){0, 0, 0, 0};
  float ss0 = 0.f, ss1 = 0.f, sq0 = 0.f, sq1 = 0.f, hs_s = 0.f, hs_q = 0.f;
  const bf16x8 ones = {(__bf16)1.0f, (__bf16)1.0f, (__bf16)1.0f, (__bf16)1.0f, (__bf16)1.0f, (__bf16)1.0f, (__bf16)1.0f, (__bf16)1.0f};
  auto mm = [&](const Frags& F, int gi) {
    if (gi < 4) {
      acc[0][gi] = mfma32(F.b[0], F.a[gi], acc[0][gi]);
      acc[1][gi] = mfma32(F.b[1], F.a[gi], acc[1][gi]);
      acc[2][gi] = TA ? mfma32(F.a[gi], F.b[2], acc[2][gi]) : mfma32(F.b[2], F.a[gi], acc[2][gi]);
      acc[3][gi] = mfma32(F.b[3], F.a[gi], acc[3][gi]);
    } else if (TA) {
      acch = mfma32(F.be, F.ah, acch);
      cs0 = mfma32(F.e0, ones, cs0); cs1 = mfma32(F.be, ones, cs1);
    } else {
      accx[0] = mfma32(F.bx, F.a[0], accx[0]); accx[1] = mfma32(F.bx, F.a[1], accx[1]);
      cs0 = mfma32(F.b[0], ones, cs0); cs1 = mfma32(F.b[1], ones, cs1); csx = mfma32(F.bx, ones, csx);
    }
  };
  auto st2 = [&](bf16x8 v, int i, float& s0, float& s1, float& q0, float& q1) {
    const unsigned u = __builtin_bit_cast(u32x4, v)[i];
    const float lo = __uint_as_float(u << 16), hi = __uint_as_float(u & 0xffff0000u);
    s0 += lo; q0 = fmaf(lo, lo, q0); s1 += hi; q1 = fmaf(hi, hi, q1);
  };
  constexpr int RPG = (NRD + 4) / 5;
  auto half = [&](const Frags& Fc, Frags& Fn, int so_n, int ks_n, int d0, int nd, int st_d, int so_d) {
#pragma unroll
    for (int gi = 0; gi < 5; ++gi) {
#pragma unroll
      for (int i = 0; i < RPG; ++i) { const int idx = gi * RPG + i; if (idx < NRD) rdf(Fn, idx, so_n, ks_n); }
      mm(Fc, gi);
      if (gi < 4) st2(TA ? Fc.as : Fc.a[0], gi, ss0, ss1, sq0, sq1);
#pragma unroll
      for (int k = 0; k < 5; ++k) {
        __builtin_amdgcn_sched_group_barrier(0x8, 1, 0);
        __builtin_amdgcn_sched_group_barrier(0x100, 1, 0);
        __builtin_amdgcn_sched_group_barrier(0x2, 2, 0);
      }
      __builtin_amdgcn_sched_group_barrier(0x100, 8, 0);
      asm volatile("" : "+v"(ss0), "+v"(ss1), "+v"(sq0), "+v"(sq1));
      SB0();
      if (gi < nd) { dma(d0 + gi, st_d, so_d); SB0(); }
    }
  };

  Frags F0, F1;
  WAITV(14); BAR();
  STAMP(2);
#pragma unroll
  for (int i = 0; i < NRD; ++i) rdf(F0, i, 0, 0);
  int so = 0, sop = 2 * SLOT;
#pragma unroll 1
  for (int s = 0; s < 8; ++s) {
    half(F0, F1, so, 1, 4, 3, s == 0 ? 8 : s + 2, sop);
    if (TA && s == w) {
#pragma unroll
      for (int i = 0; i < 4; ++i) st2(F0.ah, i, hs_s, hs_s, hs_q, hs_q);
    }
    const int son = (so == 2 * SLOT) ? 0 : so + SLOT;
    WAITV(7);
    WAITL0();
    BAR();
    half(F1, F0, son, 0, 0, 4, s + 3, so);
    if (TA && s == w) {
#pragma unroll
      for (int i = 0; i < 4; ++i) st2(F1.ah, i, hs_s, hs_s, hs_q, hs_q);
    }
    sop = so; so = son;
  }
  float st_s = ss0 + ss1, st_q = sq0 + sq1;
  STAMP(4);
  {
    LAS f32x4* CSw = (LAS f32x4*)(lds + SC_CS);
    if (r16 == 0) {
      const int u0 = TA ? (mi ? 8 + fu : nj) : (mi * 8 + 2 * nj), u1 = TA ? (mi ? 8 + nj : 4 + nj) : (mi * 8 + 2 * nj + 1);
      CSw[u0 * 4 + g] = cs0; CSw[u1 * 4 + g] = cs1;
    }
    st_s = xg_sum(st_s); st_q = xg_sum(st_q);
    const float m = st_s * (1.0f / 512.0f), var = fmaxf(st_q * (1.0f / 512.0f) - m * m, 0.f);
    if (lane < 16) ((LAS f32x2*)(lds + SC_MR))[16 * w + lane] = (f32x2){m, __builtin_amdgcn_rsqf(var + 1e-5f)};
  }
  WAITV(0);
  if (TA) { hs_s = xg_sum(hs_s); hs_q = xg_sum(hs_q); }
  WAITL0(); BAR();
  STAMP(5);
  LAS f32x4* CS = (LAS f32x4*)(lds + SC_CS);
  LAS f32x2* MR = (LAS f32x2*)(lds + SC_MR);
  char* ohl = p.oh + (size_t)lane * 16;
  if (!TA) {
    const f32x4 c0 = CS[(2 * nj) * 4 + g], c1 = CS[(2 * nj + 1) * 4 + g], c2 = CS[(8 + 2 * nj) * 4 + g], c3 = CS[(9 + 2 * nj) * 4 + g];
    const int tp = tm + 2 * nj;
    LAS char* fx = lds + SC_FX;
    { const f32x2 m0 = MR[64 * mi + 16 * nj + r16], m1 = MR[64 * mi + 16 * ((nj + 1) & 3) + r16];
      *(LAS s16x4*)(fx + ((mi * 2 + (nj & 1)) * 4 + nj) * 512 + lane * 8) = pack4(lnc(accx[0], m0, csx));
      *(LAS s16x4*)(fx + ((mi * 2 + (nj & 1)) * 4 + ((nj + 1) & 3)) * 512 + lane * 8) = pack4(lnc(accx[1], m1, csx)); }
#pragma unroll
    for (int i = 0; i < 4; ++i) {
      const int rt = (i + nj) & 3;
      const f32x2 mr = MR[64 * mi + 16 * rt + r16];
      const f32x4 g0 = lnc(mi ? acc[2][i] : acc[0][i], mr, c0), u0 = lnc(mi ? acc[0][i] : acc[2][i], mr, c2);
      const f32x4 g1 = lnc(mi ? acc[3][i] : acc[1][i], mr, c1), u1 = lnc(mi ? acc[1][i] : acc[3][i], mr, c3);
      const u32x2 h0 = __builtin_bit_cast(u32x2, pack4(swiglu4(g0, u0))), h1 = __builtin_bit_cast(u32x2, pack4(swiglu4(g1, u1)));
      if (tp <= 85) *(u32x4*)(ohl + ((size_t)(rg * 8 + 4 * mi + rt) * NKS + 16 + (tp >> 1)) * 1024) = (u32x4){h0[0], h0[1], h1[0], h1[1]};
    }
    WAITL0(); BAR();
    const f32x4 gx = unpack4(*(LAS s16x4*)(fx + ((mi * 2 + 0) * 4 + nj) * 512 + lane * 8)), ux = unpack4(*(LAS s16x4*)(fx + ((mi * 2 + 1) * 4 + nj) * 512 + lane * 8));
    *(s16x4*)(ohl + ((size_t)(rg * 8 + w) * NKS + 16 + (te >> 1)) * 1024 + (te & 1) * 8) = pack4(swiglu4(gx, ux));
  } else {
    if (lane < 16) ((LAS f32x2*)(lds + SC_HS))[lane * 8 + w] = (f32x2){hs_s, hs_q};
    s16x4 Q[4], K[4], V[4], Fv[4];
    const f32x4 cq = CS[nj * 4 + g], ck = CS[(4 + nj) * 4 + g], cf = CS[(8 + fu) * 4 + g];
    const float cv = ((LAS float*)CS)[(8 + nj) * 16 + r16];
#pragma unroll
    for (int rt = 0; rt < 4; ++rt) {
      const f32x2 mr = MR[64 * mi + 16 * rt + r16];
      Q[rt] = pack4(lnc(acc[0][rt], mr, cq)); K[rt] = pack4(lnc(acc[1][rt], mr, ck)); Fv[rt] = pack4(lnc(acc[3][rt], mr, cf));
      const f32x4 t01 = *(LAS f32x4*)(MR + 64 * mi + 16 * rt + 4 * g), t23 = *(LAS f32x4*)(MR + 64 * mi + 16 * rt + 4 * g + 2);
      const f32x4 mm4 = {t01[0], t01[2], t23[0], t23[2]}, rr4 = {t01[1], t01[3], t23[1], t23[3]};
      V[rt] = pack4((acc[2][rt] - mm4 * cv) * rr4);
    }
    LAS char* kvb = lds + SC_KV + nj * 1536;
    LAS char* fx = lds + SC_FX + w * 1024;
    if (mi == 0) { *(LAS s16x4*)(kvb + 512 + lane * 8) = K[3]; *(LAS s16x4*)(kvb + 1024 + lane * 8) = V[3]; }
    *(LAS s16x4*)(fx + lane * 8) = (nj & 1) ? Fv[0] : Fv[2]; *(LAS s16x4*)(fx + 512 + lane * 8) = (nj & 1) ? Fv[1] : Fv[3];
    float mka[8];
#pragma unroll
    for (int r = 0; r < 4; ++r) {
      const int dp = r16 + 16 - (4 * g + r), dc = r16 - (4 * g + r);
      mka[r] = (!(dp & 1) && dp <= 14) ? 0.f : -__builtin_inff();
      mka[4 + r] = (!(dc & 1) && dc >= 0 && dc <= 14) ? 0.f : -__builtin_inff();
    }
    const int RT0 = rg * 8 + 4 * mi;
    char* od = ohl + ((size_t)RT0 * NKS + 2 * cg + (nj >> 1)) * 1024 + (nj & 1) * 8;
    auto attn = [&](s16x4 kp, s16x4 vp, s16x4 kc, s16x4 vc, s16x4 qq, bool noprev, int rt) {
      const f32x4 z = {0, 0, 0, 0};
      const f32x4 sp = mfma16(kp, qq, z), sc = mfma16(kc, qq, z);
      const float sc2 = 0.25f * 1.44269504089f;
      float l[8];
#pragma unroll
      for (int r = 0; r < 4; ++r) { l[r] = fmaf(sp[r], sc2, noprev ? -__builtin_inff() : mka[r]); l[4 + r] = fmaf(sc[r], sc2, mka[4 + r]); }
      float mx = fmaxf(fmaxf(fmaxf(l[0], l[1]), fmaxf(l[2], l[3])), fmaxf(fmaxf(l[4], l[5]), fmaxf(l[6], l[7])));
      mx = xg_max(mx);
      float sum = 0.f;
#pragma unroll
      for (int r = 0; r < 8; ++r) { l[r] = __builtin_amdgcn_exp2f(l[r] - mx); sum += l[r]; }
      sum = xg_sum(sum);
      const float inv = __builtin_amdgcn_rcpf(sum);
      const s16x4 p0 = pack4((f32x4){l[0], l[1], l[2], l[3]} * inv), p1 = pack4((f32x4){l[4], l[5], l[6], l[7]} * inv);
      const bf16x8 pf = __builtin_bit_cast(bf16x8, __builtin_shufflevector(p0, p1, 0, 1, 2, 3, 4, 5, 6, 7));
      const bf16x8 vf = __builtin_bit_cast(bf16x8, __builtin_shufflevector(vp, vc, 0, 1, 2, 3, 4, 5, 6, 7));
      *(s16x4*)(od + (size_t)rt * NKS * 1024) = pack4(mfma32(vf, pf, z));
    };
#pragma unroll
    for (int rt = 1; rt < 4; ++rt) attn(K[rt - 1], V[rt - 1], K[rt], V[rt], Q[rt], false, rt);
    WAITL0(); BAR();
    float hsum = 0.f, hsq = 0.f;
    { LAS f32x4* HS = (LAS f32x4*)(lds + SC_HS) + r16 * 4;
#pragma unroll
      for (int i = 0; i < 4; ++i) { const f32x4 t = HS[i]; hsum += t[0] + t[2]; hsq += t[1] + t[3]; } }
    const float hm = hsum * (1.0f / 512.0f), hr = __builtin_amdgcn_rsqf(fmaxf(hsq * (1.0f / 512.0f) - hm * hm, 0.f) + 1e-5f);
    const s16x4 hv = pack4(lnc(acch, (f32x2){hm, hr}, CS[((mi ? 8 : 4) + nj) * 4 + g]));
    if (mi) *(LAS s16x4*)(kvb + r16 * 32 + g * 8) = hv;
    {
      LAS char* fo = lds + SC_FX + (w ^ 1) * 1024;
      const s16x4 o0 = *(LAS s16x4*)(fo + lane * 8), o1 = *(LAS s16x4*)(fo + 512 + lane * 8);
      const int rb = (nj & 1) * 2;
      const f32x4 a0 = unpack4((nj & 1) ? Fv[2] : Fv[0]), a1 = unpack4((nj & 1) ? Fv[3] : Fv[1]), b0 = unpack4(o0), b1 = unpack4(o1);
      char* hd = ohl + ((size_t)(RT0 + rb) * NKS + 16 + cg) * 1024 + (nj >> 1) * 8;
      *(s16x4*)hd = pack4((nj & 1) ? swiglu4(b0, a0) : swiglu4(a0, b0));
      *(s16x4*)(hd + NKS * 1024) = pack4((nj & 1) ? swiglu4(b1, a1) : swiglu4(a1, b1));
    }
    WAITL0(); BAR();
    s16x4 Kp, Vp;
    if (mi == 0) { Kp = hv; Vp = __builtin_amdgcn_ds_read_tr16_b64_v4i16((LAS s16x4*)(kvb + (4 * g + q) * 32 + pp * 8)); }
    else { Kp = *(LAS s16x4*)(kvb + 512 + lane * 8); Vp = *(LAS s16x4*)(kvb + 1024 + lane * 8); }
    attn(Kp, Vp, K[0], V[0], Q[0], RT0 == 0, 0);
  }
  STAMP(6);
  STAMP(7);
}

__global__ __launch_bounds__(512, 2) void k_phase1(Params p) {
  extern __shared__ __attribute__((aligned(16))) char shm[];
  LAS char* lds = (LAS char*)shm;
  const int b = blockIdx.x, xcd = b & 7, idx = b >> 3;
  const int rg = (xcd & 1) * 8 + (idx & 7), cg = (xcd >> 1) * 4 + (idx >> 3);
#define ROTF 0
  const int rot = ROTF;
  if (cg < 8) phase1<true>(p, lds, rg, cg, rot); else phase1<false>(p, lds, rg, cg - 8, rot);
}

constexpr int P2D = 5;
constexpr int P2NA = P2D + 1;
constexpr int P2NS = P2D + 2;
DI void gload16(bf16x8& dst, const void* p) { asm volatile("global_load_dwordx4 %0, %1, off" : "=v"(dst) : "v"(p) : "memory"); }
template <int N> DI void waitA(bf16x8& a, bf16x8& b) { asm volatile("s_waitcnt vmcnt(%2)" : "+v"(a), "+v"(b) : "n"(N) : "memory"); }
template <int N> DI void waitVn() { asm volatile("s_waitcnt vmcnt(%0)" :: "n"(N) : "memory"); }
__global__ __launch_bounds__(512, 2) void k_phase2(Params p) {
  extern __shared__ __attribute__((aligned(16))) char shm[];
  LAS char* lds = (LAS char*)shm;
  const int tid = threadIdx.x, lane = tid & 63;
  const int w = rfl(tid >> 6), kh = w >> 2, m4 = w & 3;
  const int r16 = lane & 15, g = lane >> 4, q = r16 >> 2, pp = lane & 3;
  const unsigned ldsb = (unsigned)(uintptr_t)lds;
  const int b = blockIdx.x, xcd = b & 7, idx = b >> 3;
  const int rt64 = xcd * 4 + (idx & 3), ct64 = idx >> 2;
  constexpr int PH = 1; STAMP(0);
  constexpr int NR = (NKS + 3) / 4;
  const char* pa = p.oh + ((size_t)(rt64 * 4 + m4) * NKS + kh) * 1024 + lane * 16;
  int boff;
  { const int row = 8 * m4 + (lane >> 3), pc = lane & 7; const int c = (pc >> 1) ^ ((row >> 1) & 3);
    boff = row * 1024 + ct64 * 128 + (2 * c + (pc & 1)) * 16; }
  const char* pbo = p.wo + (size_t)kh * 32768 + boff;
  const char* pb2 = p.w2 + (long)(kh - 16) * 32768 + boff;
  bf16x8 A[P2NA][2];
  auto issue = [&](int r) {
#pragma unroll
    for (int j = 0; j < 2; ++j) {
      int ke = 4 * r + 2 * j;
      if (ke + 1 > NKS - 1) ke -= 2 * (ke + 1 > NKS - 1 ? 1 : 0) * 0;
      const bool over = (ke + 1 > NKS - 1);
      const long ko = over ? (long)ke - 2 * kh : (long)ke;
      gload16(A[r % P2NA][j], pa + ko * 1024);
      glds16((ke < 16 ? pbo : pb2) + ko * 32768, ldsb + (r % P2NS) * 16384 + (2 * j) * 4096 + kh * 4096 + m4 * 1024);
    }
  };
#pragma unroll
  for (int r = 0; r < P2D; ++r) issue(r);
  STAMP(1);
  int bl[4];
#pragma unroll
  for (int c = 0; c < 4; ++c) bl[c] = (4 * g + q) * 128 + ((c ^ ((2 * g + (q >> 1)) & 3)) << 5) + pp * 8 + kh * 4096;
  f32x4 acc[4] = {{0, 0, 0, 0}, {0, 0, 0, 0}, {0, 0, 0, 0}, {0, 0, 0, 0}};
  const size_t ooff = (size_t)(rt64 * 64 + 16 * m4 + r16) * 1024 + (ct64 * 64 + 32 * kh + 4 * g) * 2;
  bf16x4 xv0, xv1;
  bf16x8 bf[2][2][4];
  auto readB = [&](int r) {
    LAS char* sb = lds + (r % P2NS) * 16384;
#pragma unroll
    for (int j = 0; j < 2; ++j)
#pragma unroll
      for (int c = 0; c < 4; ++c) bf[r & 1][j][c] = ldsB(sb + j * 8192 + bl[c], 2048);
  };
  waitVn<4 * (P2D - 1)>(); BAR();
  STAMP(2);
  readB(0);
#pragma unroll
  for (int r = 0; r < NR; ++r) {
    if (r + 1 < NR) {
      const int after = (NR - 1 - (r + 1)) < (P2D - 2) ? (NR - 1 - (r + 1)) : (P2D - 2);
      if (after == 3) waitVn<12>(); else if (after == 2) waitVn<8>(); else if (after == 1) waitVn<4>(); else waitVn<0>();
      BAR();
      if (r == 8) STAMP(3);
      if (r + P2D < NR) issue(r + P2D);
      readB(r + 1);
    } else { xv0 = *(const bf16x4*)(p.x + ooff); xv1 = *(const bf16x4*)(p.x + ooff + 32); }
    asm volatile("" : "+v"(A[r % P2NA][0]), "+v"(A[r % P2NA][1]));
#pragma unroll
    for (int j = 0; j < 2; ++j) {
      if (4 * r + 2 * j + 1 > NKS - 1 && kh) continue;
#pragma unroll
      for (int c = 0; c < 4; ++c) acc[c] = mfma32(bf[r & 1][j][c], A[r % P2NA][j], acc[c]);
    }
  }
  STAMP(4);
  {
    LAS f32x4* red = (LAS f32x4*)(lds + 7 * 16384);
    red[(w * 2 + 0) * 64 + lane] = kh ? acc[0] : acc[2];
    red[(w * 2 + 1) * 64 + lane] = kh ? acc[1] : acc[3];
    WAITL0(); BAR();
    const f32x4 o0 = red[((w ^ 4) * 2 + 0) * 64 + lane], o1 = red[((w ^ 4) * 2 + 1) * 64 + lane];
    const f32x4 s0 = (kh ? acc[2] : acc[0]) + o0, s1 = (kh ? acc[3] : acc[1]) + o1;
    const f32x4 x0 = __builtin_convertvector(xv0, f32x4), x1 = __builtin_convertvector(xv1, f32x4);
    *(s16x4*)(p.out + ooff) = pack4(s0 + x0);
    *(s16x4*)(p.out + ooff + 32) = pack4(s1 + x1);
  }
  STAMP(5);
  STAMP(6); STAMP(7);
}

extern "C" void kernel_launch(void* const* d_in, const int* in_sizes, int n_in, void* d_out, int out_size, void* d_ws, size_t ws_size,
                              hipStream_t stream) {
  Params p;
  p.x = (const char*)d_in[0]; p.wqkv = (const char*)d_in[1]; p.wo = (const char*)d_in[2];
  p.w1 = (const char*)d_in[4]; p.w3 = (const char*)d_in[5]; p.w2 = (const char*)d_in[6];
  p.oh = (char*)d_ws; p.out = (char*)d_out; p.stamps = (unsigned long long*)((char*)d_ws + (128u << 20));
  (void)hipFuncSetAttribute((const void*)k_phase1, hipFuncAttributeMaxDynamicSharedMemorySize, P1_LDS);
  (void)hipFuncSetAttribute((const void*)k_phase2, hipFuncAttributeMaxDynamicSharedMemorySize, P2_LDS);
  k_phase1<<<256, 512, P1_LDS, stream>>>(p);
  k_phase2<<<256, 512, P2_LDS, stream>>>(p);
}
```

```cpp
#include <hip/hip_runtime.h>
#include <stdint.h>

#define LAS __attribute__((address_space(3)))
#define DI __device__ __forceinline__
typedef __bf16 bf16x8 __attribute__((ext_vector_type(8)));
typedef __bf16 bf16x4 __attribute__((ext_vector_type(4)));
typedef short s16x4 __attribute__((ext_vector_type(4)));
typedef float f32x4 __attribute__((ext_vector_type(4)));
typedef float f32x2 __attribute__((ext_vector_type(2)));
typedef unsigned u32x2 __attribute__((ext_vector_type(2)));
typedef unsigned u32x4 __attribute__((ext_vector_type(4)));

constexpr int NKS = 59;
constexpr int SLOT = 53248;
constexpr int WOFF = 16384, EOFF = 49152;
constexpr int DUMMY = 3 * SLOT;
constexpr int SC_CS = DUMMY + 1024;
constexpr int SC_MR = SC_CS + 1152;
constexpr int P1_LDS = 163840;
constexpr int SC_HS = 86016;
constexpr int SC_KV = SC_HS + 1024;
constexpr int SC_FX = SC_KV + 6144;
constexpr int SC_RED = 86016;
__host__ __device__ constexpr int bslot(int r) { return (r & 7) < 3 ? 2 * SLOT + (r & 7) * 16384 : (r & 7) < 6 ? ((r & 7) - 3) * 16384 : SLOT + ((r & 7) - 6) * 16384; }
constexpr unsigned long long FMAGIC = 0x5AC3E17Bull << 32;
constexpr size_t WS_FLAGS = 4096, WS_OH = 65536;

#define STAMP(i) do {} while (0)

DI int rfl(int v) { return __builtin_amdgcn_readfirstlane(v); }
DI void glds16(const void* g, unsigned lds_dst) {
  unsigned keep;
  asm volatile("s_mov_b32 %0, m0\n\ts_mov_b32 m0, %2\n\ts_nop 0\n\tglobal_load_lds_dwordx4 %1, off\n\ts_mov_b32 m0, %0"
               : "=&s"(keep) : "v"(g), "s"(lds_dst) : "memory");
}
DI void glds16s(const char* sbase, unsigned voff, unsigned lds_dst) {
  asm volatile("s_mov_b32 m0, %2\n\ts_nop 0\n\tglobal_load_lds_dwordx4 %1, %0" :: "s"(sbase), "v"(voff), "s"(lds_dst) : "memory", "m0");
}
#define WAITV(n) asm volatile("s_waitcnt vmcnt(" #n ")" ::: "memory")
#define WAITL0() do { asm volatile("" ::: "memory"); __builtin_amdgcn_s_waitcnt(0xC07F); asm volatile("" ::: "memory"); } while (0)
#define BAR() do { asm volatile("" ::: "memory"); __builtin_amdgcn_s_barrier(); asm volatile("" ::: "memory"); } while (0)
#define SB0() __builtin_amdgcn_sched_barrier(0)

DI bf16x8 ldsA(LAS char* p) { return *(LAS bf16x8*)p; }
DI bf16x8 ldsB(LAS char* p, int toff) {
  s16x4 lo = __builtin_amdgcn_ds_read_tr16_b64_v4i16((LAS s16x4*)p);
  s16x4 hi = __builtin_amdgcn_ds_read_tr16_b64_v4i16((LAS s16x4*)(p + toff));
  return __builtin_bit_cast(bf16x8, __builtin_shufflevector(lo, hi, 0, 1, 2, 3, 4, 5, 6, 7));
}
DI f32x4 mfma32(bf16x8 a, bf16x8 b, f32x4 c) { return __builtin_amdgcn_mfma_f32_16x16x32_bf16(a, b, c, 0, 0, 0); }
DI f32x4 mfma16(s16x4 a, s16x4 b, f32x4 c) { return __builtin_amdgcn_mfma_f32_16x16x16bf16_1k(a, b, c, 0, 0, 0); }
DI s16x4 pack4(f32x4 v) { return __builtin_bit_cast(s16x4, __builtin_convertvector(v, bf16x4)); }
DI f32x4 unpack4(s16x4 v) { return __builtin_convertvector(__builtin_bit_cast(bf16x4, v), f32x4); }
DI float xg_sum(float v) {
  auto r = __builtin_amdgcn_permlane16_swap(__float_as_uint(v), __float_as_uint(v), false, false);
  v = __uint_as_float(r[0]) + __uint_as_float(r[1]);
  auto s = __builtin_amdgcn_permlane32_swap(__float_as_uint(v), __float_as_uint(v), false, false);
  return __uint_as_float(s[0]) + __uint_as_float(s[1]);
}
DI float xg_max(float v) {
  auto r = __builtin_amdgcn_permlane16_swap(__float_as_uint(v), __float_as_uint(v), false, false);
  v = fmaxf(__uint_as_float(r[0]), __uint_as_float(r[1]));
  auto s = __builtin_amdgcn_permlane32_swap(__float_as_uint(v), __float_as_uint(v), false, false);
  return fmaxf(__uint_as_float(s[0]), __uint_as_float(s[1]));
}
DI f32x4 lnc(f32x4 a, f32x2 mr, f32x4 c) { return (a - c * mr[0]) * mr[1]; }
DI f32x4 swiglu4(f32x4 g, f32x4 u) {
  const f32x4 t = g * -1.44269504089f;
  f32x4 d;
#pragma unroll
  for (int r = 0; r < 4; ++r) d[r] = __builtin_amdgcn_rcpf(1.0f + __builtin_amdgcn_exp2f(t[r]));
  return g * u * d;
}

struct Params {
  const char* x; const char* wqkv; const char* wo; const char* w1; const char* w3; const char* w2;
  char* ws; char* out; unsigned long long* stamps;
};
typedef __attribute__((address_space(1))) unsigned gu32;
typedef __attribute__((address_space(1))) unsigned long long gu64;
#define RLX_AGENT __ATOMIC_RELAXED, __HIP_MEMORY_SCOPE_AGENT
DI void st16(__amdgpu_buffer_rsrc_t r, unsigned off, u32x4 v) { __builtin_amdgcn_raw_buffer_store_b128(v, r, off, 0, 16); }
DI void st8(__amdgpu_buffer_rsrc_t r, unsigned off, s16x4 v) { __builtin_amdgcn_raw_buffer_store_b64(__builtin_bit_cast(u32x2, v), r, off, 0, 16); }

struct Frags { bf16x8 a[4], as, ah, b[4], bx, be, e0; };

template <bool TA>
DI void phase1(const Params& p, LAS char* lds, int rg, int cg, int ct64) {
  constexpr int rot = 0;
  const int tid = threadIdx.x, lane = tid & 63;
  const int w = rfl(tid >> 6), mi = w >> 2, nj = w & 3;
  const int r16 = lane & 15, g = lane >> 4, q = r16 >> 2, pp = lane & 3;
  constexpr int PH = 0; STAMP(0);
  const unsigned ldsb = (unsigned)(uintptr_t)lds;
  const int row0 = rg * 128;
  const int tm = TA ? 2 * cg : 16 + 18 * (cg >> 1) + ((cg & 1) ? 10 : 0);
  const int te = 16 + 18 * (cg >> 1) + 8 + (cg & 1);
  unsigned vx[2], vw[4], ve;
#pragma unroll
  for (int m = 0; m < 2; ++m) {
    const int row = 16 * w + 8 * m + (lane >> 3);
    vx[m] = (unsigned)((row0 + row) * 1024 + (((lane & 7) ^ ((row >> 1) & 7)) << 4));
  }
  const char* bw[4]; unsigned ldw[4], dw[4];
#pragma unroll
  for (int m = 0; m < 4; ++m) {
    if (!TA || m < 2) {
      const int pi = TA ? (2 * w + m) : (2 * w + (m & 1));
      const int kr = 4 * pi + (lane >> 4), pc = lane & 15;
      const int c = (pc >> 1) ^ ((kr & 3) | (((kr >> 3) & 1) << 2));
      if (TA) { bw[m] = p.wqkv; ldw[m] = 3072; vw[m] = kr * 3072 + ((c >> 2) * 512 + (4 * cg + (c & 3)) * 16) * 2 + (pc & 1) * 16; dw[m] = WOFF + pi * 1024; }
      else { bw[m] = (m >> 1) ? p.w3 : p.w1; ldw[m] = 2752; int t = tm + c; t = t > 85 ? 85 : t; vw[m] = kr * 2752 + t * 32 + (pc & 1) * 16; dw[m] = WOFF + (m >> 1) * 16384 + pi * 1024; }
    } else if (m == 2) {
      const int kr = 8 * w + (lane >> 3), pc = lane & 7;
      const int c = (pc >> 1) ^ (((kr >> 1) & 1) | (((kr >> 3) & 1) << 1));
      bw[m] = p.wqkv; ldw[m] = 3072; vw[m] = kr * 3072 + (1024 + (4 * cg + c) * 16) * 2 + (pc & 1) * 16; dw[m] = WOFF + 16384 + w * 1024;
    } else {
      const int kr = 16 * (w & 3) + (lane >> 2), pc = lane & 3;
      const int e = (pc >> 1) ^ ((kr >> 3) & 1);
      bw[m] = mi ? p.w3 : p.w1; ldw[m] = 2752; vw[m] = kr * 2752 + (tm + e) * 32 + (pc & 1) * 16; dw[m] = WOFF + 24576 + mi * 4096 + (w & 3) * 1024;
    }
  }
  const bool has_e = TA ? (w < 2) : (w < 4);
  const char* be_; unsigned lde, de;
  if (TA) {
    const int row = 8 * (w & 1) + (lane >> 3);
    int gr = row0 - 16 + row; gr = gr < 0 ? 0 : gr;
    be_ = p.x; lde = 2; ve = gr * 1024 + (((lane & 7) ^ ((row >> 1) & 7)) << 4); de = EOFF + w * 1024;
  } else {
    const int pos = 32 * (w & 1) + (lane >> 1), kr = pos ^ (((pos >> 3) & 1) << 2);
    be_ = (w & 2) ? p.w3 : p.w1; lde = 2752; ve = kr * 2752 + te * 32 + (lane & 1) * 16; de = EOFF + (w & 3) * 1024;
  }
  if (!has_e) { be_ = p.x; lde = 0; ve = 0; }
  unsigned vb;
  { const int row = 8 * nj + (lane >> 3), pc = lane & 7; const int c = (pc >> 1) ^ ((row >> 1) & 3);
    vb = row * 1024 + ct64 * 128 + (2 * c + (pc & 1)) * 16; }
  const unsigned emask = has_e ? 0xffffffffu : 0u;
  if (!has_e) de = DUMMY;
  const char* sx = p.x; const char* se = be_; const char* sw[4] = {bw[0], bw[1], bw[2], bw[3]};
  auto dma = [&](int i, int so) {
    if (i == 0) { glds16s(se, ve, ldsb + (so & emask) + de); se += 64u * lde; }
    else if (i <= 2) { glds16s(sx, vx[i - 1], ldsb + so + (2 * w + i - 1) * 1024); if (i == 2) sx += 128; }
    else { glds16s(sw[i - 3], vw[i - 3], ldsb + so + dw[i - 3]); sw[i - 3] += 64u * ldw[i - 3]; }
  };
  auto dmab = [&](int rr, int j) {
    const int ks2 = 4 * rr + 2 * j + mi;
    const char* src = (4 * rr + 2 * j < 16) ? p.wo + ks2 * 32768 : p.w2 + (ks2 - 16) * 32768;
    glds16s(src, vb, ldsb + bslot(rr) + (2 * j + mi) * 4096 + nj * 1024);
  };
#pragma unroll
  for (int st = 0; st < 2; ++st)
#pragma unroll
    for (int i = 0; i < 7; ++i) dma(i, st * SLOT);
  STAMP(1);
  const int xa0 = r16 * 128 + ((g ^ (r16 >> 1)) << 4);
  const int fl = q | ((g & 1) << 2);
  const int w256 = (8 * g + q) * 256 + (fl << 5) + pp * 8;
  const int w128 = (8 * g + q) * 128 + ((((q >> 1) & 1) | ((g & 1) << 1)) << 5) + pp * 8;
  const int w64 = (8 * g + q) * 64 + ((g & 1) << 5) + pp * 8;
  const int w32 = (8 * g + ((g & 1) << 2) + q) * 32 + pp * 8;
  const int dt32 = (g & 1) ? -128 : 128;
  const int tb0 = (w256 ^ (nj << 6)) + (mi ? 16384 : 0), tb2 = (w256 ^ (nj << 6)) + (mi ? 0 : 16384);
  const int tbx = EOFF + (nj & 1) * 2048 + w32;
  const int taq = w256 ^ (nj << 5), tak = w256 ^ ((4 + nj) << 5), tav = 16384 + (w128 ^ (nj << 5)), taf = 24576 + (nj & 1) * 4096 + (w64 ^ ((nj >> 1) << 5));
  const int fu = 4 + ((nj >> 1) | ((nj & 1) << 1));
  int xr[4];
#pragma unroll
  for (int i = 0; i < 4; ++i) xr[i] = mi * 8192 + (TA ? i : ((i + nj) & 3)) * 2048;
  const int bea = mi ? tav : tak, bes = mi ? 4096 : 8192, bet = mi ? 512 : 1024;
  const int e0a = mi ? taf : taq, e0s = mi ? 2048 : 8192, e0t = mi ? 256 : 1024;
  constexpr int NRD = TA ? 12 : 9;
  auto ldsB2 = [&](LAS char* p0, LAS char* p1) {
    s16x4 lo = __builtin_amdgcn_ds_read_tr16_b64_v4i16((LAS s16x4*)p0), hi = __builtin_amdgcn_ds_read_tr16_b64_v4i16((LAS s16x4*)p1);
    return __builtin_bit_cast(bf16x8, __builtin_shufflevector(lo, hi, 0, 1, 2, 3, 4, 5, 6, 7));
  };
  auto rdf = [&](Frags& F, int i, int so, int ks) {
    LAS char* sb = lds + so;
    const int xa = xa0 ^ (ks * 64);
    LAS char* wb = sb + WOFF;
    if (TA) {
      switch (i) {
        case 0: F.a[0] = ldsA(sb + xa + xr[0]); break;
        case 1: F.b[0] = ldsB(wb + taq + ks * 8192, 1024); break;
        case 2: F.b[1] = ldsB(wb + tak + ks * 8192, 1024); break;
        case 3: F.b[2] = ldsB(wb + tav + ks * 4096, 512); break;
        case 4: F.b[3] = ldsB(wb + taf + ks * 2048, 256); break;
        case 5: F.a[1] = ldsA(sb + xa + xr[1]); break;
        case 6: F.a[2] = ldsA(sb + xa + xr[2]); break;
        case 7: F.a[3] = ldsA(sb + xa + xr[3]); break;
        case 8: F.as = ldsA(sb + xa + w * 2048); break;
        case 9: F.be = ldsB2(wb + bea + ks * bes, wb + bea + ks * bes + bet); break;
        case 10: F.ah = ldsA(sb + EOFF + xa); break;
        default: F.e0 = ldsB2(wb + e0a + ks * e0s, wb + e0a + ks * e0s + e0t); break;
      }
    } else {
      switch (i) {
        case 0: F.a[0] = ldsA(sb + xa + xr[0]); break;
        case 1: F.b[0] = ldsB(wb + tb0 + ks * 8192, 1024); break;
        case 2: F.b[1] = ldsB(wb + (tb0 ^ 32) + ks * 8192, 1024); break;
        case 3: F.b[2] = ldsB(wb + tb2 + ks * 8192, 1024); break;
        case 4: F.b[3] = ldsB(wb + (tb2 ^ 32) + ks * 8192, 1024); break;
        case 5: F.a[1] = ldsA(sb + xa + xr[1]); break;
        case 6: F.a[2] = ldsA(sb + xa + xr[2]); break;
        case 7: F.a[3] = ldsA(sb + xa + xr[3]); break;
        default: F.bx = ldsB2(sb + tbx + ks * 1024, sb + tbx + ks * 1024 + dt32); break;
      }
    }
  };
  f32x4 acc[4][4], accx[2], acch = {0, 0, 0, 0}, cs0 = {0, 0, 0, 0}, cs1 = {0, 0, 0, 0}, csx = {0, 0, 0, 0};
  accx[0] = (f32x4){0, 0, 0, 0}; accx[1] = (f32x4){0, 0, 0, 0};
#pragma unroll
  for (int c = 0; c < 4; ++c)
#pragma unroll
    for (int rt = 0; rt < 4; ++rt) acc[c][rt] = (f32x4){0, 0, 0, 0};
  float ss0 = 0.f, ss1 = 0.f, sq0 = 0.f, sq1 = 0.f, hs_s = 0.f, hs_q = 0.f;
  const bf16x8 ones = {(__bf16)1.0f, (__bf16)1.0f, (__bf16)1.0f, (__bf16)1.0f, (__bf16)1.0f, (__bf16)1.0f, (__bf16)1.0f, (__bf16)1.0f};
  auto mm = [&](const Frags& F, int gi) {
    if (gi < 4) {
      acc[0][gi] = mfma32(F.b[0], F.a[gi], acc[0][gi]);
      acc[1][gi] = mfma32(F.b[1], F.a[gi], acc[1][gi]);
      acc[2][gi] = TA ? mfma32(F.a[gi], F.b[2], acc[2][gi]) : mfma32(F.b[2], F.a[gi], acc[2][gi]);
      acc[3][gi] = mfma32(F.b[3], F.a[gi], acc[3][gi]);
    } else if (TA) {
      acch = mfma32(F.be, F.ah, acch);
      cs0 = mfma32(F.e0, ones, cs0); cs1 = mfma32(F.be, ones, cs1);
    } else {
      accx[0] = mfma32(F.bx, F.a[0], accx[0]); accx[1] = mfma32(F.bx, F.a[1], accx[1]);
      cs0 = mfma32(F.b[0], ones, cs0); cs1 = mfma32(F.b[1], ones, cs1); csx = mfma32(F.bx, ones, csx);
    }
  };
  auto st2 = [&](bf16x8 v, int i, float& s0, float& s1, float& q0, float& q1) {
    const unsigned u = __builtin_bit_cast(u32x4, v)[i];
    const float lo = __uint_as_float(u << 16), hi = __uint_as_float(u & 0xffff0000u);
    s0 += lo; q0 = fmaf(lo, lo, q0); s1 += hi; q1 = fmaf(hi, hi, q1);
  };
  constexpr int RPG = (NRD + 4) / 5;
  auto half = [&](const Frags& Fc, Frags& Fn, int so_n, int ks_n, int mode, int d0, int nd, int so_d, int bt) {
#pragma unroll
    for (int gi = 0; gi < 5; ++gi) {
#pragma unroll
      for (int i = 0; i < RPG; ++i) { const int idx = gi * RPG + i; if (idx < NRD) rdf(Fn, idx, so_n, ks_n); }
      mm(Fc, gi);
      if (gi < 4) st2(TA ? Fc.as : Fc.a[0], gi, ss0, ss1, sq0, sq1);
#pragma unroll
      for (int k = 0; k < 5; ++k) {
        __builtin_amdgcn_sched_group_barrier(0x8, 1, 0);
        __builtin_amdgcn_sched_group_barrier(0x100, 1, 0);
        __builtin_amdgcn_sched_group_barrier(0x2, 2, 0);
      }
      __builtin_amdgcn_sched_group_barrier(0x100, 8, 0);
      asm volatile("" : "+v"(ss0), "+v"(ss1), "+v"(sq0), "+v"(sq1));
      SB0();
      if (mode == 0 && gi < nd) { dma(d0 + gi, so_d); SB0(); }
      if (mode == 1 && gi < nd && d0 + gi < 6) { dmab(3 * bt + ((d0 + gi) >> 1), (d0 + gi) & 1); SB0(); }
    }
  };
  auto hstat = [&](const Frags& F, int s) {
    if (TA && s == w) {
#pragma unroll
      for (int i = 0; i < 4; ++i) st2(F.ah, i, hs_s, hs_s, hs_q, hs_q);
    }
  };

  Frags F0, F1;
  WAITV(7); BAR();
  STAMP(2);
#pragma unroll
  for (int i = 0; i < NRD; ++i) rdf(F0, i, 0, 0);
#pragma unroll
  for (int i = 0; i < 3; ++i) dma(i, 2 * SLOT);
  half(F0, F1, 0, 1, 0, 3, 4, 2 * SLOT, 0);
  hstat(F0, 0);
  int sop = 0, so = SLOT;
#pragma unroll 1
  for (int t = 1; t < 6; ++t) {
    WAITV(7); WAITL0(); BAR();
    half(F1, F0, so, 0, 0, 0, 4, sop, 0);
    hstat(F1, t - 1);
    half(F0, F1, so, 1, 0, 4, 3, sop, 0);
    hstat(F0, t);
    sop = so; so = (so == 2 * SLOT) ? 0 : so + SLOT;
  }
  WAITV(7); WAITL0(); BAR();
  half(F1, F0, 0, 0, 1, 0, 4, 0, 0); hstat(F1, 5);
  half(F0, F1, 0, 1, 1, 4, 3, 0, 0); hstat(F0, 6);
  WAITV(6); WAITL0(); BAR();
  half(F1, F0, SLOT, 0, 1, 0, 4, 0, 1); hstat(F1, 6);
  half(F0, F1, SLOT, 1, 1, 4, 3, 0, 1); hstat(F0, 7);
#pragma unroll
  for (int gi = 0; gi < 5; ++gi) {
    mm(F1, gi);
    if (gi < 4) st2(TA ? F1.as : F1.a[0], gi, ss0, ss1, sq0, sq1);
  }
  hstat(F1, 7);
  float st_s = ss0 + ss1, st_q = sq0 + sq1;
  STAMP(4);
  {
    LAS f32x4* CSw = (LAS f32x4*)(lds + SC_CS);
    if (r16 == 0) {
      const int u0 = TA ? (mi ? 8 + fu : nj) : (mi * 8 + 2 * nj), u1 = TA ? (mi ? 8 + nj : 4 + nj) : (mi * 8 + 2 * nj + 1);
      CSw[u0 * 4 + g] = cs0; CSw[u1 * 4 + g] = cs1;
    }
    st_s = xg_sum(st_s); st_q = xg_sum(st_q);
    const float m = st_s * (1.0f / 512.0f), var = fmaxf(st_q * (1.0f / 512.0f) - m * m, 0.f);
    if (lane < 16) ((LAS f32x2*)(lds + SC_MR))[16 * w + lane] = (f32x2){m, __builtin_amdgcn_rsqf(var + 1e-5f)};
  }
  if (TA) { hs_s = xg_sum(hs_s); hs_q = xg_sum(hs_q); }
  WAITL0(); BAR();
  dmab(6, 0); dmab(6, 1); dmab(7, 0); dmab(7, 1);
  STAMP(5);
  LAS f32x4* CS = (LAS f32x4*)(lds + SC_CS);
  LAS f32x2* MR = (LAS f32x2*)(lds + SC_MR);
  const __amdgpu_buffer_rsrc_t ohr = __builtin_amdgcn_make_buffer_rsrc(p.ws + WS_OH, 0, 128 * NKS * 1024, 0x00020000);
  const unsigned ohl = lane * 16;
  if (!TA) {
    const f32x4 c0 = CS[(2 * nj) * 4 + g], c1 = CS[(2 * nj + 1) * 4 + g], c2 = CS[(8 + 2 * nj) * 4 + g], c3 = CS[(9 + 2 * nj) * 4 + g];
    const int tp = tm + 2 * nj;
    LAS char* fx = lds + SC_FX;
    { const f32x2 m0 = MR[64 * mi + 16 * nj + r16], m1 = MR[64 * mi + 16 * ((nj + 1) & 3) + r16];
      *(LAS s16x4*)(fx + ((mi * 2 + (nj & 1)) * 4 + nj) * 512 + lane * 8) = pack4(lnc(accx[0], m0, csx));
      *(LAS s16x4*)(fx + ((mi * 2 + (nj & 1)) * 4 + ((nj + 1) & 3)) * 512 + lane * 8) = pack4(lnc(accx[1], m1, csx)); }
#pragma unroll
    for (int i = 0; i < 4; ++i) {
      const int rt = (i + nj) & 3;
      const f32x2 mr = MR[64 * mi + 16 * rt + r16];
      const f32x4 g0 = lnc(mi ? acc[2][i] : acc[0][i], mr, c0), u0 = lnc(mi ? acc[0][i] : acc[2][i], mr, c2);
      const f32x4 g1 = lnc(mi ? acc[3][i] : acc[1][i], mr, c1), u1 = lnc(mi ? acc[1][i] : acc[3][i], mr, c3);
      const u32x2 h0 = __builtin_bit_cast(u32x2, pack4(swiglu4(g0, u0))), h1 = __builtin_bit_cast(u32x2, pack4(swiglu4(g1, u1)));
      if (tp <= 85) st16(ohr, ohl + ((rg * 8 + 4 * mi + rt) * NKS + 16 + (tp >> 1)) * 1024, (u32x4){h0[0], h0[1], h1[0], h1[1]});
    }
    WAITL0(); BAR();
    const f32x4 gx = unpack4(*(LAS s16x4*)(fx + ((mi * 2 + 0) * 4 + nj) * 512 + lane * 8)), ux = unpack4(*(LAS s16x4*)(fx + ((mi * 2 + 1) * 4 + nj) * 512 + lane * 8));
    st8(ohr, ohl + ((rg * 8 + w) * NKS + 16 + (te >> 1)) * 1024 + (te & 1) * 8, pack4(swiglu4(gx, ux)));
  } else {
    if (lane < 16) ((LAS f32x2*)(lds + SC_HS))[lane * 8 + w] = (f32x2){hs_s, hs_q};
    s16x4 Q[4], K[4], V[4], Fv[4];
    const f32x4 cq = CS[nj * 4 + g], ck = CS[(4 + nj) * 4 + g], cf = CS[(8 + fu) * 4 + g];
    const float cv = ((LAS float*)CS)[(8 + nj) * 16 + r16];
#pragma unroll
    for (int rt = 0; rt < 4; ++rt) {
      const f32x2 mr = MR[64 * mi + 16 * rt + r16];
      Q[rt] = pack4(lnc(acc[0][rt], mr, cq)); K[rt] = pack4(lnc(acc[1][rt], mr, ck)); Fv[rt] = pack4(lnc(acc[3][rt], mr, cf));
      const f32x4 t01 = *(LAS f32x4*)(MR + 64 * mi + 16 * rt + 4 * g), t23 = *(LAS f32x4*)(MR + 64 * mi + 16 * rt + 4 * g + 2);
      const f32x4 mm4 = {t01[0], t01[2], t23[0], t23[2]}, rr4 = {t01[1], t01[3], t23[1], t23[3]};
      V[rt] = pack4((acc[2][rt] - mm4 * cv) * rr4);
    }
    LAS char* kvb = lds + SC_KV + nj * 1536;
    LAS char* fx = lds + SC_FX + w * 1024;
    if (mi == 0) { *(LAS s16x4*)(kvb + 512 + lane * 8) = K[3]; *(LAS s16x4*)(kvb + 1024 + lane * 8) = V[3]; }
    *(LAS s16x4*)(fx + lane * 8) = (nj & 1) ? Fv[0] : Fv[2]; *(LAS s16x4*)(fx + 512 + lane * 8) = (nj & 1) ? Fv[1] : Fv[3];
    float mka[8];
#pragma unroll
    for (int r = 0; r < 4; ++r) {
      const int dp = r16 + 16 - (4 * g + r), dc = r16 - (4 * g + r);
      mka[r] = (!(dp & 1) && dp <= 14) ? 0.f : -__builtin_inff();
      mka[4 + r] = (!(dc & 1) && dc >= 0 && dc <= 14) ? 0.f : -__builtin_inff();
    }
    const int RT0 = rg * 8 + 4 * mi;
    const unsigned od = ohl + (RT0 * NKS + 2 * cg + (nj >> 1)) * 1024 + (nj & 1) * 8;
    auto attn = [&](s16x4 kp, s16x4 vp, s16x4 kc, s16x4 vc, s16x4 qq, bool noprev, int rt) {
      const f32x4 z = {0, 0, 0, 0};
      const f32x4 sp = mfma16(kp, qq, z), sc = mfma16(kc, qq, z);
      const float sc2 = 0.25f * 1.44269504089f;
      float l[8];
#pragma unroll
      for (int r = 0; r < 4; ++r) { l[r] = fmaf(sp[r], sc2, noprev ? -__builtin_inff() : mka[r]); l[4 + r] = fmaf(sc[r], sc2, mka[4 + r]); }
      float mx = fmaxf(fmaxf(fmaxf(l[0], l[1]), fmaxf(l[2], l[3])), fmaxf(fmaxf(l[4], l[5]), fmaxf(l[6], l[7])));
      mx = xg_max(mx);
      float sum = 0.f;
#pragma unroll
      for (int r = 0; r < 8; ++r) { l[r] = __builtin_amdgcn_exp2f(l[r] - mx); sum += l[r]; }
      sum = xg_sum(sum);
      const float inv = __builtin_amdgcn_rcpf(sum);
      const s16x4 p0 = pack4((f32x4){l[0], l[1], l[2], l[3]} * inv), p1 = pack4((f32x4){l[4], l[5], l[6], l[7]} * inv);
      const bf16x8 pf = __builtin_bit_cast(bf16x8, __builtin_shufflevector(p0, p1, 0, 1, 2, 3, 4, 5, 6, 7));
      const bf16x8 vf = __builtin_bit_cast(bf16x8, __builtin_shufflevector(vp, vc, 0, 1, 2, 3, 4, 5, 6, 7));
      st8(ohr, od + rt * NKS * 1024, pack4(mfma32(vf, pf, z)));
    };
#pragma unroll
    for (int rt = 1; rt < 4; ++rt) attn(K[rt - 1], V[rt - 1], K[rt], V[rt], Q[rt], false, rt);
    WAITL0(); BAR();
    float hsum = 0.f, hsq = 0.f;
    { LAS f32x4* HS = (LAS f32x4*)(lds + SC_HS) + r16 * 4;
#pragma unroll
      for (int i = 0; i < 4; ++i) { const f32x4 t = HS[i]; hsum += t[0] + t[2]; hsq += t[1] + t[3]; } }
    const float hm = hsum * (1.0f / 512.0f), hr = __builtin_amdgcn_rsqf(fmaxf(hsq * (1.0f / 512.0f) - hm * hm, 0.f) + 1e-5f);
    const s16x4 hv = pack4(lnc(acch, (f32x2){hm, hr}, CS[((mi ? 8 : 4) + nj) * 4 + g]));
    if (mi) *(LAS s16x4*)(kvb + r16 * 32 + g * 8) = hv;
    {
      LAS char* fo = lds + SC_FX + (w ^ 1) * 1024;
      const s16x4 o0 = *(LAS s16x4*)(fo + lane * 8), o1 = *(LAS s16x4*)(fo + 512 + lane * 8);
      const int rb = (nj & 1) * 2;
      const f32x4 a0 = unpack4((nj & 1) ? Fv[2] : Fv[0]), a1 = unpack4((nj & 1) ? Fv[3] : Fv[1]), b0 = unpack4(o0), b1 = unpack4(o1);
      const unsigned hd = ohl + ((RT0 + rb) * NKS + 16 + cg) * 1024 + (nj >> 1) * 8;
      st8(ohr, hd, pack4((nj & 1) ? swiglu4(b0, a0) : swiglu4(a0, b0)));
      st8(ohr, hd + NKS * 1024, pack4((nj & 1) ? swiglu4(b1, a1) : swiglu4(a1, b1)));
    }
    WAITL0(); BAR();
    s16x4 Kp, Vp;
    if (mi == 0) { Kp = hv; Vp = __builtin_amdgcn_ds_read_tr16_b64_v4i16((LAS s16x4*)(kvb + (4 * g + q) * 32 + pp * 8)); }
    else { Kp = *(LAS s16x4*)(kvb + 512 + lane * 8); Vp = *(LAS s16x4*)(kvb + 1024 + lane * 8); }
    attn(Kp, Vp, K[0], V[0], Q[0], RT0 == 0, 0);
  }
  STAMP(6);
}

constexpr int P2D = 5;
constexpr int P2NA = P2D + 1;
constexpr int NR = (NKS + 3) / 4;
DI void gload16(bf16x8& dst, const char* sbase, unsigned voff) { asm volatile("global_load_dwordx4 %0, %1, %2 sc1" : "=v"(dst) : "v"(voff), "s"(sbase) : "memory"); }
template <int N> DI void waitVn() { asm volatile("s_waitcnt vmcnt(%0)" :: "n"(N) : "memory"); }
constexpr int p2cnt(int r) { return r >= NR ? 0 : (r >= 8 ? 4 : 2); }
constexpr int p2after(int r) { return p2cnt(r + 2) + p2cnt(r + 3) + p2cnt(r + 4); }

DI void phase2(const Params& p, LAS char* lds, int rt64, int ct64, unsigned tag) {
  const int tid = threadIdx.x, lane = tid & 63;
  const int w = rfl(tid >> 6), kh = w >> 2, m4 = w & 3;
  const int r16 = lane & 15, g = lane >> 4, q = r16 >> 2, pp = lane & 3;
  const unsigned ldsb = (unsigned)(uintptr_t)lds;
  constexpr int PH = 1; STAMP(0);
  const char* pa = p.ws + WS_OH + ((size_t)(rt64 * 4 + m4) * NKS + kh) * 1024;
  const unsigned va = lane * 16;
  unsigned vb;
  { const int row = 8 * m4 + (lane >> 3), pc = lane & 7; const int c = (pc >> 1) ^ ((row >> 1) & 3);
    vb = row * 1024 + ct64 * 128 + (2 * c + (pc & 1)) * 16; }
  const char* pb2 = p.w2 + (long)(kh - 16) * 32768;
  if (w == 0) {
    gu64* fl = (gu64*)(p.ws + WS_FLAGS) + (rt64 >> 1) * 16 + (lane & 15);
    const unsigned long long want = FMAGIC | tag;
    for (unsigned spins = 0; spins < (1u << 20); ++spins) {
      const unsigned long long v = __hip_atomic_load(fl, RLX_AGENT);
      if (__all(v == want)) break;
      __builtin_amdgcn_s_sleep(1);
    }
  }
  BAR();
  STAMP(1);
  bf16x8 A[P2NA][2];
  auto issue = [&](int r) {
#pragma unroll
    for (int j = 0; j < 2; ++j) {
      const int ke = 4 * r + 2 * j;
      const bool over = (ke + 1 > NKS - 1);
      const long ko = over ? (long)ke - 2 * kh : (long)ke;
      gload16(A[r % P2NA][j], pa + ko * 1024, va);
      if (r >= 8) glds16s(pb2 + ko * 32768, vb, ldsb + bslot(r) + (2 * j) * 4096 + kh * 4096 + m4 * 1024);
    }
  };
#pragma unroll
  for (int r = 0; r < P2D; ++r) issue(r);
  int bl[4];
#pragma unroll
  for (int c = 0; c < 4; ++c) bl[c] = (4 * g + q) * 128 + ((c ^ ((2 * g + (q >> 1)) & 3)) << 5) + pp * 8 + kh * 4096;
  f32x4 acc[4] = {{0, 0, 0, 0}, {0, 0, 0, 0}, {0, 0, 0, 0}, {0, 0, 0, 0}};
  const size_t ooff = (size_t)(rt64 * 64 + 16 * m4 + r16) * 1024 + (ct64 * 64 + 32 * kh + 4 * g) * 2;
  bf16x4 xv0, xv1;
  bf16x8 bf[2][2][4];
  auto readB = [&](int r) {
    LAS char* sb = lds + bslot(r);
#pragma unroll
    for (int j = 0; j < 2; ++j)
#pragma unroll
      for (int c = 0; c < 4; ++c) bf[r & 1][j][c] = ldsB(sb + j * 8192 + bl[c], 2048);
  };
  readB(0);
  waitVn<p2cnt(1) + p2after(0)>();
  STAMP(2);
#pragma unroll
  for (int r = 0; r < NR; ++r) {
    if (r + 1 < NR) {
      if (r + 1 >= 8) {
        const int n = p2after(r);
        if (n == 12) waitVn<12>(); else if (n == 8) waitVn<8>(); else if (n == 4) waitVn<4>(); else if (n == 10) waitVn<10>(); else if (n == 6) waitVn<6>(); else waitVn<0>();
        BAR();
      } else if (r + P2D >= 8 && r + P2D < NR) BAR();
      if (r + P2D < NR) issue(r + P2D);
      readB(r + 1);
    } else { xv0 = *(const bf16x4*)(p.x + ooff); xv1 = *(const bf16x4*)(p.x + ooff + 32); }
    if (r + 1 < 8 && r > 0) {
      const int n = p2cnt(r + 1) + p2after(r) + p2cnt(r + 5);
      if (n == 10) waitVn<10>(); else if (n == 12) waitVn<12>(); else if (n == 14) waitVn<14>(); else if (n == 16) waitVn<16>(); else if (n == 18) waitVn<18>(); else waitVn<0>();
    }
    asm volatile("" : "+v"(A[r % P2NA][0]), "+v"(A[r % P2NA][1]));
    if (r == 8) STAMP(3);
#pragma unroll
    for (int j = 0; j < 2; ++j) {
      if (4 * r + 2 * j + 1 > NKS - 1 && kh) continue;
#pragma unroll
      for (int c = 0; c < 4; ++c) acc[c] = mfma32(bf[r & 1][j][c], A[r % P2NA][j], acc[c]);
    }
  }
  STAMP(4);
  {
    LAS f32x4* red = (LAS f32x4*)(lds + SC_RED);
    red[(w * 2 + 0) * 64 + lane] = kh ? acc[0] : acc[2];
    red[(w * 2 + 1) * 64 + lane] = kh ? acc[1] : acc[3];
    WAITL0(); BAR();
    const f32x4 o0 = red[((w ^ 4) * 2 + 0) * 64 + lane], o1 = red[((w ^ 4) * 2 + 1) * 64 + lane];
    const f32x4 s0 = (kh ? acc[2] : acc[0]) + o0, s1 = (kh ? acc[3] : acc[1]) + o1;
    const f32x4 x0 = __builtin_convertvector(xv0, f32x4), x1 = __builtin_convertvector(xv1, f32x4);
    *(s16x4*)(p.out + ooff) = pack4(s0 + x0);
    *(s16x4*)(p.out + ooff + 32) = pack4(s1 + x1);
  }
  STAMP(5);
}

__global__ __launch_bounds__(512, 2) void k_fused(Params p) {
  extern __shared__ __attribute__((aligned(16))) char shm[];
  LAS char* lds = (LAS char*)shm;
  const int b = blockIdx.x, xcd = b & 7, idx = b >> 3;
  const int rg = (xcd & 1) * 8 + (idx & 7), cg = (xcd >> 1) * 4 + (idx >> 3);
  const int rt64 = xcd * 4 + (idx & 3), ct64 = idx >> 2;
  const unsigned tag = __hip_atomic_load((gu32*)p.ws, RLX_AGENT) + 1u;
  if (cg < 8) phase1<true>(p, lds, rg, cg, ct64); else phase1<false>(p, lds, rg, cg - 8, ct64);
  asm volatile("s_waitcnt vmcnt(0)" ::: "memory");
  BAR();
  if (threadIdx.x == 0) __hip_atomic_store((gu64*)(p.ws + WS_FLAGS) + rg * 16 + cg, FMAGIC | tag, RLX_AGENT);
  { constexpr int PH = 0; STAMP(7); }
  phase2(p, lds, rt64, ct64, tag);
  if (b == 0 && threadIdx.x < 64) {
    gu64* fl = (gu64*)(p.ws + WS_FLAGS) + threadIdx.x;
    const unsigned long long want = FMAGIC | tag;
    for (unsigned spins = 0; spins < (1u << 20); ++spins) {
      const bool ok = __hip_atomic_load(fl, RLX_AGENT) == want && __hip_atomic_load(fl + 64, RLX_AGENT) == want &&
                      __hip_atomic_load(fl + 128, RLX_AGENT) == want && __hip_atomic_load(fl + 192, RLX_AGENT) == want;
      if (__all(ok)) break;
      __builtin_amdgcn_s_sleep(1);
    }
    if (threadIdx.x == 0) __hip_atomic_store((gu32*)p.ws, tag, RLX_AGENT);
  }
  { constexpr int PH = 1; STAMP(6); STAMP(7); }
}

extern "C" void kernel_launch(void* const* d_in, const int* in_sizes, int n_in, void* d_out, int out_size, void* d_ws, size_t ws_size,
                              hipStream_t stream) {
  Params p;
  p.x = (const char*)d_in[0]; p.wqkv = (const char*)d_in[1]; p.wo = (const char*)d_in[2];
  p.w1 = (const char*)d_in[4]; p.w3 = (const char*)d_in[5]; p.w2 = (const char*)d_in[6];
  p.ws = (char*)d_ws; p.out = (char*)d_out; p.stamps = (unsigned long long*)((char*)d_ws + (128u << 20));
  (void)hipFuncSetAttribute((const void*)k_fused, hipFuncAttributeMaxDynamicSharedMemorySize, P1_LDS);
  k_fused<<<256, 512, P1_LDS, stream>>>(p);
}
```

```cpp
#include <hip/hip_runtime.h>
#include <stdint.h>

#define LAS __attribute__((address_space(3)))
#define DI __device__ __forceinline__
typedef __bf16 bf16x8 __attribute__((ext_vector_type(8)));
typedef __bf16 bf16x4 __attribute__((ext_vector_type(4)));
typedef short s16x4 __attribute__((ext_vector_type(4)));
typedef float f32x4 __attribute__((ext_vector_type(4)));
typedef float f32x2 __attribute__((ext_vector_type(2)));
typedef unsigned u32x2 __attribute__((ext_vector_type(2)));
typedef unsigned u32x4 __attribute__((ext_vector_type(4)));

constexpr int NKS = 59;
constexpr int SLOT = 53248;
constexpr int WOFF = 16384, EOFF = 49152;
constexpr int DUMMY = 3 * SLOT;
constexpr int SC_CS = DUMMY + 1024;
constexpr int SC_MR = SC_CS + 1152;
constexpr int P1_LDS = 163840;
constexpr int SC_HS = 86016;
constexpr int SC_KV = SC_HS + 1024;
constexpr int SC_FX = SC_KV + 6144;
constexpr int SC_RED = 86016;
__host__ __device__ constexpr int bslot(int r) { return (r & 7) < 3 ? 2 * SLOT + (r & 7) * 16384 : (r & 7) < 6 ? ((r & 7) - 3) * 16384 : SLOT + ((r & 7) - 6) * 16384; }
constexpr unsigned long long FMAGIC = 0x5AC3E17Bull << 32;
constexpr size_t WS_FLAGS = 4096, WS_STATS = 8192, WS_OH = 65536;

#define STAMP(i) do {} while (0)

template <int V> struct IC { static constexpr int value = V; };
template <int N, int I = 0, class F> DI void static_for(F& f) { if constexpr (I < N) { f(IC<I>{}); static_for<N, I + 1>(f); } }
DI int rfl(int v) { return __builtin_amdgcn_readfirstlane(v); }
DI void glds16(const void* g, unsigned lds_dst) {
  unsigned keep;
  asm volatile("s_mov_b32 %0, m0\n\ts_mov_b32 m0, %2\n\ts_nop 0\n\tglobal_load_lds_dwordx4 %1, off\n\ts_mov_b32 m0, %0"
               : "=&s"(keep) : "v"(g), "s"(lds_dst) : "memory");
}
DI void glds16s(const char* sbase, unsigned voff, unsigned lds_dst) {
  asm volatile("s_mov_b32 m0, %2\n\ts_nop 0\n\tglobal_load_lds_dwordx4 %1, %0" :: "s"(sbase), "v"(voff), "s"(lds_dst) : "memory", "m0");
}
#define WAITV(n) asm volatile("s_waitcnt vmcnt(" #n ")" ::: "memory")
#define WAITL0() do { asm volatile("" ::: "memory"); __builtin_amdgcn_s_waitcnt(0xC07F); asm volatile("" ::: "memory"); } while (0)
#define BAR() do { asm volatile("" ::: "memory"); __builtin_amdgcn_s_barrier(); asm volatile("" ::: "memory"); } while (0)
#define SB0() __builtin_amdgcn_sched_barrier(0)

DI bf16x8 ldsA(LAS char* p) { return *(LAS bf16x8*)p; }
DI bf16x8 ldsB(LAS char* p, int toff) {
  s16x4 lo = __builtin_amdgcn_ds_read_tr16_b64_v4i16((LAS s16x4*)p);
  s16x4 hi = __builtin_amdgcn_ds_read_tr16_b64_v4i16((LAS s16x4*)(p + toff));
  return __builtin_bit_cast(bf16x8, __builtin_shufflevector(lo, hi, 0, 1, 2, 3, 4, 5, 6, 7));
}
DI f32x4 mfma32(bf16x8 a, bf16x8 b, f32x4 c) { return __builtin_amdgcn_mfma_f32_16x16x32_bf16(a, b, c, 0, 0, 0); }
DI f32x4 mfma16(s16x4 a, s16x4 b, f32x4 c) { return __builtin_amdgcn_mfma_f32_16x16x16bf16_1k(a, b, c, 0, 0, 0); }
DI s16x4 pack4(f32x4 v) { return __builtin_bit_cast(s16x4, __builtin_convertvector(v, bf16x4)); }
DI f32x4 unpack4(s16x4 v) { return __builtin_convertvector(__builtin_bit_cast(bf16x4, v), f32x4); }
DI float xg_sum(float v) {
  auto r = __builtin_amdgcn_permlane16_swap(__float_as_uint(v), __float_as_uint(v), false, false);
  v = __uint_as_float(r[0]) + __uint_as_float(r[1]);
  auto s = __builtin_amdgcn_permlane32_swap(__float_as_uint(v), __float_as_uint(v), false, false);
  return __uint_as_float(s[0]) + __uint_as_float(s[1]);
}
DI float xg_max(float v) {
  auto r = __builtin_amdgcn_permlane16_swap(__float_as_uint(v), __float_as_uint(v), false, false);
  v = fmaxf(__uint_as_float(r[0]), __uint_as_float(r[1]));
  auto s = __builtin_amdgcn_permlane32_swap(__float_as_uint(v), __float_as_uint(v), false, false);
  return fmaxf(__uint_as_float(s[0]), __uint_as_float(s[1]));
}
DI float row_sum16(float v) {
  v += __builtin_bit_cast(float, __builtin_amdgcn_update_dpp(0, __builtin_bit_cast(int, v), 0x128, 0xf, 0xf, false));
  v += __builtin_bit_cast(float, __builtin_amdgcn_update_dpp(0, __builtin_bit_cast(int, v), 0x124, 0xf, 0xf, false));
  v += __builtin_bit_cast(float, __builtin_amdgcn_update_dpp(0, __builtin_bit_cast(int, v), 0x122, 0xf, 0xf, false));
  v += __builtin_bit_cast(float, __builtin_amdgcn_update_dpp(0, __builtin_bit_cast(int, v), 0x121, 0xf, 0xf, false));
  return v;
}
DI f32x4 lnc(f32x4 a, f32x2 mr, f32x4 c) { return (a - c * mr[0]) * mr[1]; }
DI f32x4 swiglu4(f32x4 g, f32x4 u) {
  const f32x4 t = g * -1.44269504089f;
  f32x4 d;
#pragma unroll
  for (int r = 0; r < 4; ++r) d[r] = __builtin_amdgcn_rcpf(1.0f + __builtin_amdgcn_exp2f(t[r]));
  return g * u * d;
}

struct Params {
  const char* x; const char* wqkv; const char* wo; const char* w1; const char* w3; const char* w2;
  char* ws; char* out; unsigned long long* stamps;
};
typedef __attribute__((address_space(1))) unsigned gu32;
typedef __attribute__((address_space(1))) unsigned long long gu64;
#define RLX_AGENT __ATOMIC_RELAXED, __HIP_MEMORY_SCOPE_AGENT
DI void st16(__amdgpu_buffer_rsrc_t r, unsigned off, u32x4 v) { __builtin_amdgcn_raw_buffer_store_b128(v, r, off, 0, 16); }
DI void st8(__amdgpu_buffer_rsrc_t r, unsigned off, s16x4 v) { __builtin_amdgcn_raw_buffer_store_b64(__builtin_bit_cast(u32x2, v), r, off, 0, 16); }

struct Frags { bf16x8 a[4], ah, b[4], bx, be, e0; };

template <bool TA>
DI unsigned phase1(const Params& p, LAS char* lds, int rg, int cg, int ct64, int idx) {
  const int tid = threadIdx.x, lane = tid & 63;
  const int w = rfl(tid >> 6), mi = w >> 2, nj = w & 3;
  const int r16 = lane & 15, g = lane >> 4, q = r16 >> 2, pp = lane & 3;
  constexpr int PH = 0; STAMP(0);
  const unsigned ldsb = (unsigned)(uintptr_t)lds;
  const int row0 = rg * 128;
  const int srow = row0 + (TA ? cg : 8 + cg) * 8 + w;
  unsigned ev; u32x4 xrow;
  asm volatile("global_load_dword %0, %1, %2 sc1" : "=v"(ev) : "v"(0u), "s"(p.ws) : "memory");
  asm volatile("global_load_dwordx4 %0, %1, %2" : "=v"(xrow) : "v"((unsigned)(srow * 1024 + lane * 16)), "s"(p.x) : "memory");
  const int tm = TA ? 2 * cg : 16 + 18 * (cg >> 1) + ((cg & 1) ? 10 : 0);
  const int te = 16 + 18 * (cg >> 1) + 8 + (cg & 1);
  unsigned vx[2], vw[4], ve;
#pragma unroll
  for (int m = 0; m < 2; ++m) {
    const int row = 16 * w + 8 * m + (lane >> 3);
    vx[m] = (unsigned)((row0 + row) * 1024 + (((lane & 7) ^ ((row >> 1) & 7)) << 4));
  }
  const char* bw[4]; unsigned ldw[4], dw[4];
#pragma unroll
  for (int m = 0; m < 4; ++m) {
    if (!TA || m < 2) {
      const int pi = TA ? (2 * w + m) : (2 * w + (m & 1));
      const int kr = 4 * pi + (lane >> 4), pc = lane & 15;
      const int c = (pc >> 1) ^ ((kr & 3) | (((kr >> 3) & 1) << 2));
      if (TA) { bw[m] = p.wqkv; ldw[m] = 3072; vw[m] = kr * 3072 + ((c >> 2) * 512 + (4 * cg + (c & 3)) * 16) * 2 + (pc & 1) * 16; dw[m] = WOFF + pi * 1024; }
      else { bw[m] = (m >> 1) ? p.w3 : p.w1; ldw[m] = 2752; int t = tm + c; t = t > 85 ? 85 : t; vw[m] = kr * 2752 + t * 32 + (pc & 1) * 16; dw[m] = WOFF + (m >> 1) * 16384 + pi * 1024; }
    } else if (m == 2) {
      const int kr = 8 * w + (lane >> 3), pc = lane & 7;
      const int c = (pc >> 1) ^ (((kr >> 1) & 1) | (((kr >> 3) & 1) << 1));
      bw[m] = p.wqkv; ldw[m] = 3072; vw[m] = kr * 3072 + (1024 + (4 * cg + c) * 16) * 2 + (pc & 1) * 16; dw[m] = WOFF + 16384 + w * 1024;
    } else {
      const int kr = 16 * (w & 3) + (lane >> 2), pc = lane & 3;
      const int e = (pc >> 1) ^ ((kr >> 3) & 1);
      bw[m] = mi ? p.w3 : p.w1; ldw[m] = 2752; vw[m] = kr * 2752 + (tm + e) * 32 + (pc & 1) * 16; dw[m] = WOFF + 24576 + mi * 4096 + (w & 3) * 1024;
    }
  }
  const bool has_e = TA ? (w < 2) : (w < 4);
  const char* be_; unsigned lde, de;
  if (TA) {
    const int row = 8 * (w & 1) + (lane >> 3);
    int gr = row0 - 16 + row; gr = gr < 0 ? 0 : gr;
    be_ = p.x; lde = 2; ve = gr * 1024 + (((lane & 7) ^ ((row >> 1) & 7)) << 4); de = EOFF + w * 1024;
  } else {
    const int pos = 32 * (w & 1) + (lane >> 1), kr = pos ^ (((pos >> 3) & 1) << 2);
    be_ = (w & 2) ? p.w3 : p.w1; lde = 2752; ve = kr * 2752 + te * 32 + (lane & 1) * 16; de = EOFF + (w & 3) * 1024;
  }
  if (!has_e) { be_ = p.x; lde = 0; ve = 0; }
  unsigned vb;
  { const int row = 8 * nj + (lane >> 3), pc = lane & 7; const int c = (pc >> 1) ^ ((row >> 1) & 3);
    vb = row * 1024 + ct64 * 128 + (2 * c + (pc & 1)) * 16; }
  const unsigned emask = has_e ? 0xffffffffu : 0u;
  if (!has_e) de = DUMMY;
  const char* sx = p.x; const char* se = be_; const char* sw[4] = {bw[0], bw[1], bw[2], bw[3]};
  auto dma = [&](int i, int so) {
    if (i == 0) { glds16s(se, ve, ldsb + (so & emask) + de); se += 64u * lde; }
    else if (i <= 2) { glds16s(sx, vx[i - 1], ldsb + so + (2 * w + i - 1) * 1024); if (i == 2) sx += 128; }
    else { glds16s(sw[i - 3], vw[i - 3], ldsb + so + dw[i - 3]); sw[i - 3] += 64u * ldw[i - 3]; }
  };
  auto dmab = [&](int rr, int j) {
    const int ks2 = 4 * rr + 2 * j + mi;
    const char* src = (4 * rr + 2 * j < 16) ? p.wo + ks2 * 32768 : p.w2 + (ks2 - 16) * 32768;
    glds16s(src, vb, ldsb + bslot(rr) + (2 * j + mi) * 4096 + nj * 1024);
  };
#pragma unroll
  for (int i = 0; i < 7; ++i) dma(i, 0);
  {
    const int slot = idx * 8 + w;
    const char* a = p.w2 + (size_t)512 * 1024 + (size_t)(27 * slot + (lane < 27 ? lane : 0)) * 128;
    if (lane == 29) a = p.out + (size_t)((blockIdx.x & 7) * 256 + 32 * w) * 1024;
    if (lane == 30) a = p.wo + (size_t)w * 65536;
    asm volatile("s_mov_b32 m0, %1\n\ts_nop 0\n\tglobal_load_lds_dword %0, off" :: "v"(a), "s"(ldsb + DUMMY) : "memory", "m0");
  }
#pragma unroll
  for (int i = 0; i < 7; ++i) dma(i, SLOT);
  STAMP(1);
  asm volatile("s_waitcnt vmcnt(15)" : "+v"(ev), "+v"(xrow) :: "memory");
  const unsigned tag = (unsigned)rfl((int)ev) + 1u;
  {
    float s1 = 0.f, s2 = 0.f;
#pragma unroll
    for (int i = 0; i < 4; ++i) {
      const float lo = __uint_as_float(xrow[i] << 16), hi = __uint_as_float(xrow[i] & 0xffff0000u);
      s1 += lo + hi; s2 = fmaf(lo, lo, fmaf(hi, hi, s2));
    }
    s1 = xg_sum(row_sum16(s1)); s2 = xg_sum(row_sum16(s2));
    const float m = s1 * (1.0f / 512.0f), var = fmaxf(s2 * (1.0f / 512.0f) - m * m, 0.f);
    const float rs = __builtin_amdgcn_rsqf(var + 1e-5f);
    const __amdgpu_buffer_rsrc_t str = __builtin_amdgcn_make_buffer_rsrc(p.ws + WS_STATS, 0, 2048 * 16, 0x00020000);
    if (lane == 0) st16(str, srow * 16, (u32x4){__float_as_uint(m), tag, __float_as_uint(rs), tag});
  }
  const int xa0 = r16 * 128 + ((g ^ (r16 >> 1)) << 4);
  const int fl = q | ((g & 1) << 2);
  const int w256 = (8 * g + q) * 256 + (fl << 5) + pp * 8;
  const int w128 = (8 * g + q) * 128 + ((((q >> 1) & 1) | ((g & 1) << 1)) << 5) + pp * 8;
  const int w64 = (8 * g + q) * 64 + ((g & 1) << 5) + pp * 8;
  const int w32 = (8 * g + ((g & 1) << 2) + q) * 32 + pp * 8;
  const int dt32 = (g & 1) ? -128 : 128;
  const int tb0 = (w256 ^ (nj << 6)) + (mi ? 16384 : 0), tb2 = (w256 ^ (nj << 6)) + (mi ? 0 : 16384);
  const int tbx = EOFF + (nj & 1) * 2048 + w32;
  const int taq = w256 ^ (nj << 5), tak = w256 ^ ((4 + nj) << 5), tav = 16384 + (w128 ^ (nj << 5)), taf = 24576 + (nj & 1) * 4096 + (w64 ^ ((nj >> 1) << 5));
  const int fu = 4 + ((nj >> 1) | ((nj & 1) << 1));
  int xr[4];
#pragma unroll
  for (int i = 0; i < 4; ++i) xr[i] = mi * 8192 + (TA ? i : ((i + nj) & 3)) * 2048;
  const int bea = mi ? tav : tak, bes = mi ? 4096 : 8192, bet = mi ? 512 : 1024;
  const int e0a = mi ? taf : taq, e0s = mi ? 2048 : 8192, e0t = mi ? 256 : 1024;
  constexpr int NRD = TA ? 11 : 9;
  auto ldsB2 = [&](LAS char* p0, LAS char* p1) {
    s16x4 lo = __builtin_amdgcn_ds_read_tr16_b64_v4i16((LAS s16x4*)p0), hi = __builtin_amdgcn_ds_read_tr16_b64_v4i16((LAS s16x4*)p1);
    return __builtin_bit_cast(bf16x8, __builtin_shufflevector(lo, hi, 0, 1, 2, 3, 4, 5, 6, 7));
  };
  auto rdf = [&](Frags& F, int i, int so, int ks) {
    LAS char* sb = lds + so;
    const int xa = xa0 ^ (ks * 64);
    LAS char* wb = sb + WOFF;
    if (TA) {
      switch (i) {
        case 0: F.a[0] = ldsA(sb + xa + xr[0]); break;
        case 1: F.b[0] = ldsB(wb + taq + ks * 8192, 1024); break;
        case 2: F.b[1] = ldsB(wb + tak + ks * 8192, 1024); break;
        case 3: F.b[2] = ldsB(wb + tav + ks * 4096, 512); break;
        case 4: F.b[3] = ldsB(wb + taf + ks * 2048, 256); break;
        case 5: F.a[1] = ldsA(sb + xa + xr[1]); break;
        case 6: F.a[2] = ldsA(sb + xa + xr[2]); break;
        case 7: F.a[3] = ldsA(sb + xa + xr[3]); break;
        case 8: F.be = ldsB2(wb + bea + ks * bes, wb + bea + ks * bes + bet); break;
        case 9: F.ah = ldsA(sb + EOFF + xa); break;
        default: F.e0 = ldsB2(wb + e0a + ks * e0s, wb + e0a + ks * e0s + e0t); break;
      }
    } else {
      switch (i) {
        case 0: F.a[0] = ldsA(sb + xa + xr[0]); break;
        case 1: F.b[0] = ldsB(wb + tb0 + ks * 8192, 1024); break;
        case 2: F.b[1] = ldsB(wb + (tb0 ^ 32) + ks * 8192, 1024); break;
        case 3: F.b[2] = ldsB(wb + tb2 + ks * 8192, 1024); break;
        case 4: F.b[3] = ldsB(wb + (tb2 ^ 32) + ks * 8192, 1024); break;
        case 5: F.a[1] = ldsA(sb + xa + xr[1]); break;
        case 6: F.a[2] = ldsA(sb + xa + xr[2]); break;
        case 7: F.a[3] = ldsA(sb + xa + xr[3]); break;
        default: F.bx = ldsB2(sb + tbx + ks * 1024, sb + tbx + ks * 1024 + dt32); break;
      }
    }
  };
  f32x4 acc[4][4], accx[2], acch = {0, 0, 0, 0}, cs0 = {0, 0, 0, 0}, cs1 = {0, 0, 0, 0}, csx = {0, 0, 0, 0};
  accx[0] = (f32x4){0, 0, 0, 0}; accx[1] = (f32x4){0, 0, 0, 0};
#pragma unroll
  for (int c = 0; c < 4; ++c)
#pragma unroll
    for (int rt = 0; rt < 4; ++rt) acc[c][rt] = (f32x4){0, 0, 0, 0};
  const bf16x8 ones = {(__bf16)1.0f, (__bf16)1.0f, (__bf16)1.0f, (__bf16)1.0f, (__bf16)1.0f, (__bf16)1.0f, (__bf16)1.0f, (__bf16)1.0f};
  auto mm = [&](const Frags& F, int gi) {
    if (gi < 4) {
      acc[0][gi] = mfma32(F.b[0], F.a[gi], acc[0][gi]);
      acc[1][gi] = mfma32(F.b[1], F.a[gi], acc[1][gi]);
      acc[2][gi] = TA ? mfma32(F.a[gi], F.b[2], acc[2][gi]) : mfma32(F.b[2], F.a[gi], acc[2][gi]);
      acc[3][gi] = mfma32(F.b[3], F.a[gi], acc[3][gi]);
    } else if (TA) {
      acch = mfma32(F.be, F.ah, acch);
      cs0 = mfma32(F.e0, ones, cs0); cs1 = mfma32(F.be, ones, cs1);
    } else {
      accx[0] = mfma32(F.bx, F.a[0], accx[0]); accx[1] = mfma32(F.bx, F.a[1], accx[1]);
      cs0 = mfma32(F.b[0], ones, cs0); cs1 = mfma32(F.b[1], ones, cs1); csx = mfma32(F.bx, ones, csx);
    }
  };
  constexpr int RPG = (NRD + 4) / 5;
  auto half = [&](const Frags& Fc, Frags& Fn, int so_n, int ks_n, int mode, int d0, int nd, int so_d, int bt) {
#pragma unroll
    for (int gi = 0; gi < 5; ++gi) {
#pragma unroll
      for (int i = 0; i < RPG; ++i) { const int idx = gi * RPG + i; if (idx < NRD) rdf(Fn, idx, so_n, ks_n); }
      mm(Fc, gi);
#pragma unroll
      for (int k = 0; k < 5; ++k) {
        __builtin_amdgcn_sched_group_barrier(0x8, 1, 0);
        __builtin_amdgcn_sched_group_barrier(0x100, 1, 0);
        __builtin_amdgcn_sched_group_barrier(0x2, 2, 0);
      }
      __builtin_amdgcn_sched_group_barrier(0x100, 8, 0);
      SB0();
      if (mode == 0 && gi < nd) { dma(d0 + gi, so_d); SB0(); }
      if (mode == 1 && gi < nd && d0 + gi < 6) { dmab(3 * bt + ((d0 + gi) >> 1), (d0 + gi) & 1); SB0(); }
    }
  };
  Frags F0, F1;
  WAITV(9); BAR();
  STAMP(2);
#pragma unroll
  for (int i = 0; i < NRD; ++i) rdf(F0, i, 0, 0);
#pragma unroll
  for (int i = 0; i < 3; ++i) dma(i, 2 * SLOT);
  half(F0, F1, 0, 1, 0, 3, 4, 2 * SLOT, 0);
  int sop = 0, so = SLOT;
#pragma unroll 1
  for (int t = 1; t < 6; ++t) {
    WAITV(7); WAITL0(); BAR();
    half(F1, F0, so, 0, 0, 0, 4, sop, 0);
    half(F0, F1, so, 1, 0, 4, 3, sop, 0);
    sop = so; so = (so == 2 * SLOT) ? 0 : so + SLOT;
  }
  WAITV(7); WAITL0(); BAR();
  u32x4 stg;
  { int r = (w < 2) ? row0 + 64 * w + lane : (w == 2 ? row0 - 16 + (lane & 15) : row0 + lane); r = r < 0 ? 0 : r;
    asm volatile("global_load_dwordx4 %0, %1, %2 sc1" : "=v"(stg) : "v"((unsigned)(r * 16)), "s"(p.ws + WS_STATS) : "memory"); }
  half(F1, F0, 0, 0, 1, 0, 4, 0, 0);
  half(F0, F1, 0, 1, 1, 4, 3, 0, 0);
  WAITV(7); WAITL0(); BAR();
  half(F1, F0, SLOT, 0, 1, 0, 4, 0, 1);
  half(F0, F1, SLOT, 1, 1, 4, 3, 0, 1);
#pragma unroll
  for (int gi = 0; gi < 5; ++gi) mm(F1, gi);
  STAMP(4);
  {
    LAS f32x4* CSw = (LAS f32x4*)(lds + SC_CS);
    if (r16 == 0) {
      const int u0 = TA ? (mi ? 8 + fu : nj) : (mi * 8 + 2 * nj), u1 = TA ? (mi ? 8 + nj : 4 + nj) : (mi * 8 + 2 * nj + 1);
      CSw[u0 * 4 + g] = cs0; CSw[u1 * 4 + g] = cs1;
    }
    asm volatile("s_waitcnt vmcnt(12)" : "+v"(stg) :: "memory");
    if (w < 3) {
      int r = (w < 2) ? row0 + 64 * w + lane : row0 - 16 + (lane & 15); r = r < 0 ? 0 : r;
      if (!__all(stg[1] == tag && stg[3] == tag)) {
        gu64* gp = (gu64*)(p.ws + WS_STATS) + r * 2;
        for (unsigned spins = 0; spins < (1u << 20); ++spins) {
          const unsigned long long a = __hip_atomic_load(gp, RLX_AGENT), b = __hip_atomic_load(gp + 1, RLX_AGENT);
          stg = (u32x4){(unsigned)a, (unsigned)(a >> 32), (unsigned)b, (unsigned)(b >> 32)};
          if (__all(stg[1] == tag && stg[3] == tag)) break;
          __builtin_amdgcn_s_sleep(1);
        }
      }
      if (w < 2 || lane < 16) ((LAS f32x2*)(lds + SC_MR))[64 * w + lane] = (f32x2){__uint_as_float(stg[0]), __uint_as_float(stg[2])};
    }
  }
  WAITL0(); BAR();
  dmab(6, 0); dmab(6, 1); dmab(7, 0); dmab(7, 1);
  STAMP(5);
  LAS f32x4* CS = (LAS f32x4*)(lds + SC_CS);
  LAS f32x2* MR = (LAS f32x2*)(lds + SC_MR);
  const __amdgpu_buffer_rsrc_t ohr = __builtin_amdgcn_make_buffer_rsrc(p.ws + WS_OH, 0, 128 * NKS * 1024, 0x00020000);
  const unsigned ohl = lane * 16;
  if (!TA) {
    const f32x4 c0 = CS[(2 * nj) * 4 + g], c1 = CS[(2 * nj + 1) * 4 + g], c2 = CS[(8 + 2 * nj) * 4 + g], c3 = CS[(9 + 2 * nj) * 4 + g];
    const int tp = tm + 2 * nj;
    LAS char* fx = lds + SC_FX;
    { const f32x2 m0 = MR[64 * mi + 16 * nj + r16], m1 = MR[64 * mi + 16 * ((nj + 1) & 3) + r16];
      *(LAS s16x4*)(fx + ((mi * 2 + (nj & 1)) * 4 + nj) * 512 + lane * 8) = pack4(lnc(accx[0], m0, csx));
      *(LAS s16x4*)(fx + ((mi * 2 + (nj & 1)) * 4 + ((nj + 1) & 3)) * 512 + lane * 8) = pack4(lnc(accx[1], m1, csx)); }
    auto ffn = [&](const f32x4 (&G0)[4], const f32x4 (&U0)[4], const f32x4 (&G1)[4], const f32x4 (&U1)[4]) {
#pragma unroll
      for (int i = 0; i < 4; ++i) {
        const int rt = (i + nj) & 3;
        const f32x2 mr = MR[64 * mi + 16 * rt + r16];
        const u32x2 h0 = __builtin_bit_cast(u32x2, pack4(swiglu4(lnc(G0[i], mr, c0), lnc(U0[i], mr, c2))));
        const u32x2 h1 = __builtin_bit_cast(u32x2, pack4(swiglu4(lnc(G1[i], mr, c1), lnc(U1[i], mr, c3))));
        if (tp <= 85) st16(ohr, ohl + ((rg * 8 + 4 * mi + rt) * NKS + 16 + (tp >> 1)) * 1024, (u32x4){h0[0], h0[1], h1[0], h1[1]});
      }
    };
    if (mi) ffn(acc[2], acc[0], acc[3], acc[1]); else ffn(acc[0], acc[2], acc[1], acc[3]);
    WAITL0(); BAR();
    const f32x4 gx = unpack4(*(LAS s16x4*)(fx + ((mi * 2 + 0) * 4 + nj) * 512 + lane * 8)), ux = unpack4(*(LAS s16x4*)(fx + ((mi * 2 + 1) * 4 + nj) * 512 + lane * 8));
    st8(ohr, ohl + ((rg * 8 + w) * NKS + 16 + (te >> 1)) * 1024 + (te & 1) * 8, pack4(swiglu4(gx, ux)));
  } else {
    s16x4 Q[4], K[4], V[4]; u32x2 Fv[4];
    const f32x4 cq = CS[nj * 4 + g], ck = CS[(4 + nj) * 4 + g], cf = CS[(8 + fu) * 4 + g];
    const float cv = ((LAS float*)CS)[(8 + nj) * 16 + r16];
#pragma unroll
    for (int rt = 0; rt < 4; ++rt) {
      const f32x2 mr = MR[64 * mi + 16 * rt + r16];
      Q[rt] = pack4(lnc(acc[0][rt], mr, cq)); K[rt] = pack4(lnc(acc[1][rt], mr, ck)); Fv[rt] = __builtin_bit_cast(u32x2, pack4(lnc(acc[3][rt], mr, cf)));
      const f32x4 t01 = *(LAS f32x4*)(MR + 64 * mi + 16 * rt + 4 * g), t23 = *(LAS f32x4*)(MR + 64 * mi + 16 * rt + 4 * g + 2);
      const f32x4 mm4 = {t01[0], t01[2], t23[0], t23[2]}, rr4 = {t01[1], t01[3], t23[1], t23[3]};
      V[rt] = pack4((acc[2][rt] - mm4 * cv) * rr4);
    }
    LAS char* kvb = lds + SC_KV + nj * 1536;
    LAS char* fx = lds + SC_FX + w * 1024;
    const s16x4 hv = pack4(lnc(acch, MR[128 + r16], CS[((mi ? 8 : 4) + nj) * 4 + g]));
    if (mi == 0) { *(LAS s16x4*)(kvb + 512 + lane * 8) = K[3]; *(LAS s16x4*)(kvb + 1024 + lane * 8) = V[3]; }
    else *(LAS s16x4*)(kvb + r16 * 32 + g * 8) = hv;
    *(LAS u32x2*)(fx + lane * 8) = (nj & 1) ? Fv[0] : Fv[2]; *(LAS u32x2*)(fx + 512 + lane * 8) = (nj & 1) ? Fv[1] : Fv[3];
    f32x4 mkp, mkc;
#pragma unroll
    for (int r = 0; r < 4; ++r) {
      const int dp = r16 + 16 - (4 * g + r), dc = r16 - (4 * g + r);
      mkp[r] = (!(dp & 1) && dp <= 14) ? 0.f : -__builtin_inff();
      mkc[r] = (!(dc & 1) && dc >= 0 && dc <= 14) ? 0.f : -__builtin_inff();
    }
    const bool odd = r16 & 1;
    const unsigned psh = odd ? 16u : 0u;
    const int RT0 = rg * 8 + 4 * mi;
    const unsigned od = ohl + (RT0 * NKS + 2 * cg + (nj >> 1)) * 1024 + (nj & 1) * 8;
    const s16x4 ones4 = {0x3f80, 0x3f80, 0x3f80, 0x3f80};
    const bf16x8 ones8 = __builtin_bit_cast(bf16x8, __builtin_shufflevector(ones4, ones4, 0, 1, 2, 3, 4, 5, 6, 7));
    auto attn = [&](s16x4 kp, s16x4 vp, s16x4 kc, s16x4 vc, s16x4 qq, f32x4 mp, int rt) {
      const f32x4 z = {0, 0, 0, 0};
      const f32x4 sp = mfma16(kp, qq, mp), sc = mfma16(kc, qq, mkc);
      const float sc2 = 0.25f * 1.44269504089f;
      const float e0 = odd ? sp[1] : sp[0], e1 = odd ? sp[3] : sp[2], e2 = odd ? sc[1] : sc[0], e3 = odd ? sc[3] : sc[2];
      const float mx = xg_max(fmaxf(fmaxf(e0, e1), fmaxf(e2, e3))) * sc2;
      const float p0 = __builtin_amdgcn_exp2f(fmaf(e0, sc2, -mx)), p1 = __builtin_amdgcn_exp2f(fmaf(e1, sc2, -mx));
      const float p2 = __builtin_amdgcn_exp2f(fmaf(e2, sc2, -mx)), p3 = __builtin_amdgcn_exp2f(fmaf(e3, sc2, -mx));
      const u32x2 k01 = __builtin_bit_cast(u32x2, pack4((f32x4){p0, 0.f, p1, 0.f})), k23 = __builtin_bit_cast(u32x2, pack4((f32x4){p2, 0.f, p3, 0.f}));
      const u32x4 pw4 = {k01[0] << psh, k01[1] << psh, k23[0] << psh, k23[1] << psh};
      const bf16x8 pf = __builtin_bit_cast(bf16x8, pw4);
      const bf16x8 vf = __builtin_bit_cast(bf16x8, __builtin_shufflevector(vp, vc, 0, 1, 2, 3, 4, 5, 6, 7));
      const f32x4 o = mfma32(vf, pf, z), sm = mfma32(ones8, pf, z);
      st8(ohr, od + rt * NKS * 1024, pack4(o * __builtin_amdgcn_rcpf(sm[0])));
    };
#pragma unroll
    for (int rt = 1; rt < 4; ++rt) attn(K[rt - 1], V[rt - 1], K[rt], V[rt], Q[rt], mkp, rt);
    WAITL0(); BAR();
    {
      LAS char* fo = lds + SC_FX + (w ^ 1) * 1024;
      const u32x2 o0 = *(LAS u32x2*)(fo + lane * 8), o1 = *(LAS u32x2*)(fo + 512 + lane * 8);
      const int rb = (nj & 1) * 2;
      const u32x2 m0 = (nj & 1) ? Fv[2] : Fv[0], m1 = (nj & 1) ? Fv[3] : Fv[1];
      const u32x2 g0 = (nj & 1) ? o0 : m0, u0 = (nj & 1) ? m0 : o0, g1 = (nj & 1) ? o1 : m1, u1 = (nj & 1) ? m1 : o1;
      const unsigned hd = ohl + ((RT0 + rb) * NKS + 16 + cg) * 1024 + (nj >> 1) * 8;
      st8(ohr, hd, pack4(swiglu4(unpack4(__builtin_bit_cast(s16x4, g0)), unpack4(__builtin_bit_cast(s16x4, u0)))));
      st8(ohr, hd + NKS * 1024, pack4(swiglu4(unpack4(__builtin_bit_cast(s16x4, g1)), unpack4(__builtin_bit_cast(s16x4, u1)))));
    }
    s16x4 Kp, Vp;
    if (mi == 0) { Kp = hv; Vp = __builtin_amdgcn_ds_read_tr16_b64_v4i16((LAS s16x4*)(kvb + (4 * g + q) * 32 + pp * 8)); }
    else { Kp = *(LAS s16x4*)(kvb + 512 + lane * 8); Vp = *(LAS s16x4*)(kvb + 1024 + lane * 8); }
    const float ninf = -__builtin_inff();
    attn(Kp, Vp, K[0], V[0], Q[0], RT0 == 0 ? (f32x4){ninf, ninf, ninf, ninf} : mkp, 0);
  }
  STAMP(6);
  return tag;
}

constexpr int NR = (NKS + 3) / 4;
constexpr int P2D = 5;
DI void gload16(bf16x8& dst, const char* sbase, unsigned voff) { asm volatile("global_load_dwordx4 %0, %1, %2 sc1" : "=v"(dst) : "v"(voff), "s"(sbase) : "memory"); }
template <int N> DI void waitVn() { asm volatile("s_waitcnt vmcnt(%0)" :: "n"(N) : "memory"); }
constexpr int P2A = 6;
constexpr int P2NA = P2A + 1;
constexpr int p2opsA(int q) { return (q + P2A < NR) ? 2 : 0; }
constexpr int p2opsB(int q) { return (q + P2D >= 8 && q + P2D < NR) ? 2 : 0; }
constexpr int p2posA(int r) { if (r < P2A) return 2 * (r + 1); int n = 2 * P2A; for (int q = 0; q < r - P2A; ++q) n += p2opsA(q) + p2opsB(q); return n + 2; }
constexpr int p2posB(int r) { int n = 2 * P2A; for (int q = 0; q < r - P2D; ++q) n += p2opsA(q) + p2opsB(q); return n + p2opsA(r - P2D) + 2; }
constexpr int p2issued(int r) { int n = 2 * P2A; for (int q = 0; q < r; ++q) n += p2opsA(q) + p2opsB(q); return n; }
template <int R> struct P2W {
  static constexpr int need = (R + 1 >= 8 && R + 1 < NR && p2posB(R + 1) > p2posA(R)) ? p2posB(R + 1) : p2posA(R);
  static constexpr int n = p2issued(R) - need;
};

DI void phase2(const Params& p, LAS char* lds, int rt64, int ct64, unsigned tag, bool boss) {
  const int tid = threadIdx.x, lane = tid & 63;
  const int w = rfl(tid >> 6), kh = w >> 2, m4 = w & 3;
  const int r16 = lane & 15, g = lane >> 4, q = r16 >> 2, pp = lane & 3;
  const unsigned ldsb = (unsigned)(uintptr_t)lds;
  constexpr int PH = 1; STAMP(0);
  const char* pa = p.ws + WS_OH + ((size_t)(rt64 * 4 + m4) * NKS + kh) * 1024;
  const unsigned va = lane * 16;
  unsigned vb;
  { const int row = 8 * m4 + (lane >> 3), pc = lane & 7; const int c = (pc >> 1) ^ ((row >> 1) & 3);
    vb = row * 1024 + ct64 * 128 + (2 * c + (pc & 1)) * 16; }
  const char* pb2 = p.w2 + (long)(kh - 16) * 32768;
  if (w == 0) {
    gu64* fl = (gu64*)(p.ws + WS_FLAGS) + (rt64 >> 1) * 16 + (lane & 15);
    const unsigned long long want = FMAGIC | tag;
    for (unsigned spins = 0; spins < (1u << 20); ++spins) {
      const unsigned long long v = __hip_atomic_load(fl, RLX_AGENT);
      if (__all(v == want)) break;
      __builtin_amdgcn_s_sleep(1);
    }
  }
  BAR();
  STAMP(1);
  bf16x8 A[P2NA][2];
  auto issueA = [&](int r) {
#pragma unroll
    for (int j = 0; j < 2; ++j) {
      const int ke = 4 * r + 2 * j;
      const bool over = (ke + 1 > NKS - 1);
      gload16(A[r % P2NA][j], pa + (over ? (long)ke - 2 * kh : (long)ke) * 1024, va);
    }
  };
#pragma unroll
  for (int r = 0; r < P2A; ++r) { issueA(r); if (r + 1 < P2A) BAR(); }
  auto issueB = [&](int r) {
#pragma unroll
    for (int j = 0; j < 2; ++j) {
      const int ke = 4 * r + 2 * j;
      const bool over = (ke + 1 > NKS - 1);
      glds16s(pb2 + (over ? (long)ke - 2 * kh : (long)ke) * 32768, vb, ldsb + bslot(r) + (2 * j) * 4096 + kh * 4096 + m4 * 1024);
    }
  };
  int bl[4];
#pragma unroll
  for (int c = 0; c < 4; ++c) bl[c] = (4 * g + q) * 128 + ((c ^ ((2 * g + (q >> 1)) & 3)) << 5) + pp * 8 + kh * 4096;
  f32x4 acc[4] = {{0, 0, 0, 0}, {0, 0, 0, 0}, {0, 0, 0, 0}, {0, 0, 0, 0}};
  const size_t ooff = (size_t)(rt64 * 64 + 16 * m4 + r16) * 1024 + (ct64 * 64 + 32 * kh + 4 * g) * 2;
  bf16x4 xv0, xv1;
  u32x4 fl0 = {0, 0, 0, 0}, fl1 = {0, 0, 0, 0};
  bf16x8 bf[2][2][4];
  auto readB = [&](int r) {
    LAS char* sb = lds + bslot(r);
#pragma unroll
    for (int j = 0; j < 2; ++j)
#pragma unroll
      for (int c = 0; c < 4; ++c) bf[r & 1][j][c] = ldsB(sb + j * 8192 + bl[c], 2048);
  };
  readB(0);
  auto round = [&](auto RC) {
    constexpr int r = decltype(RC)::value;
    waitVn<P2W<r>::n>();
    asm volatile("" : "+v"(A[r % P2NA][0]), "+v"(A[r % P2NA][1]));
    if (r + 1 < NR && (r + 1 >= 8 || r + P2D >= 8)) BAR();
    if (r + P2A < NR) issueA(r + P2A);
    if (r + P2D >= 8 && r + P2D < NR) issueB(r + P2D);
    if (r + 1 == NR) { xv0 = *(const bf16x4*)(p.x + ooff); xv1 = *(const bf16x4*)(p.x + ooff + 32); }
    if (r == 0) STAMP(2);
    if (r == 8) STAMP(3);
    if (r == 11 && boss) {
      const char* fb = p.ws + WS_FLAGS;
      asm volatile("global_load_dwordx4 %0, %2, %3 sc1\n\tglobal_load_dwordx4 %1, %2, %3 offset:1024 sc1" : "=&v"(fl0), "=&v"(fl1) : "v"(va), "s"(fb) : "memory");
    }
    SB0();
    if (r + 1 < NR) readB(r + 1);
#pragma unroll
    for (int j = 0; j < 2; ++j) {
      if (4 * r + 2 * j + 1 > NKS - 1 && kh) continue;
#pragma unroll
      for (int c = 0; c < 4; ++c) acc[c] = mfma32(bf[r & 1][j][c], A[r % P2NA][j], acc[c]);
    }
#pragma unroll
    for (int k = 0; k < 8; ++k) {
      __builtin_amdgcn_sched_group_barrier(0x8, 1, 0);
      __builtin_amdgcn_sched_group_barrier(0x100, 2, 0);
    }
    SB0();
  };
  static_for<NR>(round);
  STAMP(4);
  {
    LAS f32x4* red = (LAS f32x4*)(lds + SC_RED);
    red[(w * 2 + 0) * 64 + lane] = kh ? acc[0] : acc[2];
    red[(w * 2 + 1) * 64 + lane] = kh ? acc[1] : acc[3];
    WAITL0(); BAR();
    const f32x4 o0 = red[((w ^ 4) * 2 + 0) * 64 + lane], o1 = red[((w ^ 4) * 2 + 1) * 64 + lane];
    const f32x4 s0 = (kh ? acc[2] : acc[0]) + o0, s1 = (kh ? acc[3] : acc[1]) + o1;
    const f32x4 x0 = __builtin_convertvector(xv0, f32x4), x1 = __builtin_convertvector(xv1, f32x4);
    *(s16x4*)(p.out + ooff) = pack4(s0 + x0);
    *(s16x4*)(p.out + ooff + 32) = pack4(s1 + x1);
  }
  STAMP(5);
  if (boss) {
    asm volatile("s_waitcnt vmcnt(0)" : "+v"(fl0), "+v"(fl1) :: "memory");
    const unsigned hi = (unsigned)(FMAGIC >> 32);
    bool ok = fl0[0] == tag && fl0[1] == hi && fl0[2] == tag && fl0[3] == hi && fl1[0] == tag && fl1[1] == hi && fl1[2] == tag && fl1[3] == hi;
    if (!__all(ok)) {
      gu64* fl = (gu64*)(p.ws + WS_FLAGS) + lane;
      const unsigned long long want = FMAGIC | tag;
      for (unsigned spins = 0; spins < (1u << 20); ++spins) {
        ok = __hip_atomic_load(fl, RLX_AGENT) == want && __hip_atomic_load(fl + 64, RLX_AGENT) == want &&
             __hip_atomic_load(fl + 128, RLX_AGENT) == want && __hip_atomic_load(fl + 192, RLX_AGENT) == want;
        if (__all(ok)) break;
        __builtin_amdgcn_s_sleep(1);
      }
    }
    if (lane == 0) __hip_atomic_store((gu32*)p.ws, tag, RLX_AGENT);
  }
}

__global__ __launch_bounds__(512, 2) void k_fused(Params p) {
  extern __shared__ __attribute__((aligned(16))) char shm[];
  LAS char* lds = (LAS char*)shm;
  const int b = blockIdx.x, xcd = b & 7, idx = b >> 3;
  const int rg = (xcd & 1) * 8 + (idx & 7), cg = (xcd >> 1) * 4 + (idx >> 3);
  const int rt64 = xcd * 4 + (idx & 3), ct64 = idx >> 2;
  const unsigned tag = (cg < 8) ? phase1<true>(p, lds, rg, cg, ct64, idx) : phase1<false>(p, lds, rg, cg - 8, ct64, idx);
  asm volatile("s_waitcnt vmcnt(0)" ::: "memory");
  BAR();
  if (threadIdx.x == 0) __hip_atomic_store((gu64*)(p.ws + WS_FLAGS) + rg * 16 + cg, FMAGIC | tag, RLX_AGENT);
  { constexpr int PH = 0; STAMP(7); }
  phase2(p, lds, rt64, ct64, tag, b == 0 && rfl(threadIdx.x >> 6) == 0);
  { constexpr int PH = 1; STAMP(6); STAMP(7); }
}

extern "C" void kernel_launch(void* const* d_in, const int* in_sizes, int n_in, void* d_out, int out_size, void* d_ws, size_t ws_size,
                              hipStream_t stream) {
  Params p;
  p.x = (const char*)d_in[0]; p.wqkv = (const char*)d_in[1]; p.wo = (const char*)d_in[2];
  p.w1 = (const char*)d_in[4]; p.w3 = (const char*)d_in[5]; p.w2 = (const char*)d_in[6];
  p.ws = (char*)d_ws; p.out = (char*)d_out; p.stamps = (unsigned long long*)((char*)d_ws + (128u << 20));
  (void)hipFuncSetAttribute((const void*)k_fused, hipFuncAttributeMaxDynamicSharedMemorySize, P1_LDS);
  k_fused<<<256, 512, P1_LDS, stream>>>(p);
}
```

```cpp
#include <hip/hip_runtime.h>
#include <stdint.h>

#define LAS __attribute__((address_space(3)))
#define DI __device__ __forceinline__
typedef __bf16 bf16x8 __attribute__((ext_vector_type(8)));
typedef __bf16 bf16x4 __attribute__((ext_vector_type(4)));
typedef short s16x4 __attribute__((ext_vector_type(4)));
typedef float f32x4 __attribute__((ext_vector_type(4)));
typedef float f32x2 __attribute__((ext_vector_type(2)));
typedef unsigned u32x2 __attribute__((ext_vector_type(2)));
typedef unsigned u32x4 __attribute__((ext_vector_type(4)));

constexpr int NKS = 59;
constexpr int SLOT = 53248;
constexpr int WOFF = 16384, EOFF = 49152;
constexpr int DUMMY = 3 * SLOT;
constexpr int SC_CS = DUMMY + 1024;
constexpr int SC_MR = SC_CS + 1152;
constexpr int P1_LDS = 163840;
constexpr int SC_HS = 86016;
constexpr int SC_KV = SC_HS + 1024;
constexpr int SC_FX = SC_KV + 6144;
constexpr int SC_RED = 86016;
__host__ __device__ constexpr int bslot(int r) { return (r & 7) < 3 ? 2 * SLOT + (r & 7) * 16384 : (r & 7) < 6 ? ((r & 7) - 3) * 16384 : SLOT + ((r & 7) - 6) * 16384; }
constexpr unsigned long long FMAGIC = 0x5AC3E17Bull << 32;
constexpr size_t WS_FLAGS = 4096, WS_STATS = 8192, WS_OH = 65536;

#define STAMP(i) do {} while (0)

template <int V> struct IC { static constexpr int value = V; };
template <int N, int I = 0, class F> DI void static_for(F& f) { if constexpr (I < N) { f(IC<I>{}); static_for<N, I + 1>(f); } }
DI int rfl(int v) { return __builtin_amdgcn_readfirstlane(v); }
DI void glds16(const void* g, unsigned lds_dst) {
  unsigned keep;
  asm volatile("s_mov_b32 %0, m0\n\ts_mov_b32 m0, %2\n\ts_nop 0\n\tglobal_load_lds_dwordx4 %1, off\n\ts_mov_b32 m0, %0"
               : "=&s"(keep) : "v"(g), "s"(lds_dst) : "memory");
}
DI void glds16s(const char* sbase, unsigned voff, unsigned lds_dst) {
  asm volatile("s_mov_b32 m0, %2\n\ts_nop 0\n\tglobal_load_lds_dwordx4 %1, %0" :: "s"(sbase), "v"(voff), "s"(lds_dst) : "memory", "m0");
}
#define WAITV(n) asm volatile("s_waitcnt vmcnt(" #n ")" ::: "memory")
#define WAITL0() do { asm volatile("" ::: "memory"); __builtin_amdgcn_s_waitcnt(0xC07F); asm volatile("" ::: "memory"); } while (0)
#define BAR() do { asm volatile("" ::: "memory"); __builtin_amdgcn_s_barrier(); asm volatile("" ::: "memory"); } while (0)
#define SB0() __builtin_amdgcn_sched_barrier(0)

DI bf16x8 ldsA(LAS char* p) { return *(LAS bf16x8*)p; }
DI bf16x8 ldsB(LAS char* p, int toff) {
  s16x4 lo = __builtin_amdgcn_ds_read_tr16_b64_v4i16((LAS s16x4*)p);
  s16x4 hi = __builtin_amdgcn_ds_read_tr16_b64_v4i16((LAS s16x4*)(p + toff));
  return __builtin_bit_cast(bf16x8, __builtin_shufflevector(lo, hi, 0, 1, 2, 3, 4, 5, 6, 7));
}
DI f32x4 mfma32(bf16x8 a, bf16x8 b, f32x4 c) { return __builtin_amdgcn_mfma_f32_16x16x32_bf16(a, b, c, 0, 0, 0); }
DI f32x4 mfma16(s16x4 a, s16x4 b, f32x4 c) { return __builtin_amdgcn_mfma_f32_16x16x16bf16_1k(a, b, c, 0, 0, 0); }
DI s16x4 pack4(f32x4 v) { return __builtin_bit_cast(s16x4, __builtin_convertvector(v, bf16x4)); }
DI f32x4 unpack4(s16x4 v) { return __builtin_convertvector(__builtin_bit_cast(bf16x4, v), f32x4); }
DI float xg_sum(float v) {
  auto r = __builtin_amdgcn_permlane16_swap(__float_as_uint(v), __float_as_uint(v), false, false);
  v = __uint_as_float(r[0]) + __uint_as_float(r[1]);
  auto s = __builtin_amdgcn_permlane32_swap(__float_as_uint(v), __float_as_uint(v), false, false);
  return __uint_as_float(s[0]) + __uint_as_float(s[1]);
}
DI float xg_max(float v) {
  auto r = __builtin_amdgcn_permlane16_swap(__float_as_uint(v), __float_as_uint(v), false, false);
  v = fmaxf(__uint_as_float(r[0]), __uint_as_float(r[1]));
  auto s = __builtin_amdgcn_permlane32_swap(__float_as_uint(v), __float_as_uint(v), false, false);
  return fmaxf(__uint_as_float(s[0]), __uint_as_float(s[1]));
}
DI float row_sum16(float v) {
  v += __builtin_bit_cast(float, __builtin_amdgcn_update_dpp(0, __builtin_bit_cast(int, v), 0x128, 0xf, 0xf, false));
  v += __builtin_bit_cast(float, __builtin_amdgcn_update_dpp(0, __builtin_bit_cast(int, v), 0x124, 0xf, 0xf, false));
  v += __builtin_bit_cast(float, __builtin_amdgcn_update_dpp(0, __builtin_bit_cast(int, v), 0x122, 0xf, 0xf, false));
  v += __builtin_bit_cast(float, __builtin_amdgcn_update_dpp(0, __builtin_bit_cast(int, v), 0x121, 0xf, 0xf, false));
  return v;
}
DI f32x4 lnc(f32x4 a, f32x2 mr, f32x4 c) { return (a - c * mr[0]) * mr[1]; }
DI f32x4 swiglu4(f32x4 g, f32x4 u) {
  const f32x4 t = g * -1.44269504089f;
  f32x4 d;
#pragma unroll
  for (int r = 0; r < 4; ++r) d[r] = __builtin_amdgcn_rcpf(1.0f + __builtin_amdgcn_exp2f(t[r]));
  return g * u * d;
}

struct Params {
  const char* x; const char* wqkv; const char* wo; const char* w1; const char* w3; const char* w2;
  char* ws; char* out; unsigned long long* stamps;
};
typedef __attribute__((address_space(1))) unsigned gu32;
typedef __attribute__((address_space(1))) unsigned long long gu64;
#define RLX_AGENT __ATOMIC_RELAXED, __HIP_MEMORY_SCOPE_AGENT
DI void st16(__amdgpu_buffer_rsrc_t r, unsigned off, u32x4 v) { __builtin_amdgcn_raw_buffer_store_b128(v, r, off, 0, 16); }
DI void st8(__amdgpu_buffer_rsrc_t r, unsigned off, s16x4 v) { __builtin_amdgcn_raw_buffer_store_b64(__builtin_bit_cast(u32x2, v), r, off, 0, 16); }

struct Frags { bf16x8 a[4], ah, b[4], bx, be, e0; };

template <bool TA>
DI unsigned phase1(const Params& p, LAS char* lds, int rg, int cg, int ct64, int idx) {
  const int tid = threadIdx.x, lane = tid & 63;
  const int w = rfl(tid >> 6), mi = w >> 2, nj = w & 3;
  const int r16 = lane & 15, g = lane >> 4, q = r16 >> 2, pp = lane & 3;
  constexpr int PH = 0; STAMP(0);
  const unsigned ldsb = (unsigned)(uintptr_t)lds;
  const int row0 = rg * 128;
  const int srow = row0 + (TA ? cg : 8 + cg) * 8 + w;
  unsigned ev; u32x4 xrow;
  asm volatile("global_load_dword %0, %1, %2 sc1" : "=v"(ev) : "v"(0u), "s"(p.ws) : "memory");
  asm volatile("global_load_dwordx4 %0, %1, %2" : "=v"(xrow) : "v"((unsigned)(srow * 1024 + lane * 16)), "s"(p.x) : "memory");
  const int tm = TA ? 2 * cg : 16 + 18 * (cg >> 1) + ((cg & 1) ? 10 : 0);
  const int te = 16 + 18 * (cg >> 1) + 8 + (cg & 1);
  unsigned vx[2], vw[4], ve;
#pragma unroll
  for (int m = 0; m < 2; ++m) {
    const int row = 16 * w + 8 * m + (lane >> 3);
    vx[m] = (unsigned)((row0 + row) * 1024 + (((lane & 7) ^ ((row >> 1) & 7)) << 4));
  }
  const char* bw[4]; unsigned ldw[4], dw[4];
#pragma unroll
  for (int m = 0; m < 4; ++m) {
    if (!TA || m < 2) {
      const int pi = TA ? (2 * w + m) : (2 * w + (m & 1));
      const int kr = 4 * pi + (lane >> 4), pc = lane & 15;
      const int c = (pc >> 1) ^ ((kr & 3) | (((kr >> 3) & 1) << 2));
      if (TA) { bw[m] = p.wqkv; ldw[m] = 3072; vw[m] = kr * 3072 + ((c >> 2) * 512 + (4 * cg + (c & 3)) * 16) * 2 + (pc & 1) * 16; dw[m] = WOFF + pi * 1024; }
      else { bw[m] = (m >> 1) ? p.w3 : p.w1; ldw[m] = 2752; int t = tm + c; t = t > 85 ? 85 : t; vw[m] = kr * 2752 + t * 32 + (pc & 1) * 16; dw[m] = WOFF + (m >> 1) * 16384 + pi * 1024; }
    } else if (m == 2) {
      const int kr = 8 * w + (lane >> 3), pc = lane & 7;
      const int c = (pc >> 1) ^ (((kr >> 1) & 1) | (((kr >> 3) & 1) << 1));
      bw[m] = p.wqkv; ldw[m] = 3072; vw[m] = kr * 3072 + (1024 + (4 * cg + c) * 16) * 2 + (pc & 1) * 16; dw[m] = WOFF + 16384 + w * 1024;
    } else {
      const int kr = 16 * (w & 3) + (lane >> 2), pc = lane & 3;
      const int e = (pc >> 1) ^ ((kr >> 3) & 1);
      bw[m] = mi ? p.w3 : p.w1; ldw[m] = 2752; vw[m] = kr * 2752 + (tm + e) * 32 + (pc & 1) * 16; dw[m] = WOFF + 24576 + mi * 4096 + (w & 3) * 1024;
    }
  }
  const bool has_e = TA ? (w < 2) : (w < 4);
  const char* be_; unsigned lde, de;
  if (TA) {
    const int row = 8 * (w & 1) + (lane >> 3);
    int gr = row0 - 16 + row; gr = gr < 0 ? 0 : gr;
    be_ = p.x; lde = 2; ve = gr * 1024 + (((lane & 7) ^ ((row >> 1) & 7)) << 4); de = EOFF + w * 1024;
  } else {
    const int pos = 32 * (w & 1) + (lane >> 1), kr = pos ^ (((pos >> 3) & 1) << 2);
    be_ = (w & 2) ? p.w3 : p.w1; lde = 2752; ve = kr * 2752 + te * 32 + (lane & 1) * 16; de = EOFF + (w & 3) * 1024;
  }
  if (!has_e) { be_ = p.x; lde = 0; ve = 0; }
  unsigned vb;
  { const int row = 8 * nj + (lane >> 3), pc = lane & 7; const int c = (pc >> 1) ^ ((row >> 1) & 3);
    vb = row * 1024 + ct64 * 128 + (2 * c + (pc & 1)) * 16; }
  const unsigned emask = has_e ? 0xffffffffu : 0u;
  if (!has_e) de = DUMMY;
  const char* sx = p.x; const char* se = be_; const char* sw[4] = {bw[0], bw[1], bw[2], bw[3]};
  auto dma = [&](int i, int so) {
    if (i == 0) { glds16s(se, ve, ldsb + (so & emask) + de); se += 64u * lde; }
    else if (i <= 2) { glds16s(sx, vx[i - 1], ldsb + so + (2 * w + i - 1) * 1024); if (i == 2) sx += 128; }
    else { glds16s(sw[i - 3], vw[i - 3], ldsb + so + dw[i - 3]); sw[i - 3] += 64u * ldw[i - 3]; }
  };
  auto dmab = [&](int rr, int j) {
    const int ks2 = 4 * rr + 2 * j + mi;
    const char* src = (4 * rr + 2 * j < 16) ? p.wo + ks2 * 32768 : p.w2 + (ks2 - 16) * 32768;
    glds16s(src, vb, ldsb + bslot(rr) + (2 * j + mi) * 4096 + nj * 1024);
  };
#pragma unroll
  for (int i = 0; i < 7; ++i) dma(i, 0);
#pragma unroll
  for (int i = 0; i < 7; ++i) dma(i, SLOT);
  STAMP(1);
  asm volatile("s_waitcnt vmcnt(14)" : "+v"(ev), "+v"(xrow) :: "memory");
  const unsigned tag = (unsigned)rfl((int)ev) + 1u;
  {
    float s1 = 0.f, s2 = 0.f;
#pragma unroll
    for (int i = 0; i < 4; ++i) {
      const float lo = __uint_as_float(xrow[i] << 16), hi = __uint_as_float(xrow[i] & 0xffff0000u);
      s1 += lo + hi; s2 = fmaf(lo, lo, fmaf(hi, hi, s2));
    }
    s1 = xg_sum(row_sum16(s1)); s2 = xg_sum(row_sum16(s2));
    const float m = s1 * (1.0f / 512.0f), var = fmaxf(s2 * (1.0f / 512.0f) - m * m, 0.f);
    const float rs = __builtin_amdgcn_rsqf(var + 1e-5f);
    const __amdgpu_buffer_rsrc_t str = __builtin_amdgcn_make_buffer_rsrc(p.ws + WS_STATS, 0, 2048 * 16, 0x00020000);
    if (lane == 0) st16(str, srow * 16, (u32x4){__float_as_uint(m), tag, __float_as_uint(rs), tag});
  }
  const char* pfp; unsigned pfs; const char* pfw;
  {
    const int l5 = lane & 31;
    const int L = w * 10 + (l5 < 10 ? l5 : 9);
    if (L < 32) { pfp = p.x + (size_t)(row0 + 32 * (idx >> 3) + L) * 1024; pfs = 128; }
    else {
      const int L2 = L - 32, ri = L2 / 6, sg = L2 - 6 * ri;
      const int kr = 8 * (idx & 7) + ri;
      if (TA) { pfp = (sg < 3) ? p.wqkv + (size_t)kr * 3072 + sg * 1024 + cg * 128 : (sg == 3 ? p.w1 : p.w3) + (size_t)kr * 2752 + tm * 32; pfs = (sg < 3) ? 64u * 3072 : 64u * 2752; }
      else { int cb = (tm < te ? tm : te) * 32 + (sg % 3) * 128; cb = cb > 2748 ? 2748 : cb; pfp = (sg < 3 ? p.w1 : p.w3) + (size_t)kr * 2752 + cb; pfs = 64u * 2752; }
    }
    pfw = p.w2 + (size_t)512 * 1024 + (size_t)((idx * 8 + w) * 27 + (lane < 14 ? lane : 13)) * 128;
  }
  auto pf = [&](const char* a) { asm volatile("s_mov_b32 m0, %1\n\ts_nop 0\n\tglobal_load_lds_dword %0, off" :: "v"(a), "s"(ldsb + DUMMY) : "memory", "m0"); };
  pf(pfp + (size_t)((lane < 32 ? 2u : 3u) * pfs));
  const int xa0 = r16 * 128 + ((g ^ (r16 >> 1)) << 4);
  const int fl = q | ((g & 1) << 2);
  const int w256 = (8 * g + q) * 256 + (fl << 5) + pp * 8;
  const int w128 = (8 * g + q) * 128 + ((((q >> 1) & 1) | ((g & 1) << 1)) << 5) + pp * 8;
  const int w64 = (8 * g + q) * 64 + ((g & 1) << 5) + pp * 8;
  const int w32 = (8 * g + ((g & 1) << 2) + q) * 32 + pp * 8;
  const int dt32 = (g & 1) ? -128 : 128;
  const int tb0 = (w256 ^ (nj << 6)) + (mi ? 16384 : 0), tb2 = (w256 ^ (nj << 6)) + (mi ? 0 : 16384);
  const int tbx = EOFF + (nj & 1) * 2048 + w32;
  const int taq = w256 ^ (nj << 5), tak = w256 ^ ((4 + nj) << 5), tav = 16384 + (w128 ^ (nj << 5)), taf = 24576 + (nj & 1) * 4096 + (w64 ^ ((nj >> 1) << 5));
  const int fu = 4 + ((nj >> 1) | ((nj & 1) << 1));
  int xr[4];
#pragma unroll
  for (int i = 0; i < 4; ++i) xr[i] = mi * 8192 + (TA ? i : ((i + nj) & 3)) * 2048;
  const int bea = mi ? tav : tak, bes = mi ? 4096 : 8192, bet = mi ? 512 : 1024;
  const int e0a = mi ? taf : taq, e0s = mi ? 2048 : 8192, e0t = mi ? 256 : 1024;
  constexpr int NRD = TA ? 11 : 9;
  auto ldsB2 = [&](LAS char* p0, LAS char* p1) {
    s16x4 lo = __builtin_amdgcn_ds_read_tr16_b64_v4i16((LAS s16x4*)p0), hi = __builtin_amdgcn_ds_read_tr16_b64_v4i16((LAS s16x4*)p1);
    return __builtin_bit_cast(bf16x8, __builtin_shufflevector(lo, hi, 0, 1, 2, 3, 4, 5, 6, 7));
  };
  auto rdf = [&](Frags& F, int i, int so, int ks) {
    LAS char* sb = lds + so;
    const int xa = xa0 ^ (ks * 64);
    LAS char* wb = sb + WOFF;
    if (TA) {
      switch (i) {
        case 0: F.a[0] = ldsA(sb + xa + xr[0]); break;
        case 1: F.b[0] = ldsB(wb + taq + ks * 8192, 1024); break;
        case 2: F.b[1] = ldsB(wb + tak + ks * 8192, 1024); break;
        case 3: F.b[2] = ldsB(wb + tav + ks * 4096, 512); break;
        case 4: F.b[3] = ldsB(wb + taf + ks * 2048, 256); break;
        case 5: F.a[1] = ldsA(sb + xa + xr[1]); break;
        case 6: F.a[2] = ldsA(sb + xa + xr[2]); break;
        case 7: F.a[3] = ldsA(sb + xa + xr[3]); break;
        case 8: F.be = ldsB2(wb + bea + ks * bes, wb + bea + ks * bes + bet); break;
        case 9: F.ah = ldsA(sb + EOFF + xa); break;
        default: F.e0 = ldsB2(wb + e0a + ks * e0s, wb + e0a + ks * e0s + e0t); break;
      }
    } else {
      switch (i) {
        case 0: F.a[0] = ldsA(sb + xa + xr[0]); break;
        case 1: F.b[0] = ldsB(wb + tb0 + ks * 8192, 1024); break;
        case 2: F.b[1] = ldsB(wb + (tb0 ^ 32) + ks * 8192, 1024); break;
        case 3: F.b[2] = ldsB(wb + tb2 + ks * 8192, 1024); break;
        case 4: F.b[3] = ldsB(wb + (tb2 ^ 32) + ks * 8192, 1024); break;
        case 5: F.a[1] = ldsA(sb + xa + xr[1]); break;
        case 6: F.a[2] = ldsA(sb + xa + xr[2]); break;
        case 7: F.a[3] = ldsA(sb + xa + xr[3]); break;
        default: F.bx = ldsB2(sb + tbx + ks * 1024, sb + tbx + ks * 1024 + dt32); break;
      }
    }
  };
  f32x4 acc[4][4], accx[2], acch = {0, 0, 0, 0}, cs0 = {0, 0, 0, 0}, cs1 = {0, 0, 0, 0}, csx = {0, 0, 0, 0};
  accx[0] = (f32x4){0, 0, 0, 0}; accx[1] = (f32x4){0, 0, 0, 0};
#pragma unroll
  for (int c = 0; c < 4; ++c)
#pragma unroll
    for (int rt = 0; rt < 4; ++rt) acc[c][rt] = (f32x4){0, 0, 0, 0};
  const bf16x8 ones = {(__bf16)1.0f, (__bf16)1.0f, (__bf16)1.0f, (__bf16)1.0f, (__bf16)1.0f, (__bf16)1.0f, (__bf16)1.0f, (__bf16)1.0f};
  auto mm = [&](const Frags& F, int gi) {
    if (gi < 4) {
      acc[0][gi] = mfma32(F.b[0], F.a[gi], acc[0][gi]);
      acc[1][gi] = mfma32(F.b[1], F.a[gi], acc[1][gi]);
      acc[2][gi] = TA ? mfma32(F.a[gi], F.b[2], acc[2][gi]) : mfma32(F.b[2], F.a[gi], acc[2][gi]);
      acc[3][gi] = mfma32(F.b[3], F.a[gi], acc[3][gi]);
    } else if (TA) {
      acch = mfma32(F.be, F.ah, acch);
      cs0 = mfma32(F.e0, ones, cs0); cs1 = mfma32(F.be, ones, cs1);
    } else {
      accx[0] = mfma32(F.bx, F.a[0], accx[0]); accx[1] = mfma32(F.bx, F.a[1], accx[1]);
      cs0 = mfma32(F.b[0], ones, cs0); cs1 = mfma32(F.b[1], ones, cs1); csx = mfma32(F.bx, ones, csx);
    }
  };
  constexpr int RPG = (NRD + 4) / 5;
  auto half = [&](const Frags& Fc, Frags& Fn, int so_n, int ks_n, int mode, int d0, int nd, int so_d, int bt) {
#pragma unroll
    for (int gi = 0; gi < 5; ++gi) {
#pragma unroll
      for (int i = 0; i < RPG; ++i) { const int idx = gi * RPG + i; if (idx < NRD) rdf(Fn, idx, so_n, ks_n); }
      mm(Fc, gi);
#pragma unroll
      for (int k = 0; k < 5; ++k) {
        __builtin_amdgcn_sched_group_barrier(0x8, 1, 0);
        __builtin_amdgcn_sched_group_barrier(0x100, 1, 0);
        __builtin_amdgcn_sched_group_barrier(0x2, 2, 0);
      }
      __builtin_amdgcn_sched_group_barrier(0x100, 8, 0);
      SB0();
      if (mode == 0 && gi < nd) { dma(d0 + gi, so_d); SB0(); }
      if (mode == 1 && gi < nd && d0 + gi < 6) { dmab(3 * bt + ((d0 + gi) >> 1), (d0 + gi) & 1); SB0(); }
    }
  };
  Frags F0, F1;
  WAITV(9); BAR();
  STAMP(2);
#pragma unroll
  for (int i = 0; i < NRD; ++i) rdf(F0, i, 0, 0);
#pragma unroll
  for (int i = 0; i < 3; ++i) dma(i, 2 * SLOT);
  half(F0, F1, 0, 1, 0, 3, 4, 2 * SLOT, 0);
  pf(pfp + (size_t)(4u * pfs));
  int sop = 0, so = SLOT;
#pragma unroll 1
  for (int t = 1; t < 6; ++t) {
    WAITV(9); WAITL0(); BAR();
    half(F1, F0, so, 0, 0, 0, 4, sop, 0);
    half(F0, F1, so, 1, 0, 4, 3, sop, 0);
    pf(t <= 3 ? pfp + (size_t)((unsigned)(t + 4) * pfs) : (t == 4 ? pfw : pfw + (lane < 13 ? 14 * 128 : 12 * 128)));
    sop = so; so = (so == 2 * SLOT) ? 0 : so + SLOT;
  }
  WAITV(9); WAITL0(); BAR();
  u32x4 stg;
  { int r = (w < 2) ? row0 + 64 * w + lane : (w == 2 ? row0 - 16 + (lane & 15) : row0 + lane); r = r < 0 ? 0 : r;
    asm volatile("global_load_dwordx4 %0, %1, %2 sc1" : "=v"(stg) : "v"((unsigned)(r * 16)), "s"(p.ws + WS_STATS) : "memory"); }
  half(F1, F0, 0, 0, 1, 0, 4, 0, 0);
  half(F0, F1, 0, 1, 1, 4, 3, 0, 0);
  WAITV(8); WAITL0(); BAR();
  half(F1, F0, SLOT, 0, 1, 0, 4, 0, 1);
  half(F0, F1, SLOT, 1, 1, 4, 3, 0, 1);
#pragma unroll
  for (int gi = 0; gi < 5; ++gi) mm(F1, gi);
  STAMP(4);
  {
    LAS f32x4* CSw = (LAS f32x4*)(lds + SC_CS);
    if (r16 == 0) {
      const int u0 = TA ? (mi ? 8 + fu : nj) : (mi * 8 + 2 * nj), u1 = TA ? (mi ? 8 + nj : 4 + nj) : (mi * 8 + 2 * nj + 1);
      CSw[u0 * 4 + g] = cs0; CSw[u1 * 4 + g] = cs1;
    }
    asm volatile("s_waitcnt vmcnt(12)" : "+v"(stg) :: "memory");
    if (w < 3) {
      int r = (w < 2) ? row0 + 64 * w + lane : row0 - 16 + (lane & 15); r = r < 0 ? 0 : r;
      if (!__all(stg[1] == tag && stg[3] == tag)) {
        gu64* gp = (gu64*)(p.ws + WS_STATS) + r * 2;
        for (unsigned spins = 0; spins < (1u << 20); ++spins) {
          const unsigned long long a = __hip_atomic_load(gp, RLX_AGENT), b = __hip_atomic_load(gp + 1, RLX_AGENT);
          stg = (u32x4){(unsigned)a, (unsigned)(a >> 32), (unsigned)b, (unsigned)(b >> 32)};
          if (__all(stg[1] == tag && stg[3] == tag)) break;
          __builtin_amdgcn_s_sleep(1);
        }
      }
      if (w < 2 || lane < 16) ((LAS f32x2*)(lds + SC_MR))[64 * w + lane] = (f32x2){__uint_as_float(stg[0]), __uint_as_float(stg[2])};
    }
  }
  WAITL0(); BAR();
  dmab(6, 0); dmab(6, 1); dmab(7, 0); dmab(7, 1);
  STAMP(5);
  LAS f32x4* CS = (LAS f32x4*)(lds + SC_CS);
  LAS f32x2* MR = (LAS f32x2*)(lds + SC_MR);
  const __amdgpu_buffer_rsrc_t ohr = __builtin_amdgcn_make_buffer_rsrc(p.ws + WS_OH, 0, 128 * NKS * 1024, 0x00020000);
  const unsigned ohl = lane * 16;
  if (!TA) {
    const f32x4 c0 = CS[(2 * nj) * 4 + g], c1 = CS[(2 * nj + 1) * 4 + g], c2 = CS[(8 + 2 * nj) * 4 + g], c3 = CS[(9 + 2 * nj) * 4 + g];
    const int tp = tm + 2 * nj;
    LAS char* fx = lds + SC_FX;
    { const f32x2 m0 = MR[64 * mi + 16 * nj + r16], m1 = MR[64 * mi + 16 * ((nj + 1) & 3) + r16];
      *(LAS s16x4*)(fx + ((mi * 2 + (nj & 1)) * 4 + nj) * 512 + lane * 8) = pack4(lnc(accx[0], m0, csx));
      *(LAS s16x4*)(fx + ((mi * 2 + (nj & 1)) * 4 + ((nj + 1) & 3)) * 512 + lane * 8) = pack4(lnc(accx[1], m1, csx)); }
    auto ffn = [&](const f32x4 (&G0)[4], const f32x4 (&U0)[4], const f32x4 (&G1)[4], const f32x4 (&U1)[4]) {
#pragma unroll
      for (int i = 0; i < 4; ++i) {
        const int rt = (i + nj) & 3;
        const f32x2 mr = MR[64 * mi + 16 * rt + r16];
        const u32x2 h0 = __builtin_bit_cast(u32x2, pack4(swiglu4(lnc(G0[i], mr, c0), lnc(U0[i], mr, c2))));
        const u32x2 h1 = __builtin_bit_cast(u32x2, pack4(swiglu4(lnc(G1[i], mr, c1), lnc(U1[i], mr, c3))));
        if (tp <= 85) st16(ohr, ohl + ((rg * 8 + 4 * mi + rt) * NKS + 16 + (tp >> 1)) * 1024, (u32x4){h0[0], h0[1], h1[0], h1[1]});
      }
    };
    if (mi) ffn(acc[2], acc[0], acc[3], acc[1]); else ffn(acc[0], acc[2], acc[1], acc[3]);
    WAITL0(); BAR();
    const f32x4 gx = unpack4(*(LAS s16x4*)(fx + ((mi * 2 + 0) * 4 + nj) * 512 + lane * 8)), ux = unpack4(*(LAS s16x4*)(fx + ((mi * 2 + 1) * 4 + nj) * 512 + lane * 8));
    st8(ohr, ohl + ((rg * 8 + w) * NKS + 16 + (te >> 1)) * 1024 + (te & 1) * 8, pack4(swiglu4(gx, ux)));
  } else {
    s16x4 Q[4], K[4], V[4]; u32x2 Fv[4];
    const f32x4 cq = CS[nj * 4 + g], ck = CS[(4 + nj) * 4 + g], cf = CS[(8 + fu) * 4 + g];
    const float cv = ((LAS float*)CS)[(8 + nj) * 16 + r16];
#pragma unroll
    for (int rt = 0; rt < 4; ++rt) {
      const f32x2 mr = MR[64 * mi + 16 * rt + r16];
      Q[rt] = pack4(lnc(acc[0][rt], mr, cq)); K[rt] = pack4(lnc(acc[1][rt], mr, ck)); Fv[rt] = __builtin_bit_cast(u32x2, pack4(lnc(acc[3][rt], mr, cf)));
      const f32x4 t01 = *(LAS f32x4*)(MR + 64 * mi + 16 * rt + 4 * g), t23 = *(LAS f32x4*)(MR + 64 * mi + 16 * rt + 4 * g + 2);
      const f32x4 mm4 = {t01[0], t01[2], t23[0], t23[2]}, rr4 = {t01[1], t01[3], t23[1], t23[3]};
      V[rt] = pack4((acc[2][rt] - mm4 * cv) * rr4);
    }
    LAS char* kvb = lds + SC_KV + nj * 1536;
    LAS char* fx = lds + SC_FX + w * 1024;
    const s16x4 hv = pack4(lnc(acch, MR[128 + r16], CS[((mi ? 8 : 4) + nj) * 4 + g]));
    if (mi == 0) { *(LAS s16x4*)(kvb + 512 + lane * 8) = K[3]; *(LAS s16x4*)(kvb + 1024 + lane * 8) = V[3]; }
    else *(LAS s16x4*)(kvb + r16 * 32 + g * 8) = hv;
    *(LAS u32x2*)(fx + lane * 8) = (nj & 1) ? Fv[0] : Fv[2]; *(LAS u32x2*)(fx + 512 + lane * 8) = (nj & 1) ? Fv[1] : Fv[3];
    f32x4 mkp, mkc;
#pragma unroll
    for (int r = 0; r < 4; ++r) {
      const int dp = r16 + 16 - (4 * g + r), dc = r16 - (4 * g + r);
      mkp[r] = (!(dp & 1) && dp <= 14) ? 0.f : -__builtin_inff();
      mkc[r] = (!(dc & 1) && dc >= 0 && dc <= 14) ? 0.f : -__builtin_inff();
    }
    const bool odd = r16 & 1;
    const unsigned psh = odd ? 16u : 0u;
    const int RT0 = rg * 8 + 4 * mi;
    const unsigned od = ohl + (RT0 * NKS + 2 * cg + (nj >> 1)) * 1024 + (nj & 1) * 8;
    const s16x4 ones4 = {0x3f80, 0x3f80, 0x3f80, 0x3f80};
    const bf16x8 ones8 = __builtin_bit_cast(bf16x8, __builtin_shufflevector(ones4, ones4, 0, 1, 2, 3, 4, 5, 6, 7));
    auto attn = [&](s16x4 kp, s16x4 vp, s16x4 kc, s16x4 vc, s16x4 qq, f32x4 mp, int rt) {
      const f32x4 z = {0, 0, 0, 0};
      const f32x4 sp = mfma16(kp, qq, mp), sc = mfma16(kc, qq, mkc);
      const float sc2 = 0.25f * 1.44269504089f;
      const float e0 = odd ? sp[1] : sp[0], e1 = odd ? sp[3] : sp[2], e2 = odd ? sc[1] : sc[0], e3 = odd ? sc[3] : sc[2];
      const float mx = xg_max(fmaxf(fmaxf(e0, e1), fmaxf(e2, e3))) * sc2;
      const float p0 = __builtin_amdgcn_exp2f(fmaf(e0, sc2, -mx)), p1 = __builtin_amdgcn_exp2f(fmaf(e1, sc2, -mx));
      const float p2 = __builtin_amdgcn_exp2f(fmaf(e2, sc2, -mx)), p3 = __builtin_amdgcn_exp2f(fmaf(e3, sc2, -mx));
      const u32x2 k01 = __builtin_bit_cast(u32x2, pack4((f32x4){p0, 0.f, p1, 0.f})), k23 = __builtin_bit_cast(u32x2, pack4((f32x4){p2, 0.f, p3, 0.f}));
      const u32x4 pw4 = {k01[0] << psh, k01[1] << psh, k23[0] << psh, k23[1] << psh};
      const bf16x8 pf = __builtin_bit_cast(bf16x8, pw4);
      const bf16x8 vf = __builtin_bit_cast(bf16x8, __builtin_shufflevector(vp, vc, 0, 1, 2, 3, 4, 5, 6, 7));
      const f32x4 o = mfma32(vf, pf, z), sm = mfma32(ones8, pf, z);
      st8(ohr, od + rt * NKS * 1024, pack4(o * __builtin_amdgcn_rcpf(sm[0])));
    };
#pragma unroll
    for (int rt = 1; rt < 4; ++rt) attn(K[rt - 1], V[rt - 1], K[rt], V[rt], Q[rt], mkp, rt);
    WAITL0(); BAR();
    {
      LAS char* fo = lds + SC_FX + (w ^ 1) * 1024;
      const u32x2 o0 = *(LAS u32x2*)(fo + lane * 8), o1 = *(LAS u32x2*)(fo + 512 + lane * 8);
      const int rb = (nj & 1) * 2;
      const u32x2 m0 = (nj & 1) ? Fv[2] : Fv[0], m1 = (nj & 1) ? Fv[3] : Fv[1];
      const u32x2 g0 = (nj & 1) ? o0 : m0, u0 = (nj & 1) ? m0 : o0, g1 = (nj & 1) ? o1 : m1, u1 = (nj & 1) ? m1 : o1;
      const unsigned hd = ohl + ((RT0 + rb) * NKS + 16 + cg) * 1024 + (nj >> 1) * 8;
      st8(ohr, hd, pack4(swiglu4(unpack4(__builtin_bit_cast(s16x4, g0)), unpack4(__builtin_bit_cast(s16x4, u0)))));
      st8(ohr, hd + NKS * 1024, pack4(swiglu4(unpack4(__builtin_bit_cast(s16x4, g1)), unpack4(__builtin_bit_cast(s16x4, u1)))));
    }
    s16x4 Kp, Vp;
    if (mi == 0) { Kp = hv; Vp = __builtin_amdgcn_ds_read_tr16_b64_v4i16((LAS s16x4*)(kvb + (4 * g + q) * 32 + pp * 8)); }
    else { Kp = *(LAS s16x4*)(kvb + 512 + lane * 8); Vp = *(LAS s16x4*)(kvb + 1024 + lane * 8); }
    const float ninf = -__builtin_inff();
    attn(Kp, Vp, K[0], V[0], Q[0], RT0 == 0 ? (f32x4){ninf, ninf, ninf, ninf} : mkp, 0);
  }
  STAMP(6);
  return tag;
}

constexpr int NR = (NKS + 3) / 4;
constexpr int P2D = 5;
DI void gload16(bf16x8& dst, const char* sbase, unsigned voff) { asm volatile("global_load_dwordx4 %0, %1, %2 sc1" : "=v"(dst) : "v"(voff), "s"(sbase) : "memory"); }
template <int N> DI void waitVn() { asm volatile("s_waitcnt vmcnt(%0)" :: "n"(N) : "memory"); }
constexpr int P2A = 6;
constexpr int P2NA = P2A + 1;
constexpr int p2opsA(int q) { return (q + P2A < NR) ? 2 : 0; }
constexpr int p2opsB(int q) { return (q + P2D >= 8 && q + P2D < NR) ? 2 : 0; }
constexpr int p2posA(int r) { if (r < P2A) return 2 * (r + 1); int n = 2 * P2A; for (int q = 0; q < r - P2A; ++q) n += p2opsA(q) + p2opsB(q); return n + 2; }
constexpr int p2posB(int r) { int n = 2 * P2A; for (int q = 0; q < r - P2D; ++q) n += p2opsA(q) + p2opsB(q); return n + p2opsA(r - P2D) + 2; }
constexpr int p2issued(int r) { int n = 2 * P2A; for (int q = 0; q < r; ++q) n += p2opsA(q) + p2opsB(q); return n; }
template <int R> struct P2W {
  static constexpr int need = (R + 1 >= 8 && R + 1 < NR && p2posB(R + 1) > p2posA(R)) ? p2posB(R + 1) : p2posA(R);
  static constexpr int n = p2issued(R) - need;
};

DI void phase2(const Params& p, LAS char* lds, int rt64, int ct64, unsigned tag, bool boss) {
  const int tid = threadIdx.x, lane = tid & 63;
  const int w = rfl(tid >> 6), kh = w >> 2, m4 = w & 3;
  const int r16 = lane & 15, g = lane >> 4, q = r16 >> 2, pp = lane & 3;
  const unsigned ldsb = (unsigned)(uintptr_t)lds;
  constexpr int PH = 1; STAMP(0);
  const char* pa = p.ws + WS_OH + ((size_t)(rt64 * 4 + m4) * NKS + kh) * 1024;
  const unsigned va = lane * 16;
  unsigned vb;
  { const int row = 8 * m4 + (lane >> 3), pc = lane & 7; const int c = (pc >> 1) ^ ((row >> 1) & 3);
    vb = row * 1024 + ct64 * 128 + (2 * c + (pc & 1)) * 16; }
  const char* pb2 = p.w2 + (long)(kh - 16) * 32768;
  if (w == 0) {
    gu64* fl = (gu64*)(p.ws + WS_FLAGS) + (rt64 >> 1) * 16 + (lane & 15);
    const unsigned long long want = FMAGIC | tag;
    for (unsigned spins = 0; spins < (1u << 20); ++spins) {
      const unsigned long long v = __hip_atomic_load(fl, RLX_AGENT);
      if (__all(v == want)) break;
      __builtin_amdgcn_s_sleep(1);
    }
  }
  BAR();
  STAMP(1);
  bf16x8 A[P2NA][2];
  auto issueA = [&](int r) {
#pragma unroll
    for (int j = 0; j < 2; ++j) {
      const int ke = 4 * r + 2 * j;
      const bool over = (ke + 1 > NKS - 1);
      gload16(A[r % P2NA][j], pa + (over ? (long)ke - 2 * kh : (long)ke) * 1024, va);
    }
  };
#pragma unroll
  for (int r = 0; r < P2A; ++r) { issueA(r); if (r + 1 < P2A) BAR(); }
  auto issueB = [&](int r) {
#pragma unroll
    for (int j = 0; j < 2; ++j) {
      const int ke = 4 * r + 2 * j;
      const bool over = (ke + 1 > NKS - 1);
      glds16s(pb2 + (over ? (long)ke - 2 * kh : (long)ke) * 32768, vb, ldsb + bslot(r) + (2 * j) * 4096 + kh * 4096 + m4 * 1024);
    }
  };
  int bl[4];
#pragma unroll
  for (int c = 0; c < 4; ++c) bl[c] = (4 * g + q) * 128 + ((c ^ ((2 * g + (q >> 1)) & 3)) << 5) + pp * 8 + kh * 4096;
  f32x4 acc[4] = {{0, 0, 0, 0}, {0, 0, 0, 0}, {0, 0, 0, 0}, {0, 0, 0, 0}};
  const size_t ooff = (size_t)(rt64 * 64 + 16 * m4 + r16) * 1024 + (ct64 * 64 + 32 * kh + 4 * g) * 2;
  bf16x4 xv0, xv1;
  u32x4 fl0 = {0, 0, 0, 0}, fl1 = {0, 0, 0, 0};
  bf16x8 bf[2][2][4];
  auto readB = [&](int r) {
    LAS char* sb = lds + bslot(r);
#pragma unroll
    for (int j = 0; j < 2; ++j)
#pragma unroll
      for (int c = 0; c < 4; ++c) bf[r & 1][j][c] = ldsB(sb + j * 8192 + bl[c], 2048);
  };
  readB(0);
  auto round = [&](auto RC) {
    constexpr int r = decltype(RC)::value;
    waitVn<P2W<r>::n>();
    asm volatile("" : "+v"(A[r % P2NA][0]), "+v"(A[r % P2NA][1]));
    if (r + 1 < NR && (r + 1 >= 8 || r + P2D >= 8)) BAR();
    if (r + P2A < NR) issueA(r + P2A);
    if (r + P2D >= 8 && r + P2D < NR) issueB(r + P2D);
    if (r + 1 == NR) { xv0 = *(const bf16x4*)(p.x + ooff); xv1 = *(const bf16x4*)(p.x + ooff + 32); }
    if (r == 0) STAMP(2);
    if (r == 8) STAMP(3);
    if (r == 11 && boss) {
      const char* fb = p.ws + WS_FLAGS;
      asm volatile("global_load_dwordx4 %0, %2, %3 sc1\n\tglobal_load_dwordx4 %1, %2, %3 offset:1024 sc1" : "=&v"(fl0), "=&v"(fl1) : "v"(va), "s"(fb) : "memory");
    }
    SB0();
    if (r + 1 < NR) readB(r + 1);
#pragma unroll
    for (int j = 0; j < 2; ++j) {
      if (4 * r + 2 * j + 1 > NKS - 1 && kh) continue;
#pragma unroll
      for (int c = 0; c < 4; ++c) acc[c] = mfma32(bf[r & 1][j][c], A[r % P2NA][j], acc[c]);
    }
#pragma unroll
    for (int k = 0; k < 8; ++k) {
      __builtin_amdgcn_sched_group_barrier(0x8, 1, 0);
      __builtin_amdgcn_sched_group_barrier(0x100, 2, 0);
    }
    SB0();
  };
  static_for<NR>(round);
  STAMP(4);
  {
    LAS f32x4* red = (LAS f32x4*)(lds + SC_RED);
    red[(w * 2 + 0) * 64 + lane] = kh ? acc[0] : acc[2];
    red[(w * 2 + 1) * 64 + lane] = kh ? acc[1] : acc[3];
    WAITL0(); BAR();
    const f32x4 o0 = red[((w ^ 4) * 2 + 0) * 64 + lane], o1 = red[((w ^ 4) * 2 + 1) * 64 + lane];
    const f32x4 s0 = (kh ? acc[2] : acc[0]) + o0, s1 = (kh ? acc[3] : acc[1]) + o1;
    const f32x4 x0 = __builtin_convertvector(xv0, f32x4), x1 = __builtin_convertvector(xv1, f32x4);
    *(s16x4*)(p.out + ooff) = pack4(s0 + x0);
    *(s16x4*)(p.out + ooff + 32) = pack4(s1 + x1);
  }
  STAMP(5);
  if (boss) {
    asm volatile("s_waitcnt vmcnt(0)" : "+v"(fl0), "+v"(fl1) :: "memory");
    const unsigned hi = (unsigned)(FMAGIC >> 32);
    bool ok = fl0[0] == tag && fl0[1] == hi && fl0[2] == tag && fl0[3] == hi && fl1[0] == tag && fl1[1] == hi && fl1[2] == tag && fl1[3] == hi;
    if (!__all(ok)) {
      gu64* fl = (gu64*)(p.ws + WS_FLAGS) + lane;
      const unsigned long long want = FMAGIC | tag;
      for (unsigned spins = 0; spins < (1u << 20); ++spins) {
        ok = __hip_atomic_load(fl, RLX_AGENT) == want && __hip_atomic_load(fl + 64, RLX_AGENT) == want &&
             __hip_atomic_load(fl + 128, RLX_AGENT) == want && __hip_atomic_load(fl + 192, RLX_AGENT) == want;
        if (__all(ok)) break;
        __builtin_amdgcn_s_sleep(1);
      }
    }
    if (lane == 0) __hip_atomic_store((gu32*)p.ws, tag, RLX_AGENT);
  }
}

__global__ __launch_bounds__(512, 2) void k_fused(Params p) {
  extern __shared__ __attribute__((aligned(16))) char shm[];
  LAS char* lds = (LAS char*)shm;
  const int b = blockIdx.x, xcd = b & 7, idx = b >> 3;
  const int rg = (xcd & 1) * 8 + (idx & 7), cg = (xcd >> 1) * 4 + (idx >> 3);
  const int rt64 = xcd * 4 + (idx & 3), ct64 = idx >> 2;
  const unsigned tag = (cg < 8) ? phase1<true>(p, lds, rg, cg, ct64, idx) : phase1<false>(p, lds, rg, cg - 8, ct64, idx);
  asm volatile("s_waitcnt vmcnt(0)" ::: "memory");
  BAR();
  if (threadIdx.x == 0) __hip_atomic_store((gu64*)(p.ws + WS_FLAGS) + rg * 16 + cg, FMAGIC | tag, RLX_AGENT);
  { constexpr int PH = 0; STAMP(7); }
  phase2(p, lds, rt64, ct64, tag, b == 0 && rfl(threadIdx.x >> 6) == 0);
  { constexpr int PH = 1; STAMP(6); STAMP(7); }
}

extern "C" void kernel_launch(void* const* d_in, const int* in_sizes, int n_in, void* d_out, int out_size, void* d_ws, size_t ws_size,
                              hipStream_t stream) {
  Params p;
  p.x = (const char*)d_in[0]; p.wqkv = (const char*)d_in[1]; p.wo = (const char*)d_in[2];
  p.w1 = (const char*)d_in[4]; p.w3 = (const char*)d_in[5]; p.w2 = (const char*)d_in[6];
  p.ws = (char*)d_ws; p.out = (char*)d_out; p.stamps = (unsigned long long*)((char*)d_ws + (128u << 20));
  (void)hipFuncSetAttribute((const void*)k_fused, hipFuncAttributeMaxDynamicSharedMemorySize, P1_LDS);
  k_fused<<<256, 512, P1_LDS, stream>>>(p);
}
```

```cpp
#include <hip/hip_runtime.h>
#include <stdint.h>
#include <stdio.h>

#define LAS __attribute__((address_space(3)))
#define DI __device__ __forceinline__
typedef __bf16 bf16x8 __attribute__((ext_vector_type(8)));
typedef __bf16 bf16x4 __attribute__((ext_vector_type(4)));
typedef short s16x4 __attribute__((ext_vector_type(4)));
typedef float f32x4 __attribute__((ext_vector_type(4)));
typedef float f32x2 __attribute__((ext_vector_type(2)));
typedef unsigned u32x2 __attribute__((ext_vector_type(2)));
typedef unsigned u32x4 __attribute__((ext_vector_type(4)));

constexpr int NKS = 59;
constexpr int SLOT = 53248;
constexpr int WOFF = 16384, EOFF = 49152;
constexpr int DUMMY = 3 * SLOT;
constexpr int SC_CS = DUMMY + 1024;
constexpr int SC_MR = SC_CS + 1152;
constexpr int P1_LDS = 163840;
constexpr int SC_HS = 86016;
constexpr int SC_KV = SC_HS + 1024;
constexpr int SC_FX = SC_KV + 6144;
constexpr int SC_RED = 86016;
__host__ __device__ constexpr int bslot(int r) { return (r & 7) < 3 ? 2 * SLOT + (r & 7) * 16384 : (r & 7) < 6 ? ((r & 7) - 3) * 16384 : SLOT + ((r & 7) - 6) * 16384; }
constexpr unsigned long long FMAGIC = 0x5AC3E17Bull << 32;
constexpr size_t WS_FLAGS = 4096, WS_STATS = 8192, WS_OH = 65536;

#define DIAG 1
constexpr int SC_STAMP = 163072;
#define STAMP(i) do { if (threadIdx.x == 0) { ((LAS unsigned long long*)(lds + SC_STAMP))[PH * 16 + (i)] = __builtin_amdgcn_s_memrealtime(); ((LAS unsigned long long*)(lds + SC_STAMP))[PH * 16 + 8 + (i)] = __builtin_amdgcn_s_memtime(); } } while (0)

template <int V> struct IC { static constexpr int value = V; };
template <int N, int I = 0, class F> DI void static_for(F& f) { if constexpr (I < N) { f(IC<I>{}); static_for<N, I + 1>(f); } }
DI int rfl(int v) { return __builtin_amdgcn_readfirstlane(v); }
DI void glds16(const void* g, unsigned lds_dst) {
  unsigned keep;
  asm volatile("s_mov_b32 %0, m0\n\ts_mov_b32 m0, %2\n\ts_nop 0\n\tglobal_load_lds_dwordx4 %1, off\n\ts_mov_b32 m0, %0"
               : "=&s"(keep) : "v"(g), "s"(lds_dst) : "memory");
}
DI void glds16s(const char* sbase, unsigned voff, unsigned lds_dst) {
  asm volatile("s_mov_b32 m0, %2\n\ts_nop 0\n\tglobal_load_lds_dwordx4 %1, %0" :: "s"(sbase), "v"(voff), "s"(lds_dst) : "memory", "m0");
}
#define WAITV(n) asm volatile("s_waitcnt vmcnt(" #n ")" ::: "memory")
#define WAITL0() do { asm volatile("" ::: "memory"); __builtin_amdgcn_s_waitcnt(0xC07F); asm volatile("" ::: "memory"); } while (0)
#define BAR() do { asm volatile("" ::: "memory"); __builtin_amdgcn_s_barrier(); asm volatile("" ::: "memory"); } while (0)
#define SB0() __builtin_amdgcn_sched_barrier(0)

DI bf16x8 ldsA(LAS char* p) { return *(LAS bf16x8*)p; }
DI bf16x8 ldsB(LAS char* p, int toff) {
  s16x4 lo = __builtin_amdgcn_ds_read_tr16_b64_v4i16((LAS s16x4*)p);
  s16x4 hi = __builtin_amdgcn_ds_read_tr16_b64_v4i16((LAS s16x4*)(p + toff));
  return __builtin_bit_cast(bf16x8, __builtin_shufflevector(lo, hi, 0, 1, 2, 3, 4, 5, 6, 7));
}
DI f32x4 mfma32(bf16x8 a, bf16x8 b, f32x4 c) { return __builtin_amdgcn_mfma_f32_16x16x32_bf16(a, b, c, 0, 0, 0); }
DI f32x4 mfma16(s16x4 a, s16x4 b, f32x4 c) { return __builtin_amdgcn_mfma_f32_16x16x16bf16_1k(a, b, c, 0, 0, 0); }
DI s16x4 pack4(f32x4 v) { return __builtin_bit_cast(s16x4, __builtin_convertvector(v, bf16x4)); }
DI f32x4 unpack4(s16x4 v) { return __builtin_convertvector(__builtin_bit_cast(bf16x4, v), f32x4); }
DI float xg_sum(float v) {
  auto r = __builtin_amdgcn_permlane16_swap(__float_as_uint(v), __float_as_uint(v), false, false);
  v = __uint_as_float(r[0]) + __uint_as_float(r[1]);
  auto s = __builtin_amdgcn_permlane32_swap(__float_as_uint(v), __float_as_uint(v), false, false);
  return __uint_as_float(s[0]) + __uint_as_float(s[1]);
}
DI float xg_max(float v) {
  auto r = __builtin_amdgcn_permlane16_swap(__float_as_uint(v), __float_as_uint(v), false, false);
  v = fmaxf(__uint_as_float(r[0]), __uint_as_float(r[1]));
  auto s = __builtin_amdgcn_permlane32_swap(__float_as_uint(v), __float_as_uint(v), false, false);
  return fmaxf(__uint_as_float(s[0]), __uint_as_float(s[1]));
}
DI float row_sum16(float v) {
  v += __builtin_bit_cast(float, __builtin_amdgcn_update_dpp(0, __builtin_bit_cast(int, v), 0x128, 0xf, 0xf, false));
  v += __builtin_bit_cast(float, __builtin_amdgcn_update_dpp(0, __builtin_bit_cast(int, v), 0x124, 0xf, 0xf, false));
  v += __builtin_bit_cast(float, __builtin_amdgcn_update_dpp(0, __builtin_bit_cast(int, v), 0x122, 0xf, 0xf, false));
  v += __builtin_bit_cast(float, __builtin_amdgcn_update_dpp(0, __builtin_bit_cast(int, v), 0x121, 0xf, 0xf, false));
  return v;
}
DI f32x4 lnc(f32x4 a, f32x2 mr, f32x4 c) { return (a - c * mr[0]) * mr[1]; }
DI f32x4 swiglu4(f32x4 g, f32x4 u) {
  const f32x4 t = g * -1.44269504089f;
  f32x4 d;
#pragma unroll
  for (int r = 0; r < 4; ++r) d[r] = __builtin_amdgcn_rcpf(1.0f + __builtin_amdgcn_exp2f(t[r]));
  return g * u * d;
}

struct Params {
  const char* x; const char* wqkv; const char* wo; const char* w1; const char* w3; const char* w2;
  char* ws; char* out; unsigned long long* stamps;
};
typedef __attribute__((address_space(1))) unsigned gu32;
typedef __attribute__((address_space(1))) unsigned long long gu64;
#define RLX_AGENT __ATOMIC_RELAXED, __HIP_MEMORY_SCOPE_AGENT
DI void st16(__amdgpu_buffer_rsrc_t r, unsigned off, u32x4 v) { __builtin_amdgcn_raw_buffer_store_b128(v, r, off, 0, 16); }
DI void st8(__amdgpu_buffer_rsrc_t r, unsigned off, s16x4 v) { __builtin_amdgcn_raw_buffer_store_b64(__builtin_bit_cast(u32x2, v), r, off, 0, 16); }

struct Frags { bf16x8 a[4], ah, b[4], bx, be, e0; };

template <bool TA>
DI unsigned phase1(const Params& p, LAS char* lds, int rg, int cg, int ct64, int idx) {
  const int tid = threadIdx.x, lane = tid & 63;
  const int w = rfl(tid >> 6), mi = w >> 2, nj = w & 3;
  const int r16 = lane & 15, g = lane >> 4, q = r16 >> 2, pp = lane & 3;
  constexpr int PH = 0; STAMP(0);
  const unsigned ldsb = (unsigned)(uintptr_t)lds;
  const int row0 = rg * 128;
  const int srow = row0 + (TA ? cg : 8 + cg) * 8 + w;
  unsigned ev; u32x4 xrow;
  asm volatile("global_load_dword %0, %1, %2 sc1" : "=v"(ev) : "v"(0u), "s"(p.ws) : "memory");
  asm volatile("global_load_dwordx4 %0, %1, %2" : "=v"(xrow) : "v"((unsigned)(srow * 1024 + lane * 16)), "s"(p.x) : "memory");
  const int tm = TA ? 2 * cg : 16 + 18 * (cg >> 1) + ((cg & 1) ? 10 : 0);
  const int te = 16 + 18 * (cg >> 1) + 8 + (cg & 1);
  unsigned vx[2], vw[4], ve;
#pragma unroll
  for (int m = 0; m < 2; ++m) {
    const int row = 16 * w + 8 * m + (lane >> 3);
    vx[m] = (unsigned)((row0 + row) * 1024 + (((lane & 7) ^ ((row >> 1) & 7)) << 4));
  }
  const char* bw[4]; unsigned ldw[4], dw[4];
#pragma unroll
  for (int m = 0; m < 4; ++m) {
    if (!TA || m < 2) {
      const int pi = TA ? (2 * w + m) : (2 * w + (m & 1));
      const int kr = 4 * pi + (lane >> 4), pc = lane & 15;
      const int c = (pc >> 1) ^ ((kr & 3) | (((kr >> 3) & 1) << 2));
      if (TA) { bw[m] = p.wqkv; ldw[m] = 3072; vw[m] = kr * 3072 + ((c >> 2) * 512 + (4 * cg + (c & 3)) * 16) * 2 + (pc & 1) * 16; dw[m] = WOFF + pi * 1024; }
      else { bw[m] = (m >> 1) ? p.w3 : p.w1; ldw[m] = 2752; int t = tm + c; t = t > 85 ? 85 : t; vw[m] = kr * 2752 + t * 32 + (pc & 1) * 16; dw[m] = WOFF + (m >> 1) * 16384 + pi * 1024; }
    } else if (m == 2) {
      const int kr = 8 * w + (lane >> 3), pc = lane & 7;
      const int c = (pc >> 1) ^ (((kr >> 1) & 1) | (((kr >> 3) & 1) << 1));
      bw[m] = p.wqkv; ldw[m] = 3072; vw[m] = kr * 3072 + (1024 + (4 * cg + c) * 16) * 2 + (pc & 1) * 16; dw[m] = WOFF + 16384 + w * 1024;
    } else {
      const int kr = 16 * (w & 3) + (lane >> 2), pc = lane & 3;
      const int e = (pc >> 1) ^ ((kr >> 3) & 1);
      bw[m] = mi ? p.w3 : p.w1; ldw[m] = 2752; vw[m] = kr * 2752 + (tm + e) * 32 + (pc & 1) * 16; dw[m] = WOFF + 24576 + mi * 4096 + (w & 3) * 1024;
    }
  }
  const bool has_e = TA ? (w < 2) : (w < 4);
  const char* be_; unsigned lde, de;
  if (TA) {
    const int row = 8 * (w & 1) + (lane >> 3);
    int gr = row0 - 16 + row; gr = gr < 0 ? 0 : gr;
    be_ = p.x; lde = 2; ve = gr * 1024 + (((lane & 7) ^ ((row >> 1) & 7)) << 4); de = EOFF + w * 1024;
  } else {
    const int pos = 32 * (w & 1) + (lane >> 1), kr = pos ^ (((pos >> 3) & 1) << 2);
    be_ = (w & 2) ? p.w3 : p.w1; lde = 2752; ve = kr * 2752 + te * 32 + (lane & 1) * 16; de = EOFF + (w & 3) * 1024;
  }
  if (!has_e) { be_ = p.x; lde = 0; ve = 0; }
  unsigned vb;
  { const int row = 8 * nj + (lane >> 3), pc = lane & 7; const int c = (pc >> 1) ^ ((row >> 1) & 3);
    vb = row * 1024 + ct64 * 128 + (2 * c + (pc & 1)) * 16; }
  const unsigned emask = has_e ? 0xffffffffu : 0u;
  if (!has_e) de = DUMMY;
  const char* sx = p.x; const char* se = be_; const char* sw[4] = {bw[0], bw[1], bw[2], bw[3]};
  auto dma = [&](int i, int so) {
    if (i == 0) { glds16s(se, ve, ldsb + (so & emask) + de); se += 64u * lde; }
    else if (i <= 2) { glds16s(sx, vx[i - 1], ldsb + so + (2 * w + i - 1) * 1024); if (i == 2) sx += 128; }
    else { glds16s(sw[i - 3], vw[i - 3], ldsb + so + dw[i - 3]); sw[i - 3] += 64u * ldw[i - 3]; }
  };
  auto dmab = [&](int rr, int j) {
    const int ks2 = 4 * rr + 2 * j + mi;
    const char* src = (4 * rr + 2 * j < 16) ? p.wo + ks2 * 32768 : p.w2 + (ks2 - 16) * 32768;
    glds16s(src, vb, ldsb + bslot(rr) + (2 * j + mi) * 4096 + nj * 1024);
  };
#pragma unroll
  for (int i = 0; i < 7; ++i) dma(i, 0);
#pragma unroll
  for (int i = 0; i < 7; ++i) dma(i, SLOT);
  STAMP(1);
  asm volatile("s_waitcnt vmcnt(14)" : "+v"(ev), "+v"(xrow) :: "memory");
  const unsigned tag = (unsigned)rfl((int)ev) + 1u;
  {
    float s1 = 0.f, s2 = 0.f;
#pragma unroll
    for (int i = 0; i < 4; ++i) {
      const float lo = __uint_as_float(xrow[i] << 16), hi = __uint_as_float(xrow[i] & 0xffff0000u);
      s1 += lo + hi; s2 = fmaf(lo, lo, fmaf(hi, hi, s2));
    }
    s1 = xg_sum(row_sum16(s1)); s2 = xg_sum(row_sum16(s2));
    const float m = s1 * (1.0f / 512.0f), var = fmaxf(s2 * (1.0f / 512.0f) - m * m, 0.f);
    const float rs = __builtin_amdgcn_rsqf(var + 1e-5f);
    const __amdgpu_buffer_rsrc_t str = __builtin_amdgcn_make_buffer_rsrc(p.ws + WS_STATS, 0, 2048 * 16, 0x00020000);
    if (lane == 0) st16(str, srow * 16, (u32x4){__float_as_uint(m), tag, __float_as_uint(rs), tag});
  }
  const char* pfp; unsigned pfs; const char* pfw;
  {
    const int l5 = lane & 31;
    const int L = w * 10 + (l5 < 10 ? l5 : 9);
    if (L < 32) { pfp = p.x + (size_t)(row0 + 32 * (idx >> 3) + L) * 1024; pfs = 128; }
    else {
      const int L2 = L - 32, ri = L2 / 6, sg = L2 - 6 * ri;
      const int kr = 8 * (idx & 7) + ri;
      if (TA) { pfp = (sg < 3) ? p.wqkv + (size_t)kr * 3072 + sg * 1024 + cg * 128 : (sg == 3 ? p.w1 : p.w3) + (size_t)kr * 2752 + tm * 32; pfs = (sg < 3) ? 64u * 3072 : 64u * 2752; }
      else { int cb = (tm < te ? tm : te) * 32 + (sg % 3) * 128; cb = cb > 2748 ? 2748 : cb; pfp = (sg < 3 ? p.w1 : p.w3) + (size_t)kr * 2752 + cb; pfs = 64u * 2752; }
    }
    pfw = p.w2 + (size_t)512 * 1024 + (size_t)((idx * 8 + w) * 27 + (lane < 14 ? lane : 13)) * 128;
  }
  auto pf = [&](const char* a) { asm volatile("s_mov_b32 m0, %1\n\ts_nop 0\n\tglobal_load_lds_dword %0, off" :: "v"(a), "s"(ldsb + DUMMY) : "memory", "m0"); };
  pf(pfp + (size_t)((lane < 32 ? 2u : 3u) * pfs));
  const int xa0 = r16 * 128 + ((g ^ (r16 >> 1)) << 4);
  const int fl = q | ((g & 1) << 2);
  const int w256 = (8 * g + q) * 256 + (fl << 5) + pp * 8;
  const int w128 = (8 * g + q) * 128 + ((((q >> 1) & 1) | ((g & 1) << 1)) << 5) + pp * 8;
  const int w64 = (8 * g + q) * 64 + ((g & 1) << 5) + pp * 8;
  const int w32 = (8 * g + ((g & 1) << 2) + q) * 32 + pp * 8;
  const int dt32 = (g & 1) ? -128 : 128;
  const int tb0 = (w256 ^ (nj << 6)) + (mi ? 16384 : 0), tb2 = (w256 ^ (nj << 6)) + (mi ? 0 : 16384);
  const int tbx = EOFF + (nj & 1) * 2048 + w32;
  const int taq = w256 ^ (nj << 5), tak = w256 ^ ((4 + nj) << 5), tav = 16384 + (w128 ^ (nj << 5)), taf = 24576 + (nj & 1) * 4096 + (w64 ^ ((nj >> 1) << 5));
  const int fu = 4 + ((nj >> 1) | ((nj & 1) << 1));
  int xr[4];
#pragma unroll
  for (int i = 0; i < 4; ++i) xr[i] = mi * 8192 + (TA ? i : ((i + nj) & 3)) * 2048;
  const int bea = mi ? tav : tak, bes = mi ? 4096 : 8192, bet = mi ? 512 : 1024;
  const int e0a = mi ? taf : taq, e0s = mi ? 2048 : 8192, e0t = mi ? 256 : 1024;
  constexpr int NRD = TA ? 11 : 9;
  auto ldsB2 = [&](LAS char* p0, LAS char* p1) {
    s16x4 lo = __builtin_amdgcn_ds_read_tr16_b64_v4i16((LAS s16x4*)p0), hi = __builtin_amdgcn_ds_read_tr16_b64_v4i16((LAS s16x4*)p1);
    return __builtin_bit_cast(bf16x8, __builtin_shufflevector(lo, hi, 0, 1, 2, 3, 4, 5, 6, 7));
  };
  auto rdf = [&](Frags& F, int i, int so, int ks) {
    LAS char* sb = lds + so;
    const int xa = xa0 ^ (ks * 64);
    LAS char* wb = sb + WOFF;
    if (TA) {
      switch (i) {
        case 0: F.a[0] = ldsA(sb + xa + xr[0]); break;
        case 1: F.b[0] = ldsB(wb + taq + ks * 8192, 1024); break;
        case 2: F.b[1] = ldsB(wb + tak + ks * 8192, 1024); break;
        case 3: F.b[2] = ldsB(wb + tav + ks * 4096, 512); break;
        case 4: F.b[3] = ldsB(wb + taf + ks * 2048, 256); break;
        case 5: F.a[1] = ldsA(sb + xa + xr[1]); break;
        case 6: F.a[2] = ldsA(sb + xa + xr[2]); break;
        case 7: F.a[3] = ldsA(sb + xa + xr[3]); break;
        case 8: F.be = ldsB2(wb + bea + ks * bes, wb + bea + ks * bes + bet); break;
        case 9: F.ah = ldsA(sb + EOFF + xa); break;
        default: F.e0 = ldsB2(wb + e0a + ks * e0s, wb + e0a + ks * e0s + e0t); break;
      }
    } else {
      switch (i) {
        case 0: F.a[0] = ldsA(sb + xa + xr[0]); break;
        case 1: F.b[0] = ldsB(wb + tb0 + ks * 8192, 1024); break;
        case 2: F.b[1] = ldsB(wb + (tb0 ^ 32) + ks * 8192, 1024); break;
        case 3: F.b[2] = ldsB(wb + tb2 + ks * 8192, 1024); break;
        case 4: F.b[3] = ldsB(wb + (tb2 ^ 32) + ks * 8192, 1024); break;
        case 5: F.a[1] = ldsA(sb + xa + xr[1]); break;
        case 6: F.a[2] = ldsA(sb + xa + xr[2]); break;
        case 7: F.a[3] = ldsA(sb + xa + xr[3]); break;
        default: F.bx = ldsB2(sb + tbx + ks * 1024, sb + tbx + ks * 1024 + dt32); break;
      }
    }
  };
  f32x4 acc[4][4], accx[2], acch = {0, 0, 0, 0}, cs0 = {0, 0, 0, 0}, cs1 = {0, 0, 0, 0}, csx = {0, 0, 0, 0};
  accx[0] = (f32x4){0, 0, 0, 0}; accx[1] = (f32x4){0, 0, 0, 0};
#pragma unroll
  for (int c = 0; c < 4; ++c)
#pragma unroll
    for (int rt = 0; rt < 4; ++rt) acc[c][rt] = (f32x4){0, 0, 0, 0};
  const bf16x8 ones = {(__bf16)1.0f, (__bf16)1.0f, (__bf16)1.0f, (__bf16)1.0f, (__bf16)1.0f, (__bf16)1.0f, (__bf16)1.0f, (__bf16)1.0f};
  auto mm = [&](const Frags& F, int gi) {
    if (gi < 4) {
      acc[0][gi] = mfma32(F.b[0], F.a[gi], acc[0][gi]);
      acc[1][gi] = mfma32(F.b[1], F.a[gi], acc[1][gi]);
      acc[2][gi] = TA ? mfma32(F.a[gi], F.b[2], acc[2][gi]) : mfma32(F.b[2], F.a[gi], acc[2][gi]);
      acc[3][gi] = mfma32(F.b[3], F.a[gi], acc[3][gi]);
    } else if (TA) {
      acch = mfma32(F.be, F.ah, acch);
      cs0 = mfma32(F.e0, ones, cs0); cs1 = mfma32(F.be, ones, cs1);
    } else {
      accx[0] = mfma32(F.bx, F.a[0], accx[0]); accx[1] = mfma32(F.bx, F.a[1], accx[1]);
      cs0 = mfma32(F.b[0], ones, cs0); cs1 = mfma32(F.b[1], ones, cs1); csx = mfma32(F.bx, ones, csx);
    }
  };
  constexpr int RPG = (NRD + 4) / 5;
  auto half = [&](const Frags& Fc, Frags& Fn, int so_n, int ks_n, int mode, int d0, int nd, int so_d, int bt) {
#pragma unroll
    for (int gi = 0; gi < 5; ++gi) {
#pragma unroll
      for (int i = 0; i < RPG; ++i) { const int idx = gi * RPG + i; if (idx < NRD) rdf(Fn, idx, so_n, ks_n); }
      mm(Fc, gi);
#pragma unroll
      for (int k = 0; k < 5; ++k) {
        __builtin_amdgcn_sched_group_barrier(0x8, 1, 0);
        __builtin_amdgcn_sched_group_barrier(0x100, 1, 0);
        __builtin_amdgcn_sched_group_barrier(0x2, 2, 0);
      }
      __builtin_amdgcn_sched_group_barrier(0x100, 8, 0);
      SB0();
      if (mode == 0 && gi < nd) { dma(d0 + gi, so_d); SB0(); }
      if (mode == 1 && gi < nd && d0 + gi < 6) { dmab(3 * bt + ((d0 + gi) >> 1), (d0 + gi) & 1); SB0(); }
    }
  };
  Frags F0, F1;
  WAITV(9); BAR();
  STAMP(2);
#pragma unroll
  for (int i = 0; i < NRD; ++i) rdf(F0, i, 0, 0);
#pragma unroll
  for (int i = 0; i < 3; ++i) dma(i, 2 * SLOT);
  half(F0, F1, 0, 1, 0, 3, 4, 2 * SLOT, 0);
  pf(pfp + (size_t)(4u * pfs));
  int sop = 0, so = SLOT;
#pragma unroll 1
  for (int t = 1; t < 6; ++t) {
    WAITV(9); WAITL0(); BAR();
    half(F1, F0, so, 0, 0, 0, 4, sop, 0);
    half(F0, F1, so, 1, 0, 4, 3, sop, 0);
    pf(t <= 3 ? pfp + (size_t)((unsigned)(t + 4) * pfs) : (t == 4 ? pfw : pfw + (lane < 13 ? 14 * 128 : 12 * 128)));
    sop = so; so = (so == 2 * SLOT) ? 0 : so + SLOT;
  }
  WAITV(9); WAITL0(); BAR();
  u32x4 stg;
  { int r = (w < 2) ? row0 + 64 * w + lane : (w == 2 ? row0 - 16 + (lane & 15) : row0 + lane); r = r < 0 ? 0 : r;
    asm volatile("global_load_dwordx4 %0, %1, %2 sc1" : "=v"(stg) : "v"((unsigned)(r * 16)), "s"(p.ws + WS_STATS) : "memory"); }
  half(F1, F0, 0, 0, 1, 0, 4, 0, 0);
  half(F0, F1, 0, 1, 1, 4, 3, 0, 0);
  WAITV(8); WAITL0(); BAR();
  half(F1, F0, SLOT, 0, 1, 0, 4, 0, 1);
  half(F0, F1, SLOT, 1, 1, 4, 3, 0, 1);
#pragma unroll
  for (int gi = 0; gi < 5; ++gi) mm(F1, gi);
  STAMP(4);
  {
    LAS f32x4* CSw = (LAS f32x4*)(lds + SC_CS);
    if (r16 == 0) {
      const int u0 = TA ? (mi ? 8 + fu : nj) : (mi * 8 + 2 * nj), u1 = TA ? (mi ? 8 + nj : 4 + nj) : (mi * 8 + 2 * nj + 1);
      CSw[u0 * 4 + g] = cs0; CSw[u1 * 4 + g] = cs1;
    }
    asm volatile("s_waitcnt vmcnt(12)" : "+v"(stg) :: "memory");
    if (w < 3) {
      int r = (w < 2) ? row0 + 64 * w + lane : row0 - 16 + (lane & 15); r = r < 0 ? 0 : r;
      if (!__all(stg[1] == tag && stg[3] == tag)) {
        gu64* gp = (gu64*)(p.ws + WS_STATS) + r * 2;
        for (unsigned spins = 0; spins < (1u << 20); ++spins) {
          const unsigned long long a = __hip_atomic_load(gp, RLX_AGENT), b = __hip_atomic_load(gp + 1, RLX_AGENT);
          stg = (u32x4){(unsigned)a, (unsigned)(a >> 32), (unsigned)b, (unsigned)(b >> 32)};
          if (__all(stg[1] == tag && stg[3] == tag)) break;
          __builtin_amdgcn_s_sleep(1);
        }
      }
      if (w < 2 || lane < 16) ((LAS f32x2*)(lds + SC_MR))[64 * w + lane] = (f32x2){__uint_as_float(stg[0]), __uint_as_float(stg[2])};
    }
  }
  WAITL0(); BAR();
  dmab(6, 0); dmab(6, 1); dmab(7, 0); dmab(7, 1);
  STAMP(5);
  LAS f32x4* CS = (LAS f32x4*)(lds + SC_CS);
  LAS f32x2* MR = (LAS f32x2*)(lds + SC_MR);
  const __amdgpu_buffer_rsrc_t ohr = __builtin_amdgcn_make_buffer_rsrc(p.ws + WS_OH, 0, 128 * NKS * 1024, 0x00020000);
  const unsigned ohl = lane * 16;
  if (!TA) {
    const f32x4 c0 = CS[(2 * nj) * 4 + g], c1 = CS[(2 * nj + 1) * 4 + g], c2 = CS[(8 + 2 * nj) * 4 + g], c3 = CS[(9 + 2 * nj) * 4 + g];
    const int tp = tm + 2 * nj;
    LAS char* fx = lds + SC_FX;
    { const f32x2 m0 = MR[64 * mi + 16 * nj + r16], m1 = MR[64 * mi + 16 * ((nj + 1) & 3) + r16];
      *(LAS s16x4*)(fx + ((mi * 2 + (nj & 1)) * 4 + nj) * 512 + lane * 8) = pack4(lnc(accx[0], m0, csx));
      *(LAS s16x4*)(fx + ((mi * 2 + (nj & 1)) * 4 + ((nj + 1) & 3)) * 512 + lane * 8) = pack4(lnc(accx[1], m1, csx)); }
    auto ffn = [&](const f32x4 (&G0)[4], const f32x4 (&U0)[4], const f32x4 (&G1)[4], const f32x4 (&U1)[4]) {
#pragma unroll
      for (int i = 0; i < 4; ++i) {
        const int rt = (i + nj) & 3;
        const f32x2 mr = MR[64 * mi + 16 * rt + r16];
        const u32x2 h0 = __builtin_bit_cast(u32x2, pack4(swiglu4(lnc(G0[i], mr, c0), lnc(U0[i], mr, c2))));
        const u32x2 h1 = __builtin_bit_cast(u32x2, pack4(swiglu4(lnc(G1[i], mr, c1), lnc(U1[i], mr, c3))));
        if (tp <= 85) st16(ohr, ohl + ((rg * 8 + 4 * mi + rt) * NKS + 16 + (tp >> 1)) * 1024, (u32x4){h0[0], h0[1], h1[0], h1[1]});
      }
    };
    if (mi) ffn(acc[2], acc[0], acc[3], acc[1]); else ffn(acc[0], acc[2], acc[1], acc[3]);
    WAITL0(); BAR();
    const f32x4 gx = unpack4(*(LAS s16x4*)(fx + ((mi * 2 + 0) * 4 + nj) * 512 + lane * 8)), ux = unpack4(*(LAS s16x4*)(fx + ((mi * 2 + 1) * 4 + nj) * 512 + lane * 8));
    st8(ohr, ohl + ((rg * 8 + w) * NKS + 16 + (te >> 1)) * 1024 + (te & 1) * 8, pack4(swiglu4(gx, ux)));
  } else {
    s16x4 Q[4], K[4], V[4]; u32x2 Fv[4];
    const f32x4 cq = CS[nj * 4 + g], ck = CS[(4 + nj) * 4 + g], cf = CS[(8 + fu) * 4 + g];
    const float cv = ((LAS float*)CS)[(8 + nj) * 16 + r16];
#pragma unroll
    for (int rt = 0; rt < 4; ++rt) {
      const f32x2 mr = MR[64 * mi + 16 * rt + r16];
      Q[rt] = pack4(lnc(acc[0][rt], mr, cq)); K[rt] = pack4(lnc(acc[1][rt], mr, ck)); Fv[rt] = __builtin_bit_cast(u32x2, pack4(lnc(acc[3][rt], mr, cf)));
      const f32x4 t01 = *(LAS f32x4*)(MR + 64 * mi + 16 * rt + 4 * g), t23 = *(LAS f32x4*)(MR + 64 * mi + 16 * rt + 4 * g + 2);
      const f32x4 mm4 = {t01[0], t01[2], t23[0], t23[2]}, rr4 = {t01[1], t01[3], t23[1], t23[3]};
      V[rt] = pack4((acc[2][rt] - mm4 * cv) * rr4);
    }
    LAS char* kvb = lds + SC_KV + nj * 1536;
    LAS char* fx = lds + SC_FX + w * 1024;
    const s16x4 hv = pack4(lnc(acch, MR[128 + r16], CS[((mi ? 8 : 4) + nj) * 4 + g]));
    if (mi == 0) { *(LAS s16x4*)(kvb + 512 + lane * 8) = K[3]; *(LAS s16x4*)(kvb + 1024 + lane * 8) = V[3]; }
    else *(LAS s16x4*)(kvb + r16 * 32 + g * 8) = hv;
    *(LAS u32x2*)(fx + lane * 8) = (nj & 1) ? Fv[0] : Fv[2]; *(LAS u32x2*)(fx + 512 + lane * 8) = (nj & 1) ? Fv[1] : Fv[3];
    f32x4 mkp, mkc;
#pragma unroll
    for (int r = 0; r < 4; ++r) {
      const int dp = r16 + 16 - (4 * g + r), dc = r16 - (4 * g + r);
      mkp[r] = (!(dp & 1) && dp <= 14) ? 0.f : -__builtin_inff();
      mkc[r] = (!(dc & 1) && dc >= 0 && dc <= 14) ? 0.f : -__builtin_inff();
    }
    const bool odd = r16 & 1;
    const unsigned psh = odd ? 16u : 0u;
    const int RT0 = rg * 8 + 4 * mi;
    const unsigned od = ohl + (RT0 * NKS + 2 * cg + (nj >> 1)) * 1024 + (nj & 1) * 8;
    const s16x4 ones4 = {0x3f80, 0x3f80, 0x3f80, 0x3f80};
    const bf16x8 ones8 = __builtin_bit_cast(bf16x8, __builtin_shufflevector(ones4, ones4, 0, 1, 2, 3, 4, 5, 6, 7));
    auto attn = [&](s16x4 kp, s16x4 vp, s16x4 kc, s16x4 vc, s16x4 qq, f32x4 mp, int rt) {
      const f32x4 z = {0, 0, 0, 0};
      const f32x4 sp = mfma16(kp, qq, mp), sc = mfma16(kc, qq, mkc);
      const float sc2 = 0.25f * 1.44269504089f;
      const float e0 = odd ? sp[1] : sp[0], e1 = odd ? sp[3] : sp[2], e2 = odd ? sc[1] : sc[0], e3 = odd ? sc[3] : sc[2];
      const float mx = xg_max(fmaxf(fmaxf(e0, e1), fmaxf(e2, e3))) * sc2;
      const float p0 = __builtin_amdgcn_exp2f(fmaf(e0, sc2, -mx)), p1 = __builtin_amdgcn_exp2f(fmaf(e1, sc2, -mx));
      const float p2 = __builtin_amdgcn_exp2f(fmaf(e2, sc2, -mx)), p3 = __builtin_amdgcn_exp2f(fmaf(e3, sc2, -mx));
      const u32x2 k01 = __builtin_bit_cast(u32x2, pack4((f32x4){p0, 0.f, p1, 0.f})), k23 = __builtin_bit_cast(u32x2, pack4((f32x4){p2, 0.f, p3, 0.f}));
      const u32x4 pw4 = {k01[0] << psh, k01[1] << psh, k23[0] << psh, k23[1] << psh};
      const bf16x8 pf = __builtin_bit_cast(bf16x8, pw4);
      const bf16x8 vf = __builtin_bit_cast(bf16x8, __builtin_shufflevector(vp, vc, 0, 1, 2, 3, 4, 5, 6, 7));
      const f32x4 o = mfma32(vf, pf, z), sm = mfma32(ones8, pf, z);
      st8(ohr, od + rt * NKS * 1024, pack4(o * __builtin_amdgcn_rcpf(sm[0])));
    };
#pragma unroll
    for (int rt = 1; rt < 4; ++rt) attn(K[rt - 1], V[rt - 1], K[rt], V[rt], Q[rt], mkp, rt);
    WAITL0(); BAR();
    {
      LAS char* fo = lds + SC_FX + (w ^ 1) * 1024;
      const u32x2 o0 = *(LAS u32x2*)(fo + lane * 8), o1 = *(LAS u32x2*)(fo + 512 + lane * 8);
      const int rb = (nj & 1) * 2;
      const u32x2 m0 = (nj & 1) ? Fv[2] : Fv[0], m1 = (nj & 1) ? Fv[3] : Fv[1];
      const u32x2 g0 = (nj & 1) ? o0 : m0, u0 = (nj & 1) ? m0 : o0, g1 = (nj & 1) ? o1 : m1, u1 = (nj & 1) ? m1 : o1;
      const unsigned hd = ohl + ((RT0 + rb) * NKS + 16 + cg) * 1024 + (nj >> 1) * 8;
      st8(ohr, hd, pack4(swiglu4(unpack4(__builtin_bit_cast(s16x4, g0)), unpack4(__builtin_bit_cast(s16x4, u0)))));
      st8(ohr, hd + NKS * 1024, pack4(swiglu4(unpack4(__builtin_bit_cast(s16x4, g1)), unpack4(__builtin_bit_cast(s16x4, u1)))));
    }
    s16x4 Kp, Vp;
    if (mi == 0) { Kp = hv; Vp = __builtin_amdgcn_ds_read_tr16_b64_v4i16((LAS s16x4*)(kvb + (4 * g + q) * 32 + pp * 8)); }
    else { Kp = *(LAS s16x4*)(kvb + 512 + lane * 8); Vp = *(LAS s16x4*)(kvb + 1024 + lane * 8); }
    const float ninf = -__builtin_inff();
    attn(Kp, Vp, K[0], V[0], Q[0], RT0 == 0 ? (f32x4){ninf, ninf, ninf, ninf} : mkp, 0);
  }
  STAMP(6);
  return tag;
}

constexpr int NR = (NKS + 3) / 4;
constexpr int P2D = 5;
DI void gload16(bf16x8& dst, const char* sbase, unsigned voff) { asm volatile("global_load_dwordx4 %0, %1, %2 sc1" : "=v"(dst) : "v"(voff), "s"(sbase) : "memory"); }
template <int N> DI void waitVn() { asm volatile("s_waitcnt vmcnt(%0)" :: "n"(N) : "memory"); }
constexpr int P2A = 6;
constexpr int P2NA = P2A + 1;
constexpr int p2opsA(int q) { return (q + P2A < NR) ? 2 : 0; }
constexpr int p2opsB(int q) { return (q + P2D >= 8 && q + P2D < NR) ? 2 : 0; }
constexpr int p2posA(int r) { if (r < P2A) return 2 * (r + 1); int n = 2 * P2A; for (int q = 0; q < r - P2A; ++q) n += p2opsA(q) + p2opsB(q); return n + 2; }
constexpr int p2posB(int r) { int n = 2 * P2A; for (int q = 0; q < r - P2D; ++q) n += p2opsA(q) + p2opsB(q); return n + p2opsA(r - P2D) + 2; }
constexpr int p2issued(int r) { int n = 2 * P2A; for (int q = 0; q < r; ++q) n += p2opsA(q) + p2opsB(q); return n; }
template <int R> struct P2W {
  static constexpr int need = (R + 1 >= 8 && R + 1 < NR && p2posB(R + 1) > p2posA(R)) ? p2posB(R + 1) : p2posA(R);
  static constexpr int n = p2issued(R) - need;
};

DI void phase2(const Params& p, LAS char* lds, int rt64, int ct64, unsigned tag, bool boss) {
  const int tid = threadIdx.x, lane = tid & 63;
  const int w = rfl(tid >> 6), kh = w >> 2, m4 = w & 3;
  const int r16 = lane & 15, g = lane >> 4, q = r16 >> 2, pp = lane & 3;
  const unsigned ldsb = (unsigned)(uintptr_t)lds;
  constexpr int PH = 1; STAMP(0);
  const char* pa = p.ws + WS_OH + ((size_t)(rt64 * 4 + m4) * NKS + kh) * 1024;
  const unsigned va = lane * 16;
  unsigned vb;
  { const int row = 8 * m4 + (lane >> 3), pc = lane & 7; const int c = (pc >> 1) ^ ((row >> 1) & 3);
    vb = row * 1024 + ct64 * 128 + (2 * c + (pc & 1)) * 16; }
  const char* pb2 = p.w2 + (long)(kh - 16) * 32768;
  if (w == 0) {
    gu64* fl = (gu64*)(p.ws + WS_FLAGS) + (rt64 >> 1) * 16 + (lane & 15);
    const unsigned long long want = FMAGIC | tag;
    for (unsigned spins = 0; spins < (1u << 20); ++spins) {
      const unsigned long long v = __hip_atomic_load(fl, RLX_AGENT);
      if (__all(v == want)) break;
      __builtin_amdgcn_s_sleep(1);
    }
  }
  BAR();
  STAMP(1);
  bf16x8 A[P2NA][2];
  auto issueA = [&](int r) {
#pragma unroll
    for (int j = 0; j < 2; ++j) {
      const int ke = 4 * r + 2 * j;
      const bool over = (ke + 1 > NKS - 1);
      gload16(A[r % P2NA][j], pa + (over ? (long)ke - 2 * kh : (long)ke) * 1024, va);
    }
  };
#pragma unroll
  for (int r = 0; r < P2A; ++r) { issueA(r); if (r + 1 < P2A) BAR(); }
  auto issueB = [&](int r) {
#pragma unroll
    for (int j = 0; j < 2; ++j) {
      const int ke = 4 * r + 2 * j;
      const bool over = (ke + 1 > NKS - 1);
      glds16s(pb2 + (over ? (long)ke - 2 * kh : (long)ke) * 32768, vb, ldsb + bslot(r) + (2 * j) * 4096 + kh * 4096 + m4 * 1024);
    }
  };
  int bl[4];
#pragma unroll
  for (int c = 0; c < 4; ++c) bl[c] = (4 * g + q) * 128 + ((c ^ ((2 * g + (q >> 1)) & 3)) << 5) + pp * 8 + kh * 4096;
  f32x4 acc[4] = {{0, 0, 0, 0}, {0, 0, 0, 0}, {0, 0, 0, 0}, {0, 0, 0, 0}};
  const size_t ooff = (size_t)(rt64 * 64 + 16 * m4 + r16) * 1024 + (ct64 * 64 + 32 * kh + 4 * g) * 2;
  bf16x4 xv0, xv1;
  u32x4 fl0 = {0, 0, 0, 0}, fl1 = {0, 0, 0, 0};
  bf16x8 bf[2][2][4];
  auto readB = [&](int r) {
    LAS char* sb = lds + bslot(r);
#pragma unroll
    for (int j = 0; j < 2; ++j)
#pragma unroll
      for (int c = 0; c < 4; ++c) bf[r & 1][j][c] = ldsB(sb + j * 8192 + bl[c], 2048);
  };
  readB(0);
  auto round = [&](auto RC) {
    constexpr int r = decltype(RC)::value;
    waitVn<P2W<r>::n>();
    asm volatile("" : "+v"(A[r % P2NA][0]), "+v"(A[r % P2NA][1]));
    if (r + 1 < NR && (r + 1 >= 8 || r + P2D >= 8)) BAR();
    if (r + P2A < NR) issueA(r + P2A);
    if (r + P2D >= 8 && r + P2D < NR) issueB(r + P2D);
    if (r + 1 == NR) { xv0 = *(const bf16x4*)(p.x + ooff); xv1 = *(const bf16x4*)(p.x + ooff + 32); }
    if (r == 0) STAMP(2);
    if (r == 8) STAMP(3);
    if (r == 11 && boss) {
      const char* fb = p.ws + WS_FLAGS;
      asm volatile("global_load_dwordx4 %0, %2, %3 sc1\n\tglobal_load_dwordx4 %1, %2, %3 offset:1024 sc1" : "=&v"(fl0), "=&v"(fl1) : "v"(va), "s"(fb) : "memory");
    }
    SB0();
    if (r + 1 < NR) readB(r + 1);
#pragma unroll
    for (int j = 0; j < 2; ++j) {
      if (4 * r + 2 * j + 1 > NKS - 1 && kh) continue;
#pragma unroll
      for (int c = 0; c < 4; ++c) acc[c] = mfma32(bf[r & 1][j][c], A[r % P2NA][j], acc[c]);
    }
#pragma unroll
    for (int k = 0; k < 8; ++k) {
      __builtin_amdgcn_sched_group_barrier(0x8, 1, 0);
      __builtin_amdgcn_sched_group_barrier(0x100, 2, 0);
    }
    SB0();
  };
  static_for<NR>(round);
  STAMP(4);
  {
    LAS f32x4* red = (LAS f32x4*)(lds + SC_RED);
    red[(w * 2 + 0) * 64 + lane] = kh ? acc[0] : acc[2];
    red[(w * 2 + 1) * 64 + lane] = kh ? acc[1] : acc[3];
    WAITL0(); BAR();
    const f32x4 o0 = red[((w ^ 4) * 2 + 0) * 64 + lane], o1 = red[((w ^ 4) * 2 + 1) * 64 + lane];
    const f32x4 s0 = (kh ? acc[2] : acc[0]) + o0, s1 = (kh ? acc[3] : acc[1]) + o1;
    const f32x4 x0 = __builtin_convertvector(xv0, f32x4), x1 = __builtin_convertvector(xv1, f32x4);
    *(s16x4*)(p.out + ooff) = pack4(s0 + x0);
    *(s16x4*)(p.out + ooff + 32) = pack4(s1 + x1);
  }
  STAMP(5);
  if (boss) {
    asm volatile("s_waitcnt vmcnt(0)" : "+v"(fl0), "+v"(fl1) :: "memory");
    const unsigned hi = (unsigned)(FMAGIC >> 32);
    bool ok = fl0[0] == tag && fl0[1] == hi && fl0[2] == tag && fl0[3] == hi && fl1[0] == tag && fl1[1] == hi && fl1[2] == tag && fl1[3] == hi;
    if (!__all(ok)) {
      gu64* fl = (gu64*)(p.ws + WS_FLAGS) + lane;
      const unsigned long long want = FMAGIC | tag;
      for (unsigned spins = 0; spins < (1u << 20); ++spins) {
        ok = __hip_atomic_load(fl, RLX_AGENT) == want && __hip_atomic_load(fl + 64, RLX_AGENT) == want &&
             __hip_atomic_load(fl + 128, RLX_AGENT) == want && __hip_atomic_load(fl + 192, RLX_AGENT) == want;
        if (__all(ok)) break;
        __builtin_amdgcn_s_sleep(1);
      }
    }
    if (lane == 0) __hip_atomic_store((gu32*)p.ws, tag, RLX_AGENT);
  }
}

__global__ __launch_bounds__(512, 2) void k_fused(Params p) {
  extern __shared__ __attribute__((aligned(16))) char shm[];
  LAS char* lds = (LAS char*)shm;
  const int b = blockIdx.x, xcd = b & 7, idx = b >> 3;
  const int rg = (xcd & 1) * 8 + (idx & 7), cg = (xcd >> 1) * 4 + (idx >> 3);
  const int rt64 = xcd * 4 + (idx & 3), ct64 = idx >> 2;
  const unsigned tag = (cg < 8) ? phase1<true>(p, lds, rg, cg, ct64, idx) : phase1<false>(p, lds, rg, cg - 8, ct64, idx);
  asm volatile("s_waitcnt vmcnt(0)" ::: "memory");
  BAR();
  if (threadIdx.x == 0) __hip_atomic_store((gu64*)(p.ws + WS_FLAGS) + rg * 16 + cg, FMAGIC | tag, RLX_AGENT);
  { constexpr int PH = 0; STAMP(7); }
  phase2(p, lds, rt64, ct64, tag, b == 0 && rfl(threadIdx.x >> 6) == 0);
  { constexpr int PH = 1; STAMP(6); STAMP(7); }
  if (threadIdx.x == 0 && (blockIdx.x == 0 || blockIdx.x == 7 || blockIdx.x == 130 || blockIdx.x == 255)) {
    LAS unsigned long long* st = (LAS unsigned long long*)(lds + SC_STAMP);
    const unsigned long long r0 = st[0], c0 = st[8];
    printf("b%d t0=%llu | p1 rt: %d %d %d %d %d %d cyc: %d %d %d %d %d %d | p2 rt: %d %d %d %d %d %d %d cyc %d %d %d %d\n", blockIdx.x, r0,
      (int)(st[1] - r0), (int)(st[2] - r0), (int)(st[4] - r0), (int)(st[5] - r0), (int)(st[6] - r0), (int)(st[7] - r0),
      (int)(st[9] - c0), (int)(st[10] - c0), (int)(st[12] - c0), (int)(st[13] - c0), (int)(st[14] - c0), (int)(st[15] - c0),
      (int)(st[16] - r0), (int)(st[17] - r0), (int)(st[18] - r0), (int)(st[19] - r0), (int)(st[20] - r0), (int)(st[21] - r0), (int)(st[23] - r0),
      (int)(st[25] - c0), (int)(st[26] - c0), (int)(st[28] - c0), (int)(st[31] - c0));
  }
}

extern "C" void kernel_launch(void* const* d_in, const int* in_sizes, int n_in, void* d_out, int out_size, void* d_ws, size_t ws_size,
                              hipStream_t stream) {
  Params p;
  p.x = (const char*)d_in[0]; p.wqkv = (const char*)d_in[1]; p.wo = (const char*)d_in[2];
  p.w1 = (const char*)d_in[4]; p.w3 = (const char*)d_in[5]; p.w2 = (const char*)d_in[6];
  p.ws = (char*)d_ws; p.out = (char*)d_out; p.stamps = (unsigned long long*)((char*)d_ws + (128u << 20));
  (void)hipFuncSetAttribute((const void*)k_fused, hipFuncAttributeMaxDynamicSharedMemorySize, P1_LDS);
  k_fused<<<256, 512, P1_LDS, stream>>>(p);
}
```

```cpp
#include <hip/hip_runtime.h>
#include <stdint.h>

#define LAS __attribute__((address_space(3)))
#define DI __device__ __forceinline__
typedef __bf16 bf16x8 __attribute__((ext_vector_type(8)));
typedef __bf16 bf16x4 __attribute__((ext_vector_type(4)));
typedef short s16x4 __attribute__((ext_vector_type(4)));
typedef float f32x4 __attribute__((ext_vector_type(4)));
typedef float f32x2 __attribute__((ext_vector_type(2)));
typedef unsigned u32x2 __attribute__((ext_vector_type(2)));
typedef unsigned u32x4 __attribute__((ext_vector_type(4)));

constexpr int NKS = 59;
constexpr int SLOT = 53248;
constexpr int WOFF = 16384, EOFF = 49152;
constexpr int DUMMY = 3 * SLOT;
constexpr int SC_CS = DUMMY + 1024;
constexpr int SC_MR = SC_CS + 1152;
constexpr int P1_LDS = 163840;
constexpr int SC_HS = 86016;
constexpr int SC_KV = SC_HS + 1024;
constexpr int SC_FX = SC_KV + 6144;
constexpr int SC_RED = SLOT;
__host__ __device__ constexpr int bslot(int r) { return (r & 7) < 3 ? 2 * SLOT + (r & 7) * 16384 : (r & 7) < 6 ? ((r & 7) - 3) * 16384 : SLOT + ((r & 7) - 6) * 16384; }
constexpr unsigned long long FMAGIC = 0x5AC3E17Bull << 32;
constexpr size_t WS_FLAGS = 4096, WS_STATS = 8192, WS_OH = 65536;

#define STAMP(i) do {} while (0)

template <int V> struct IC { static constexpr int value = V; };
template <int N, int I = 0, class F> DI void static_for(F& f) { if constexpr (I < N) { f(IC<I>{}); static_for<N, I + 1>(f); } }
DI int rfl(int v) { return __builtin_amdgcn_readfirstlane(v); }
DI void glds16(const void* g, unsigned lds_dst) {
  unsigned keep;
  asm volatile("s_mov_b32 %0, m0\n\ts_mov_b32 m0, %2\n\ts_nop 0\n\tglobal_load_lds_dwordx4 %1, off\n\ts_mov_b32 m0, %0"
               : "=&s"(keep) : "v"(g), "s"(lds_dst) : "memory");
}
DI void glds16s(const char* sbase, unsigned voff, unsigned lds_dst) {
  asm volatile("s_mov_b32 m0, %2\n\ts_nop 0\n\tglobal_load_lds_dwordx4 %1, %0" :: "s"(sbase), "v"(voff), "s"(lds_dst) : "memory", "m0");
}
#define WAITV(n) asm volatile("s_waitcnt vmcnt(" #n ")" ::: "memory")
#define WAITL0() do { asm volatile("" ::: "memory"); __builtin_amdgcn_s_waitcnt(0xC07F); asm volatile("" ::: "memory"); } while (0)
#define BAR() do { asm volatile("" ::: "memory"); __builtin_amdgcn_s_barrier(); asm volatile("" ::: "memory"); } while (0)
#define SB0() __builtin_amdgcn_sched_barrier(0)

DI bf16x8 ldsA(LAS char* p) { return *(LAS bf16x8*)p; }
DI bf16x8 ldsB(LAS char* p, int toff) {
  s16x4 lo = __builtin_amdgcn_ds_read_tr16_b64_v4i16((LAS s16x4*)p);
  s16x4 hi = __builtin_amdgcn_ds_read_tr16_b64_v4i16((LAS s16x4*)(p + toff));
  return __builtin_bit_cast(bf16x8, __builtin_shufflevector(lo, hi, 0, 1, 2, 3, 4, 5, 6, 7));
}
DI f32x4 mfma32(bf16x8 a, bf16x8 b, f32x4 c) { return __builtin_amdgcn_mfma_f32_16x16x32_bf16(a, b, c, 0, 0, 0); }
DI f32x4 mfma16(s16x4 a, s16x4 b, f32x4 c) { return __builtin_amdgcn_mfma_f32_16x16x16bf16_1k(a, b, c, 0, 0, 0); }
DI s16x4 pack4(f32x4 v) { return __builtin_bit_cast(s16x4, __builtin_convertvector(v, bf16x4)); }
DI f32x4 unpack4(s16x4 v) { return __builtin_convertvector(__builtin_bit_cast(bf16x4, v), f32x4); }
DI float xg_sum(float v) {
  auto r = __builtin_amdgcn_permlane16_swap(__float_as_uint(v), __float_as_uint(v), false, false);
  v = __uint_as_float(r[0]) + __uint_as_float(r[1]);
  auto s = __builtin_amdgcn_permlane32_swap(__float_as_uint(v), __float_as_uint(v), false, false);
  return __uint_as_float(s[0]) + __uint_as_float(s[1]);
}
DI float xg_max(float v) {
  auto r = __builtin_amdgcn_permlane16_swap(__float_as_uint(v), __float_as_uint(v), false, false);
  v = fmaxf(__uint_as_float(r[0]), __uint_as_float(r[1]));
  auto s = __builtin_amdgcn_permlane32_swap(__float_as_uint(v), __float_as_uint(v), false, false);
  return fmaxf(__uint_as_float(s[0]), __uint_as_float(s[1]));
}
DI float row_sum16(float v) {
  v += __builtin_bit_cast(float, __builtin_amdgcn_update_dpp(0, __builtin_bit_cast(int, v), 0x128, 0xf, 0xf, false));
  v += __builtin_bit_cast(float, __builtin_amdgcn_update_dpp(0, __builtin_bit_cast(int, v), 0x124, 0xf, 0xf, false));
  v += __builtin_bit_cast(float, __builtin_amdgcn_update_dpp(0, __builtin_bit_cast(int, v), 0x122, 0xf, 0xf, false));
  v += __builtin_bit_cast(float, __builtin_amdgcn_update_dpp(0, __builtin_bit_cast(int, v), 0x121, 0xf, 0xf, false));
  return v;
}
DI f32x4 lnc(f32x4 a, f32x2 mr, f32x4 c) { return (a - c * mr[0]) * mr[1]; }
DI f32x4 swiglu4(f32x4 g, f32x4 u) {
  const f32x4 t = g * -1.44269504089f;
  f32x4 d;
#pragma unroll
  for (int r = 0; r < 4; ++r) d[r] = __builtin_amdgcn_rcpf(1.0f + __builtin_amdgcn_exp2f(t[r]));
  return g * u * d;
}

struct Params {
  const char* x; const char* wqkv; const char* wo; const char* w1; const char* w3; const char* w2;
  char* ws; char* out; unsigned long long* stamps;
};
typedef __attribute__((address_space(1))) unsigned gu32;
typedef __attribute__((address_space(1))) unsigned long long gu64;
#define RLX_AGENT __ATOMIC_RELAXED, __HIP_MEMORY_SCOPE_AGENT
DI void st16(__amdgpu_buffer_rsrc_t r, unsigned off, u32x4 v) { __builtin_amdgcn_raw_buffer_store_b128(v, r, off, 0, 16); }
DI void st8(__amdgpu_buffer_rsrc_t r, unsigned off, s16x4 v) { __builtin_amdgcn_raw_buffer_store_b64(__builtin_bit_cast(u32x2, v), r, off, 0, 16); }

struct Frags { bf16x8 a[4], ah, b[4], bx, be, e0; };

template <bool TA>
DI unsigned phase1(const Params& p, LAS char* lds, int rg, int cg, int ct64, int idx, const char* code0) {
  const int tid = threadIdx.x, lane = tid & 63;
  const int w = rfl(tid >> 6), mi = w >> 2, nj = w & 3;
  const int r16 = lane & 15, g = lane >> 4, q = r16 >> 2, pp = lane & 3;
  constexpr int PH = 0; STAMP(0);
  const unsigned ldsb = (unsigned)(uintptr_t)lds;
  const int row0 = rg * 128;
  const int srow = row0 + (TA ? cg : 8 + cg) * 8 + w;
  unsigned ev; u32x4 xrow;
  asm volatile("global_load_dword %0, %1, %2 sc1" : "=v"(ev) : "v"(0u), "s"(p.ws) : "memory");
  asm volatile("global_load_dwordx4 %0, %1, %2" : "=v"(xrow) : "v"((unsigned)(srow * 1024 + lane * 16)), "s"(p.x) : "memory");
  const int tm = TA ? 2 * cg : 16 + 18 * (cg >> 1) + ((cg & 1) ? 10 : 0);
  const int te = 16 + 18 * (cg >> 1) + 8 + (cg & 1);
  unsigned vx[2], vw[4], ve;
#pragma unroll
  for (int m = 0; m < 2; ++m) {
    const int row = 16 * w + 8 * m + (lane >> 3);
    vx[m] = (unsigned)((row0 + row) * 1024 + (((lane & 7) ^ ((row >> 1) & 7)) << 4));
  }
  const char* bw[4]; unsigned ldw[4], dw[4];
#pragma unroll
  for (int m = 0; m < 4; ++m) {
    if (!TA || m < 2) {
      const int pi = TA ? (2 * w + m) : (2 * w + (m & 1));
      const int kr = 4 * pi + (lane >> 4), pc = lane & 15;
      const int c = (pc >> 1) ^ ((kr & 3) | (((kr >> 3) & 1) << 2));
      if (TA) { bw[m] = p.wqkv; ldw[m] = 3072; vw[m] = kr * 3072 + ((c >> 2) * 512 + (4 * cg + (c & 3)) * 16) * 2 + (pc & 1) * 16; dw[m] = WOFF + pi * 1024; }
      else { bw[m] = (m >> 1) ? p.w3 : p.w1; ldw[m] = 2752; int t = tm + c; t = t > 85 ? 85 : t; vw[m] = kr * 2752 + t * 32 + (pc & 1) * 16; dw[m] = WOFF + (m >> 1) * 16384 + pi * 1024; }
    } else if (m == 2) {
      const int kr = 8 * w + (lane >> 3), pc = lane & 7;
      const int c = (pc >> 1) ^ (((kr >> 1) & 1) | (((kr >> 3) & 1) << 1));
      bw[m] = p.wqkv; ldw[m] = 3072; vw[m] = kr * 3072 + (1024 + (4 * cg + c) * 16) * 2 + (pc & 1) * 16; dw[m] = WOFF + 16384 + w * 1024;
    } else {
      const int kr = 16 * (w & 3) + (lane >> 2), pc = lane & 3;
      const int e = (pc >> 1) ^ ((kr >> 3) & 1);
      bw[m] = mi ? p.w3 : p.w1; ldw[m] = 2752; vw[m] = kr * 2752 + (tm + e) * 32 + (pc & 1) * 16; dw[m] = WOFF + 24576 + mi * 4096 + (w & 3) * 1024;
    }
  }
  const bool has_e = TA ? (w < 2) : (w < 4);
  const char* be_; unsigned lde, de;
  if (TA) {
    const int row = 8 * (w & 1) + (lane >> 3);
    int gr = row0 - 16 + row; gr = gr < 0 ? 0 : gr;
    be_ = p.x; lde = 2; ve = gr * 1024 + (((lane & 7) ^ ((row >> 1) & 7)) << 4); de = EOFF + w * 1024;
  } else {
    const int pos = 32 * (w & 1) + (lane >> 1), kr = pos ^ (((pos >> 3) & 1) << 2);
    be_ = (w & 2) ? p.w3 : p.w1; lde = 2752; ve = kr * 2752 + te * 32 + (lane & 1) * 16; de = EOFF + (w & 3) * 1024;
  }
  if (!has_e) { be_ = p.x; lde = 0; ve = 0; }
  unsigned vb;
  { const int row = 8 * nj + (lane >> 3), pc = lane & 7; const int c = (pc >> 1) ^ ((row >> 1) & 3);
    vb = row * 1024 + ct64 * 128 + (2 * c + (pc & 1)) * 16; }
  const unsigned emask = has_e ? 0xffffffffu : 0u;
  if (!has_e) de = DUMMY;
  const char* sx = p.x; const char* se = be_; const char* sw[4] = {bw[0], bw[1], bw[2], bw[3]};
  auto dma = [&](int i, int so) {
    if (i == 0) { glds16s(se, ve, ldsb + (so & emask) + de); se += 64u * lde; }
    else if (i <= 2) { glds16s(sx, vx[i - 1], ldsb + so + (2 * w + i - 1) * 1024); if (i == 2) sx += 128; }
    else { glds16s(sw[i - 3], vw[i - 3], ldsb + so + dw[i - 3]); sw[i - 3] += 64u * ldw[i - 3]; }
  };
  auto dmab = [&](int rr, int j) {
    const int ks2 = 4 * rr + 2 * j + mi;
    const char* src = (4 * rr + 2 * j < 16) ? p.wo + ks2 * 32768 : p.w2 + (ks2 - 16) * 32768;
    glds16s(src, vb, ldsb + bslot(rr) + (2 * j + mi) * 4096 + nj * 1024);
  };
#pragma unroll
  for (int i = 0; i < 7; ++i) dma(i, 0);
#pragma unroll
  for (int i = 0; i < 7; ++i) dma(i, SLOT);
  STAMP(1);
  asm volatile("s_waitcnt vmcnt(14)" : "+v"(ev), "+v"(xrow) :: "memory");
  const unsigned tag = (unsigned)rfl((int)ev) + 1u;
  {
    float s1 = 0.f, s2 = 0.f;
#pragma unroll
    for (int i = 0; i < 4; ++i) {
      const float lo = __uint_as_float(xrow[i] << 16), hi = __uint_as_float(xrow[i] & 0xffff0000u);
      s1 += lo + hi; s2 = fmaf(lo, lo, fmaf(hi, hi, s2));
    }
    s1 = xg_sum(row_sum16(s1)); s2 = xg_sum(row_sum16(s2));
    const float m = s1 * (1.0f / 512.0f), var = fmaxf(s2 * (1.0f / 512.0f) - m * m, 0.f);
    const float rs = __builtin_amdgcn_rsqf(var + 1e-5f);
    const __amdgpu_buffer_rsrc_t str = __builtin_amdgcn_make_buffer_rsrc(p.ws + WS_STATS, 0, 2048 * 16, 0x00020000);
    if (lane == 0) st16(str, srow * 16, (u32x4){__float_as_uint(m), tag, __float_as_uint(rs), tag});
  }
  const char* pfp; unsigned pfs; const char* pfw;
  {
    const int l5 = lane & 31;
    const int L = w * 10 + (l5 < 10 ? l5 : 9);
    if (L < 32) { pfp = p.x + (size_t)(row0 + 32 * (idx >> 3) + L) * 1024; pfs = 128; }
    else {
      const int L2 = L - 32, ri = L2 / 6, sg = L2 - 6 * ri;
      const int kr = 8 * (idx & 7) + ri;
      if (TA) { pfp = (sg < 3) ? p.wqkv + (size_t)kr * 3072 + sg * 1024 + cg * 128 : (sg == 3 ? p.w1 : p.w3) + (size_t)kr * 2752 + tm * 32; pfs = (sg < 3) ? 64u * 3072 : 64u * 2752; }
      else { int cb = (tm < te ? tm : te) * 32 + (sg % 3) * 128; cb = cb > 2748 ? 2748 : cb; pfp = (sg < 3 ? p.w1 : p.w3) + (size_t)kr * 2752 + cb; pfs = 64u * 2752; }
    }
    pfw = p.w2 + (size_t)512 * 1024 + (size_t)((idx * 8 + w) * 27 + (lane < 14 ? lane : 13)) * 128;
  }
  auto pf = [&](const char* a) { asm volatile("s_mov_b32 m0, %1\n\ts_nop 0\n\tglobal_load_lds_dword %0, off" :: "v"(a), "s"(ldsb + DUMMY) : "memory", "m0"); };
  {
    const char* a = pfp + (size_t)((lane < 32 ? 2u : 3u) * pfs);
    if (lane == 10) a = code0 + (size_t)((idx * 8 + w) & 255) * 128;
    pf(a);
  }
  const int xa0 = r16 * 128 + ((g ^ (r16 >> 1)) << 4);
  const int fl = q | ((g & 1) << 2);
  const int w256 = (8 * g + q) * 256 + (fl << 5) + pp * 8;
  const int w128 = (8 * g + q) * 128 + ((((q >> 1) & 1) | ((g & 1) << 1)) << 5) + pp * 8;
  const int w64 = (8 * g + q) * 64 + ((g & 1) << 5) + pp * 8;
  const int w32 = (8 * g + ((g & 1) << 2) + q) * 32 + pp * 8;
  const int dt32 = (g & 1) ? -128 : 128;
  const int tb0 = (w256 ^ (nj << 6)) + (mi ? 16384 : 0), tb2 = (w256 ^ (nj << 6)) + (mi ? 0 : 16384);
  const int tbx = EOFF + (nj & 1) * 2048 + w32;
  const int taq = w256 ^ (nj << 5), tak = w256 ^ ((4 + nj) << 5), tav = 16384 + (w128 ^ (nj << 5)), taf = 24576 + (nj & 1) * 4096 + (w64 ^ ((nj >> 1) << 5));
  const int fu = 4 + ((nj >> 1) | ((nj & 1) << 1));
  int xr[4];
#pragma unroll
  for (int i = 0; i < 4; ++i) xr[i] = mi * 8192 + (TA ? i : ((i + nj) & 3)) * 2048;
  const int bea = mi ? tav : tak, bes = mi ? 4096 : 8192, bet = mi ? 512 : 1024;
  const int e0a = mi ? taf : taq, e0s = mi ? 2048 : 8192, e0t = mi ? 256 : 1024;
  constexpr int NRD = TA ? 11 : 9;
  auto ldsB2 = [&](LAS char* p0, LAS char* p1) {
    s16x4 lo = __builtin_amdgcn_ds_read_tr16_b64_v4i16((LAS s16x4*)p0), hi = __builtin_amdgcn_ds_read_tr16_b64_v4i16((LAS s16x4*)p1);
    return __builtin_bit_cast(bf16x8, __builtin_shufflevector(lo, hi, 0, 1, 2, 3, 4, 5, 6, 7));
  };
  auto rdf = [&](Frags& F, int i, int so, int ks) {
    LAS char* sb = lds + so;
    const int xa = xa0 ^ (ks * 64);
    LAS char* wb = sb + WOFF;
    if (TA) {
      switch (i) {
        case 0: F.a[0] = ldsA(sb + xa + xr[0]); break;
        case 1: F.b[0] = ldsB(wb + taq + ks * 8192, 1024); break;
        case 2: F.b[1] = ldsB(wb + tak + ks * 8192, 1024); break;
        case 3: F.b[2] = ldsB(wb + tav + ks * 4096, 512); break;
        case 4: F.b[3] = ldsB(wb + taf + ks * 2048, 256); break;
        case 5: F.a[1] = ldsA(sb + xa + xr[1]); break;
        case 6: F.a[2] = ldsA(sb + xa + xr[2]); break;
        case 7: F.a[3] = ldsA(sb + xa + xr[3]); break;
        case 8: F.be = ldsB2(wb + bea + ks * bes, wb + bea + ks * bes + bet); break;
        case 9: F.ah = ldsA(sb + EOFF + xa); break;
        default: F.e0 = ldsB2(wb + e0a + ks * e0s, wb + e0a + ks * e0s + e0t); break;
      }
    } else {
      switch (i) {
        case 0: F.a[0] = ldsA(sb + xa + xr[0]); break;
        case 1: F.b[0] = ldsB(wb + tb0 + ks * 8192, 1024); break;
        case 2: F.b[1] = ldsB(wb + (tb0 ^ 32) + ks * 8192, 1024); break;
        case 3: F.b[2] = ldsB(wb + tb2 + ks * 8192, 1024); break;
        case 4: F.b[3] = ldsB(wb + (tb2 ^ 32) + ks * 8192, 1024); break;
        case 5: F.a[1] = ldsA(sb + xa + xr[1]); break;
        case 6: F.a[2] = ldsA(sb + xa + xr[2]); break;
        case 7: F.a[3] = ldsA(sb + xa + xr[3]); break;
        default: F.bx = ldsB2(sb + tbx + ks * 1024, sb + tbx + ks * 1024 + dt32); break;
      }
    }
  };
  f32x4 acc[4][4], accx[2], acch = {0, 0, 0, 0}, cs0 = {0, 0, 0, 0}, cs1 = {0, 0, 0, 0}, csx = {0, 0, 0, 0};
  accx[0] = (f32x4){0, 0, 0, 0}; accx[1] = (f32x4){0, 0, 0, 0};
#pragma unroll
  for (int c = 0; c < 4; ++c)
#pragma unroll
    for (int rt = 0; rt < 4; ++rt) acc[c][rt] = (f32x4){0, 0, 0, 0};
  const bf16x8 ones = {(__bf16)1.0f, (__bf16)1.0f, (__bf16)1.0f, (__bf16)1.0f, (__bf16)1.0f, (__bf16)1.0f, (__bf16)1.0f, (__bf16)1.0f};
  auto mm = [&](const Frags& F, int gi) {
    if (gi < 4) {
      acc[0][gi] = mfma32(F.b[0], F.a[gi], acc[0][gi]);
      acc[1][gi] = mfma32(F.b[1], F.a[gi], acc[1][gi]);
      acc[2][gi] = TA ? mfma32(F.a[gi], F.b[2], acc[2][gi]) : mfma32(F.b[2], F.a[gi], acc[2][gi]);
      acc[3][gi] = mfma32(F.b[3], F.a[gi], acc[3][gi]);
    } else if (TA) {
      acch = mfma32(F.be, F.ah, acch);
      cs0 = mfma32(F.e0, ones, cs0); cs1 = mfma32(F.be, ones, cs1);
    } else {
      accx[0] = mfma32(F.bx, F.a[0], accx[0]); accx[1] = mfma32(F.bx, F.a[1], accx[1]);
      cs0 = mfma32(F.b[0], ones, cs0); cs1 = mfma32(F.b[1], ones, cs1); csx = mfma32(F.bx, ones, csx);
    }
  };
  constexpr int RPG = (NRD + 4) / 5;
  auto half = [&](const Frags& Fc, Frags& Fn, int so_n, int ks_n, int mode, int d0, int nd, int so_d, int bt) {
#pragma unroll
    for (int gi = 0; gi < 5; ++gi) {
#define PRIO 3
      if (gi == 0) { if (ks_n == 0) __builtin_amdgcn_s_setprio(3); else __builtin_amdgcn_s_setprio(1); }
      if (gi == PRIO) { if (ks_n == 0) __builtin_amdgcn_s_setprio(2); else __builtin_amdgcn_s_setprio(0); }
#pragma unroll
      for (int i = 0; i < RPG; ++i) { const int idx = gi * RPG + i; if (idx < NRD) rdf(Fn, idx, so_n, ks_n); }
      mm(Fc, gi);
#pragma unroll
      for (int k = 0; k < 5; ++k) {
        __builtin_amdgcn_sched_group_barrier(0x8, 1, 0);
        __builtin_amdgcn_sched_group_barrier(0x100, 1, 0);
        __builtin_amdgcn_sched_group_barrier(0x2, 2, 0);
      }
      __builtin_amdgcn_sched_group_barrier(0x100, 8, 0);
      SB0();
      if (mode == 0 && gi < nd) { dma(d0 + gi, so_d); SB0(); }
      if (mode == 1 && gi < nd && d0 + gi < 6) { dmab(3 * bt + ((d0 + gi) >> 1), (d0 + gi) & 1); SB0(); }
    }
  };
  Frags F0, F1;
  WAITV(9); BAR();
  STAMP(2);
#pragma unroll
  for (int i = 0; i < NRD; ++i) rdf(F0, i, 0, 0);
#pragma unroll
  for (int i = 0; i < 3; ++i) dma(i, 2 * SLOT);
  half(F0, F1, 0, 1, 0, 3, 4, 2 * SLOT, 0);
  pf(pfp + (size_t)(4u * pfs));
  int sop = 0, so = SLOT;
#pragma unroll 1
  for (int t = 1; t < 6; ++t) {
    WAITV(9); WAITL0(); BAR();
    half(F1, F0, so, 0, 0, 0, 4, sop, 0);
    half(F0, F1, so, 1, 0, 4, 3, sop, 0);
    pf(t <= 3 ? pfp + (size_t)((unsigned)(t + 4) * pfs) : (t == 4 ? pfw : pfw + (lane < 13 ? 14 * 128 : 12 * 128)));
    sop = so; so = (so == 2 * SLOT) ? 0 : so + SLOT;
  }
  WAITV(9); WAITL0(); BAR();
  u32x4 stg;
  { int r = (w < 2) ? row0 + 64 * w + lane : (w == 2 ? row0 - 16 + (lane & 15) : row0 + lane); r = r < 0 ? 0 : r;
    asm volatile("global_load_dwordx4 %0, %1, %2 sc1" : "=v"(stg) : "v"((unsigned)(r * 16)), "s"(p.ws + WS_STATS) : "memory"); }
  half(F1, F0, 0, 0, 1, 0, 4, 0, 0);
  half(F0, F1, 0, 1, 1, 4, 3, 0, 0);
  WAITV(8); WAITL0(); BAR();
  half(F1, F0, SLOT, 0, 1, 0, 4, 0, 1);
  half(F0, F1, SLOT, 1, 1, 4, 3, 0, 1);
#pragma unroll
  for (int gi = 0; gi < 5; ++gi) mm(F1, gi);
  STAMP(4);
  {
    LAS f32x4* CSw = (LAS f32x4*)(lds + SC_CS);
    if (r16 == 0) {
      const int u0 = TA ? (mi ? 8 + fu : nj) : (mi * 8 + 2 * nj), u1 = TA ? (mi ? 8 + nj : 4 + nj) : (mi * 8 + 2 * nj + 1);
      CSw[u0 * 4 + g] = cs0; CSw[u1 * 4 + g] = cs1;
    }
    asm volatile("s_waitcnt vmcnt(12)" : "+v"(stg) :: "memory");
    if (w < 3) {
      int r = (w < 2) ? row0 + 64 * w + lane : row0 - 16 + (lane & 15); r = r < 0 ? 0 : r;
      if (!__all(stg[1] == tag && stg[3] == tag)) {
        gu64* gp = (gu64*)(p.ws + WS_STATS) + r * 2;
        for (unsigned spins = 0; spins < (1u << 20); ++spins) {
          const unsigned long long a = __hip_atomic_load(gp, RLX_AGENT), b = __hip_atomic_load(gp + 1, RLX_AGENT);
          stg = (u32x4){(unsigned)a, (unsigned)(a >> 32), (unsigned)b, (unsigned)(b >> 32)};
          if (__all(stg[1] == tag && stg[3] == tag)) break;
          __builtin_amdgcn_s_sleep(1);
        }
      }
      if (w < 2 || lane < 16) ((LAS f32x2*)(lds + SC_MR))[64 * w + lane] = (f32x2){__uint_as_float(stg[0]), __uint_as_float(stg[2])};
    }
  }
  WAITL0(); BAR();
  dmab(6, 0); dmab(6, 1); dmab(7, 0); dmab(7, 1);
  STAMP(5);
  LAS f32x4* CS = (LAS f32x4*)(lds + SC_CS);
  LAS f32x2* MR = (LAS f32x2*)(lds + SC_MR);
  const __amdgpu_buffer_rsrc_t ohr = __builtin_amdgcn_make_buffer_rsrc(p.ws + WS_OH, 0, 128 * NKS * 1024, 0x00020000);
  const unsigned ohl = lane * 16;
  if (!TA) {
    const f32x4 c0 = CS[(2 * nj) * 4 + g], c1 = CS[(2 * nj + 1) * 4 + g], c2 = CS[(8 + 2 * nj) * 4 + g], c3 = CS[(9 + 2 * nj) * 4 + g];
    const int tp = tm + 2 * nj;
    LAS char* fx = lds + SC_FX;
    { const f32x2 m0 = MR[64 * mi + 16 * nj + r16], m1 = MR[64 * mi + 16 * ((nj + 1) & 3) + r16];
      *(LAS s16x4*)(fx + ((mi * 2 + (nj & 1)) * 4 + nj) * 512 + lane * 8) = pack4(lnc(accx[0], m0, csx));
      *(LAS s16x4*)(fx + ((mi * 2 + (nj & 1)) * 4 + ((nj + 1) & 3)) * 512 + lane * 8) = pack4(lnc(accx[1], m1, csx)); }
    auto ffn = [&](const f32x4 (&G0)[4], const f32x4 (&U0)[4], const f32x4 (&G1)[4], const f32x4 (&U1)[4]) {
#pragma unroll
      for (int i = 0; i < 4; ++i) {
        const int rt = (i + nj) & 3;
        const f32x2 mr = MR[64 * mi + 16 * rt + r16];
        const u32x2 h0 = __builtin_bit_cast(u32x2, pack4(swiglu4(lnc(G0[i], mr, c0), lnc(U0[i], mr, c2))));
        const u32x2 h1 = __builtin_bit_cast(u32x2, pack4(swiglu4(lnc(G1[i], mr, c1), lnc(U1[i], mr, c3))));
        if (tp <= 85) st16(ohr, ohl + ((rg * 8 + 4 * mi + rt) * NKS + 16 + (tp >> 1)) * 1024, (u32x4){h0[0], h0[1], h1[0], h1[1]});
      }
    };
    if (mi) ffn(acc[2], acc[0], acc[3], acc[1]); else ffn(acc[0], acc[2], acc[1], acc[3]);
    WAITL0(); BAR();
    const f32x4 gx = unpack4(*(LAS s16x4*)(fx + ((mi * 2 + 0) * 4 + nj) * 512 + lane * 8)), ux = unpack4(*(LAS s16x4*)(fx + ((mi * 2 + 1) * 4 + nj) * 512 + lane * 8));
    st8(ohr, ohl + ((rg * 8 + w) * NKS + 16 + (te >> 1)) * 1024 + (te & 1) * 8, pack4(swiglu4(gx, ux)));
  } else {
    s16x4 Q[4], K[4], V[4]; u32x2 Fv[4];
    const f32x4 cq = CS[nj * 4 + g], ck = CS[(4 + nj) * 4 + g], cf = CS[(8 + fu) * 4 + g];
    const float cv = ((LAS float*)CS)[(8 + nj) * 16 + r16];
#pragma unroll
    for (int rt = 0; rt < 4; ++rt) {
      const f32x2 mr = MR[64 * mi + 16 * rt + r16];
      Q[rt] = pack4(lnc(acc[0][rt], mr, cq)); K[rt] = pack4(lnc(acc[1][rt], mr, ck)); Fv[rt] = __builtin_bit_cast(u32x2, pack4(lnc(acc[3][rt], mr, cf)));
      const f32x4 t01 = *(LAS f32x4*)(MR + 64 * mi + 16 * rt + 4 * g), t23 = *(LAS f32x4*)(MR + 64 * mi + 16 * rt + 4 * g + 2);
      const f32x4 mm4 = {t01[0], t01[2], t23[0], t23[2]}, rr4 = {t01[1], t01[3], t23[1], t23[3]};
      V[rt] = pack4((acc[2][rt] - mm4 * cv) * rr4);
    }
    LAS char* kvb = lds + SC_KV + nj * 1536;
    LAS char* fx = lds + SC_FX + w * 1024;
    const s16x4 hv = pack4(lnc(acch, MR[128 + r16], CS[((mi ? 8 : 4) + nj) * 4 + g]));
    if (mi == 0) { *(LAS s16x4*)(kvb + 512 + lane * 8) = K[3]; *(LAS s16x4*)(kvb + 1024 + lane * 8) = V[3]; }
    else *(LAS s16x4*)(kvb + r16 * 32 + g * 8) = hv;
    *(LAS u32x2*)(fx + lane * 8) = (nj & 1) ? Fv[0] : Fv[2]; *(LAS u32x2*)(fx + 512 + lane * 8) = (nj & 1) ? Fv[1] : Fv[3];
    f32x4 mkp, mkc;
#pragma unroll
    for (int r = 0; r < 4; ++r) {
      const int dp = r16 + 16 - (4 * g + r), dc = r16 - (4 * g + r);
      mkp[r] = (!(dp & 1) && dp <= 14) ? 0.f : -__builtin_inff();
      mkc[r] = (!(dc & 1) && dc >= 0 && dc <= 14) ? 0.f : -__builtin_inff();
    }
    const bool odd = r16 & 1;
    const unsigned psh = odd ? 16u : 0u;
    const int RT0 = rg * 8 + 4 * mi;
    const unsigned od = ohl + (RT0 * NKS + 2 * cg + (nj >> 1)) * 1024 + (nj & 1) * 8;
    const s16x4 ones4 = {0x3f80, 0x3f80, 0x3f80, 0x3f80};
    const bf16x8 ones8 = __builtin_bit_cast(bf16x8, __builtin_shufflevector(ones4, ones4, 0, 1, 2, 3, 4, 5, 6, 7));
    auto attn = [&](s16x4 kp, s16x4 vp, s16x4 kc, s16x4 vc, s16x4 qq, f32x4 mp, int rt) {
      const f32x4 z = {0, 0, 0, 0};
      const f32x4 sp = mfma16(kp, qq, mp), sc = mfma16(kc, qq, mkc);
      const float sc2 = 0.25f * 1.44269504089f;
      const float e0 = odd ? sp[1] : sp[0], e1 = odd ? sp[3] : sp[2], e2 = odd ? sc[1] : sc[0], e3 = odd ? sc[3] : sc[2];
      const float mx = xg_max(fmaxf(fmaxf(e0, e1), fmaxf(e2, e3))) * sc2;
      const float p0 = __builtin_amdgcn_exp2f(fmaf(e0, sc2, -mx)), p1 = __builtin_amdgcn_exp2f(fmaf(e1, sc2, -mx));
      const float p2 = __builtin_amdgcn_exp2f(fmaf(e2, sc2, -mx)), p3 = __builtin_amdgcn_exp2f(fmaf(e3, sc2, -mx));
      const u32x2 k01 = __builtin_bit_cast(u32x2, pack4((f32x4){p0, 0.f, p1, 0.f})), k23 = __builtin_bit_cast(u32x2, pack4((f32x4){p2, 0.f, p3, 0.f}));
      const u32x4 pw4 = {k01[0] << psh, k01[1] << psh, k23[0] << psh, k23[1] << psh};
      const bf16x8 pf = __builtin_bit_cast(bf16x8, pw4);
      const bf16x8 vf = __builtin_bit_cast(bf16x8, __builtin_shufflevector(vp, vc, 0, 1, 2, 3, 4, 5, 6, 7));
      const f32x4 o = mfma32(vf, pf, z), sm = mfma32(ones8, pf, z);
      st8(ohr, od + rt * NKS * 1024, pack4(o * __builtin_amdgcn_rcpf(sm[0])));
    };
#pragma unroll
    for (int rt = 1; rt < 4; ++rt) attn(K[rt - 1], V[rt - 1], K[rt], V[rt], Q[rt], mkp, rt);
    WAITL0(); BAR();
    {
      LAS char* fo = lds + SC_FX + (w ^ 1) * 1024;
      const u32x2 o0 = *(LAS u32x2*)(fo + lane * 8), o1 = *(LAS u32x2*)(fo + 512 + lane * 8);
      const int rb = (nj & 1) * 2;
      const u32x2 m0 = (nj & 1) ? Fv[2] : Fv[0], m1 = (nj & 1) ? Fv[3] : Fv[1];
      const u32x2 g0 = (nj & 1) ? o0 : m0, u0 = (nj & 1) ? m0 : o0, g1 = (nj & 1) ? o1 : m1, u1 = (nj & 1) ? m1 : o1;
      const unsigned hd = ohl + ((RT0 + rb) * NKS + 16 + cg) * 1024 + (nj >> 1) * 8;
      st8(ohr, hd, pack4(swiglu4(unpack4(__builtin_bit_cast(s16x4, g0)), unpack4(__builtin_bit_cast(s16x4, u0)))));
      st8(ohr, hd + NKS * 1024, pack4(swiglu4(unpack4(__builtin_bit_cast(s16x4, g1)), unpack4(__builtin_bit_cast(s16x4, u1)))));
    }
    s16x4 Kp, Vp;
    if (mi == 0) { Kp = hv; Vp = __builtin_amdgcn_ds_read_tr16_b64_v4i16((LAS s16x4*)(kvb + (4 * g + q) * 32 + pp * 8)); }
    else { Kp = *(LAS s16x4*)(kvb + 512 + lane * 8); Vp = *(LAS s16x4*)(kvb + 1024 + lane * 8); }
    const float ninf = -__builtin_inff();
    attn(Kp, Vp, K[0], V[0], Q[0], RT0 == 0 ? (f32x4){ninf, ninf, ninf, ninf} : mkp, 0);
  }
  STAMP(6);
  return tag;
}

constexpr int NR = (NKS + 3) / 4;
DI void gload16(bf16x8& dst, const char* sbase, unsigned voff) { asm volatile("global_load_dwordx4 %0, %1, %2 sc1" : "=v"(dst) : "v"(voff), "s"(sbase) : "memory"); }
template <int N> DI void waitVn() { asm volatile("s_waitcnt vmcnt(%0)" :: "n"(N) : "memory"); }
#define P2AV 6
constexpr int P2A = P2AV;
constexpr int P2NA = P2A + 1;
constexpr int P2R = 4;
__host__ __device__ constexpr int bslot2(int r) { return r < 8 ? bslot(r) : r < 12 ? bslot(r - 8) : r == 12 ? SC_HS : bslot(r - 9); }
constexpr int p2issued(int r) { int n = 10 + 2 * P2A; for (int q = 0; q < r; ++q) n += (q + P2A < NR ? 2 : 0) + (q == 6 ? 4 : 0); return n; }
constexpr int p2posA(int r) { return r < P2A ? 10 + 2 * (r + 1) : p2issued(r - P2A) + 2; }
constexpr int p2posB14() { return p2issued(6) + 6; }
template <int R> struct P2W { static constexpr int n = p2issued(R) - ((R == 12 && p2posB14() > p2posA(R)) ? p2posB14() : p2posA(R)); };

DI void phase2(const Params& p, LAS char* lds, int rt64, int ct64, unsigned tag, bool boss) {
  const int tid = threadIdx.x, lane = tid & 63;
  const int w = rfl(tid >> 6), kh = w >> 2, m4 = w & 3;
  const int r16 = lane & 15, g = lane >> 4, q = r16 >> 2, pp = lane & 3;
  const unsigned ldsb = (unsigned)(uintptr_t)lds;
  constexpr int PH = 1; STAMP(0);
  const char* pa = p.ws + WS_OH + ((size_t)(rt64 * 4 + m4) * NKS + kh) * 1024;
  const unsigned va = lane * 16;
  unsigned vb;
  { const int row = 8 * m4 + (lane >> 3), pc = lane & 7; const int c = (pc >> 1) ^ ((row >> 1) & 3);
    vb = row * 1024 + ct64 * 128 + (2 * c + (pc & 1)) * 16; }
  const char* pb2 = p.w2 + (long)(kh - 16) * 32768;
  int bl[4];
#pragma unroll
  for (int c = 0; c < 4; ++c) bl[c] = (4 * g + q) * 128 + ((c ^ ((2 * g + (q >> 1)) & 3)) << 5) + pp * 8 + kh * 4096;
  auto readB = [&](bf16x8 (&dst)[2][4], int r) {
    LAS char* sb = lds + bslot2(r);
#pragma unroll
    for (int j = 0; j < 2; ++j)
#pragma unroll
      for (int c = 0; c < 4; ++c) dst[j][c] = ldsB(sb + j * 8192 + bl[c], 2048);
  };
  auto issueB = [&](int r) {
#pragma unroll
    for (int j = 0; j < 2; ++j) {
      const int ke = 4 * r + 2 * j;
      const bool over = (ke + 1 > NKS - 1);
      glds16s(pb2 + (over ? (long)ke - 2 * kh : (long)ke) * 32768, vb, ldsb + bslot2(r) + (2 * j) * 4096 + kh * 4096 + m4 * 1024);
    }
  };
  bf16x8 bp[P2R][2][4];
#pragma unroll
  for (int r = 0; r < P2R; ++r) readB(bp[r], r);
  WAITL0(); BAR();
#pragma unroll
  for (int r = 8; r <= 12; ++r) issueB(r);
  if (w == 0) {
    gu64* fl = (gu64*)(p.ws + WS_FLAGS) + (rt64 >> 1) * 16 + (lane & 15);
    const unsigned long long want = FMAGIC | tag;
    for (unsigned spins = 0; spins < (1u << 20); ++spins) {
      unsigned long long v;
      asm volatile("global_load_dwordx2 %0, %1, off sc1\n\ts_waitcnt vmcnt(0)" : "=v"(v) : "v"(fl) : "memory");
      if (__all(v == want)) break;
      __builtin_amdgcn_s_sleep(1);
    }
  }
  BAR();
  STAMP(1);
  bf16x8 A[P2NA][2];
  auto issueA = [&](int r) {
#pragma unroll
    for (int j = 0; j < 2; ++j) {
      const int ke = 4 * r + 2 * j;
      const bool over = (ke + 1 > NKS - 1);
      gload16(A[r % P2NA][j], pa + (over ? (long)ke - 2 * kh : (long)ke) * 1024, va);
    }
  };
#pragma unroll
  for (int r = 0; r < P2A; ++r) { issueA(r); if (r + 1 < P2A) BAR(); }
  f32x4 acc[4] = {{0, 0, 0, 0}, {0, 0, 0, 0}, {0, 0, 0, 0}, {0, 0, 0, 0}};
  const size_t ooff = (size_t)(rt64 * 64 + 16 * m4 + r16) * 1024 + (ct64 * 64 + 32 * kh + 4 * g) * 2;
  bf16x4 xv0, xv1;
  u32x4 fl0 = {0, 0, 0, 0}, fl1 = {0, 0, 0, 0};
  bf16x8 bf[2][2][4];
  auto round = [&](auto RC) {
    constexpr int r = decltype(RC)::value;
    waitVn<P2W<r>::n>();
    asm volatile("" : "+v"(A[r % P2NA][0]), "+v"(A[r % P2NA][1]));
    if (r == 6 || r == 12) BAR();
    if (r + P2A < NR) issueA(r + P2A);
    if (r == 6) { issueB(13); issueB(14); }
    if (r + 1 == NR) { xv0 = *(const bf16x4*)(p.x + ooff); xv1 = *(const bf16x4*)(p.x + ooff + 32); }
    if (r == 0) STAMP(2);
    if (r == 8) STAMP(3);
    if (r == 11 && boss) {
      const char* fb = p.ws + WS_FLAGS;
      asm volatile("global_load_dwordx4 %0, %2, %3 sc1\n\tglobal_load_dwordx4 %1, %2, %3 offset:1024 sc1" : "=&v"(fl0), "=&v"(fl1) : "v"(va), "s"(fb) : "memory");
    }
    SB0();
    if (r + 1 < NR && r + 1 >= P2R) readB(bf[(r + 1) & 1], r + 1);
#pragma unroll
    for (int j = 0; j < 2; ++j) {
      if (4 * r + 2 * j + 1 > NKS - 1 && kh) continue;
#pragma unroll
      for (int c = 0; c < 4; ++c) acc[c] = mfma32(r < P2R ? bp[r < P2R ? r : 0][j][c] : bf[r & 1][j][c], A[r % P2NA][j], acc[c]);
    }
#pragma unroll
    for (int k = 0; k < 8; ++k) {
      __builtin_amdgcn_sched_group_barrier(0x8, 1, 0);
      __builtin_amdgcn_sched_group_barrier(0x100, 2, 0);
    }
    SB0();
  };
  static_for<NR>(round);
  STAMP(4);
  {
    LAS f32x4* red = (LAS f32x4*)(lds + SC_RED);
    red[(w * 2 + 0) * 64 + lane] = kh ? acc[0] : acc[2];
    red[(w * 2 + 1) * 64 + lane] = kh ? acc[1] : acc[3];
    WAITL0(); BAR();
    const f32x4 o0 = red[((w ^ 4) * 2 + 0) * 64 + lane], o1 = red[((w ^ 4) * 2 + 1) * 64 + lane];
    const f32x4 s0 = (kh ? acc[2] : acc[0]) + o0, s1 = (kh ? acc[3] : acc[1]) + o1;
    const f32x4 x0 = __builtin_convertvector(xv0, f32x4), x1 = __builtin_convertvector(xv1, f32x4);
    *(s16x4*)(p.out + ooff) = pack4(s0 + x0);
    *(s16x4*)(p.out + ooff + 32) = pack4(s1 + x1);
  }
  STAMP(5);
  if (boss) {
    asm volatile("s_waitcnt vmcnt(0)" : "+v"(fl0), "+v"(fl1) :: "memory");
    const unsigned hi = (unsigned)(FMAGIC >> 32);
    bool ok = fl0[0] == tag && fl0[1] == hi && fl0[2] == tag && fl0[3] == hi && fl1[0] == tag && fl1[1] == hi && fl1[2] == tag && fl1[3] == hi;
    if (!__all(ok)) {
      gu64* fl = (gu64*)(p.ws + WS_FLAGS) + lane;
      const unsigned long long want = FMAGIC | tag;
      for (unsigned spins = 0; spins < (1u << 20); ++spins) {
        ok = __hip_atomic_load(fl, RLX_AGENT) == want && __hip_atomic_load(fl + 64, RLX_AGENT) == want &&
             __hip_atomic_load(fl + 128, RLX_AGENT) == want && __hip_atomic_load(fl + 192, RLX_AGENT) == want;
        if (__all(ok)) break;
        __builtin_amdgcn_s_sleep(1);
      }
    }
    if (lane == 0) __hip_atomic_store((gu32*)p.ws, tag, RLX_AGENT);
  }
}

__global__ __launch_bounds__(512, 2) void k_fused(Params p) {
  extern __shared__ __attribute__((aligned(16))) char shm[];
  LAS char* lds = (LAS char*)shm;
  const int b = blockIdx.x, xcd = b & 7, idx = b >> 3;
  const int rg = (xcd & 1) * 8 + (idx & 7), cg = (xcd >> 1) * 4 + (idx >> 3);
  const int rt64 = xcd * 4 + (idx & 3), ct64 = idx >> 2;
  const char* code0 = (const char*)(__builtin_amdgcn_s_getpc() & ~(unsigned long long)255);
  const unsigned tag = (cg < 8) ? phase1<true>(p, lds, rg, cg, ct64, idx, code0) : phase1<false>(p, lds, rg, cg - 8, ct64, idx, code0);
  asm volatile("s_waitcnt vmcnt(0)" ::: "memory");
  BAR();
  if (threadIdx.x == 0) __hip_atomic_store((gu64*)(p.ws + WS_FLAGS) + rg * 16 + cg, FMAGIC | tag, RLX_AGENT);
  { constexpr int PH = 0; STAMP(7); }
  phase2(p, lds, rt64, ct64, tag, b == 0 && rfl(threadIdx.x >> 6) == 0);
  { constexpr int PH = 1; STAMP(6); STAMP(7); }
}

extern "C" void kernel_launch(void* const* d_in, const int* in_sizes, int n_in, void* d_out, int out_size, void* d_ws, size_t ws_size,
                              hipStream_t stream) {
  Params p;
  p.x = (const char*)d_in[0]; p.wqkv = (const char*)d_in[1]; p.wo = (const char*)d_in[2];
  p.w1 = (const char*)d_in[4]; p.w3 = (const char*)d_in[5]; p.w2 = (const char*)d_in[6];
  p.ws = (char*)d_ws; p.out = (char*)d_out; p.stamps = (unsigned long long*)((char*)d_ws + (128u << 20));
  (void)hipFuncSetAttribute((const void*)k_fused, hipFuncAttributeMaxDynamicSharedMemorySize, P1_LDS);
  k_fused<<<256, 512, P1_LDS, stream>>>(p);
}
```

```cpp
#include <hip/hip_runtime.h>
#include <stdint.h>

#define LAS __attribute__((address_space(3)))
#define DI __device__ __forceinline__
typedef __bf16 bf16x8 __attribute__((ext_vector_type(8)));
typedef __bf16 bf16x4 __attribute__((ext_vector_type(4)));
typedef short s16x4 __attribute__((ext_vector_type(4)));
typedef float f32x4 __attribute__((ext_vector_type(4)));
typedef float f32x2 __attribute__((ext_vector_type(2)));
typedef unsigned u32x2 __attribute__((ext_vector_type(2)));
typedef unsigned u32x4 __attribute__((ext_vector_type(4)));

constexpr int NKS = 59;
constexpr int SLOT = 53248;
constexpr int WOFF = 16384, EOFF = 49152;
constexpr int DUMMY = 3 * SLOT;
constexpr int SC_CS = DUMMY + 1024;
constexpr int SC_MR = SC_CS + 1152;
constexpr int P1_LDS = 163840;
constexpr int SC_HS = 86016;
constexpr int SC_KV = SLOT;
constexpr int SC_FX = SC_KV + 6144;
constexpr int SC_OX = SC_FX + 16384;
constexpr int SC_RED = SLOT;
__host__ __device__ constexpr int bslot(int r) { return (r & 7) < 3 ? 2 * SLOT + (r & 7) * 16384 : (r & 7) < 6 ? ((r & 7) - 3) * 16384 : SLOT + ((r & 7) - 6) * 16384; }
__host__ __device__ constexpr int phys(int r) { return (r + 6) % 15; }
constexpr unsigned long long FMAGIC = 0x5AC3E17Bull << 32;
constexpr size_t WS_FLAGS = 4096, WS_STATS = 8192, WS_OH = 65536;

#define STAMP(i) do {} while (0)

template <int V> struct IC { static constexpr int value = V; };
template <int N, int I = 0, class F> DI void static_for(F& f) { if constexpr (I < N) { f(IC<I>{}); static_for<N, I + 1>(f); } }
DI int rfl(int v) { return __builtin_amdgcn_readfirstlane(v); }
DI void glds16(const void* g, unsigned lds_dst) {
  unsigned keep;
  asm volatile("s_mov_b32 %0, m0\n\ts_mov_b32 m0, %2\n\ts_nop 0\n\tglobal_load_lds_dwordx4 %1, off\n\ts_mov_b32 m0, %0"
               : "=&s"(keep) : "v"(g), "s"(lds_dst) : "memory");
}
DI void glds16s(const char* sbase, unsigned voff, unsigned lds_dst) {
  asm volatile("s_mov_b32 m0, %2\n\ts_nop 0\n\tglobal_load_lds_dwordx4 %1, %0" :: "s"(sbase), "v"(voff), "s"(lds_dst) : "memory", "m0");
}
#define WAITV(n) asm volatile("s_waitcnt vmcnt(" #n ")" ::: "memory")
#define WAITL0() do { asm volatile("" ::: "memory"); __builtin_amdgcn_s_waitcnt(0xC07F); asm volatile("" ::: "memory"); } while (0)
#define BAR() do { asm volatile("" ::: "memory"); __builtin_amdgcn_s_barrier(); asm volatile("" ::: "memory"); } while (0)
#define SB0() __builtin_amdgcn_sched_barrier(0)

DI bf16x8 ldsA(LAS char* p) { return *(LAS bf16x8*)p; }
DI bf16x8 ldsB(LAS char* p, int toff) {
  s16x4 lo = __builtin_amdgcn_ds_read_tr16_b64_v4i16((LAS s16x4*)p);
  s16x4 hi = __builtin_amdgcn_ds_read_tr16_b64_v4i16((LAS s16x4*)(p + toff));
  return __builtin_bit_cast(bf16x8, __builtin_shufflevector(lo, hi, 0, 1, 2, 3, 4, 5, 6, 7));
}
DI f32x4 mfma32(bf16x8 a, bf16x8 b, f32x4 c) { return __builtin_amdgcn_mfma_f32_16x16x32_bf16(a, b, c, 0, 0, 0); }
DI f32x4 mfma16(s16x4 a, s16x4 b, f32x4 c) { return __builtin_amdgcn_mfma_f32_16x16x16bf16_1k(a, b, c, 0, 0, 0); }
DI s16x4 pack4(f32x4 v) { return __builtin_bit_cast(s16x4, __builtin_convertvector(v, bf16x4)); }
DI f32x4 unpack4(s16x4 v) { return __builtin_convertvector(__builtin_bit_cast(bf16x4, v), f32x4); }
DI float xg_sum(float v) {
  auto r = __builtin_amdgcn_permlane16_swap(__float_as_uint(v), __float_as_uint(v), false, false);
  v = __uint_as_float(r[0]) + __uint_as_float(r[1]);
  auto s = __builtin_amdgcn_permlane32_swap(__float_as_uint(v), __float_as_uint(v), false, false);
  return __uint_as_float(s[0]) + __uint_as_float(s[1]);
}
DI float xg_max(float v) {
  auto r = __builtin_amdgcn_permlane16_swap(__float_as_uint(v), __float_as_uint(v), false, false);
  v = fmaxf(__uint_as_float(r[0]), __uint_as_float(r[1]));
  auto s = __builtin_amdgcn_permlane32_swap(__float_as_uint(v), __float_as_uint(v), false, false);
  return fmaxf(__uint_as_float(s[0]), __uint_as_float(s[1]));
}
DI float row_sum16(float v) {
  v += __builtin_bit_cast(float, __builtin_amdgcn_update_dpp(0, __builtin_bit_cast(int, v), 0x128, 0xf, 0xf, false));
  v += __builtin_bit_cast(float, __builtin_amdgcn_update_dpp(0, __builtin_bit_cast(int, v), 0x124, 0xf, 0xf, false));
  v += __builtin_bit_cast(float, __builtin_amdgcn_update_dpp(0, __builtin_bit_cast(int, v), 0x122, 0xf, 0xf, false));
  v += __builtin_bit_cast(float, __builtin_amdgcn_update_dpp(0, __builtin_bit_cast(int, v), 0x121, 0xf, 0xf, false));
  return v;
}
DI f32x4 lnc(f32x4 a, f32x2 mr, f32x4 c) { return (a - c * mr[0]) * mr[1]; }
DI f32x4 swiglu_ln(f32x4 ag, f32x4 au, f32x2 mr, f32x4 cg, f32x4 cu) {
  const f32x4 g = ag - cg * mr[0], u = au - cu * mr[0];
  const f32x4 t = g * (mr[1] * -1.44269504089f);
  const float ir2 = __builtin_amdgcn_rcpf(mr[1] * mr[1]);
  f32x4 e, d;
#pragma unroll
  for (int r = 0; r < 4; ++r) e[r] = __builtin_amdgcn_exp2f(t[r]);
  e = e * ir2 + ir2;
#pragma unroll
  for (int r = 0; r < 4; ++r) d[r] = __builtin_amdgcn_rcpf(e[r]);
  return g * u * d;
}
DI f32x4 swiglu4(f32x4 g, f32x4 u) {
  const f32x4 t = g * -1.44269504089f;
  f32x4 d;
#pragma unroll
  for (int r = 0; r < 4; ++r) d[r] = __builtin_amdgcn_rcpf(1.0f + __builtin_amdgcn_exp2f(t[r]));
  return g * u * d;
}

struct Params {
  const char* x; const char* wqkv; const char* wo; const char* w1; const char* w3; const char* w2;
  char* ws; char* out; unsigned long long* stamps;
};
typedef __attribute__((address_space(1))) unsigned gu32;
typedef __attribute__((address_space(1))) unsigned long long gu64;
#define RLX_AGENT __ATOMIC_RELAXED, __HIP_MEMORY_SCOPE_AGENT
DI void st16(__amdgpu_buffer_rsrc_t r, unsigned off, u32x4 v) { __builtin_amdgcn_raw_buffer_store_b128(v, r, off, 0, 16); }
DI void st8(__amdgpu_buffer_rsrc_t r, unsigned off, s16x4 v) { __builtin_amdgcn_raw_buffer_store_b64(__builtin_bit_cast(u32x2, v), r, off, 0, 16); }

struct Frags { bf16x8 a[4], ah, b[4], bx, be, e0; };

template <bool TA>
DI unsigned phase1(const Params& p, LAS char* lds, int rg, int cg, int ct64, int idx, const char* code0) {
  const int tid = threadIdx.x, lane = tid & 63;
  const int w = rfl(tid >> 6), mi = w >> 2, nj = w & 3;
  const int r16 = lane & 15, g = lane >> 4, q = r16 >> 2, pp = lane & 3;
  constexpr int PH = 0; STAMP(0);
  const unsigned ldsb = (unsigned)(uintptr_t)lds;
  const int row0 = rg * 128;
  const int srow = row0 + (TA ? cg : 8 + cg) * 8 + w;
  unsigned ev; u32x4 xrow;
  asm volatile("global_load_dword %0, %1, %2 sc1" : "=v"(ev) : "v"(0u), "s"(p.ws) : "memory");
  asm volatile("global_load_dwordx4 %0, %1, %2" : "=v"(xrow) : "v"((unsigned)(srow * 1024 + lane * 16)), "s"(p.x) : "memory");
  const int tm = TA ? 2 * cg : 16 + 18 * (cg >> 1) + ((cg & 1) ? 10 : 0);
  const int te = 16 + 18 * (cg >> 1) + 8 + (cg & 1);
  unsigned vx[2], vw[4], ve;
#pragma unroll
  for (int m = 0; m < 2; ++m) {
    const int row = 16 * w + 8 * m + (lane >> 3);
    vx[m] = (unsigned)((row0 + row) * 1024 + (((lane & 7) ^ ((row >> 1) & 7)) << 4));
  }
  const char* bw[4]; unsigned ldw[4], dw[4];
#pragma unroll
  for (int m = 0; m < 4; ++m) {
    if (!TA || m < 2) {
      const int pi = TA ? (2 * w + m) : (2 * w + (m & 1));
      const int kr = 4 * pi + (lane >> 4), pc = lane & 15;
      const int c = (pc >> 1) ^ ((kr & 3) | (((kr >> 3) & 1) << 2));
      if (TA) { bw[m] = p.wqkv; ldw[m] = 3072; vw[m] = kr * 3072 + ((c >> 2) * 512 + (4 * cg + (c & 3)) * 16) * 2 + (pc & 1) * 16; dw[m] = WOFF + pi * 1024; }
      else { bw[m] = (m >> 1) ? p.w3 : p.w1; ldw[m] = 2752; int t = tm + c; t = t > 85 ? 85 : t; vw[m] = kr * 2752 + t * 32 + (pc & 1) * 16; dw[m] = WOFF + (m >> 1) * 16384 + pi * 1024; }
    } else if (m == 2) {
      const int kr = 8 * w + (lane >> 3), pc = lane & 7;
      const int c = (pc >> 1) ^ (((kr >> 1) & 1) | (((kr >> 3) & 1) << 1));
      bw[m] = p.wqkv; ldw[m] = 3072; vw[m] = kr * 3072 + (1024 + (4 * cg + c) * 16) * 2 + (pc & 1) * 16; dw[m] = WOFF + 16384 + w * 1024;
    } else {
      const int kr = 16 * (w & 3) + (lane >> 2), pc = lane & 3;
      const int e = (pc >> 1) ^ ((kr >> 3) & 1);
      bw[m] = mi ? p.w3 : p.w1; ldw[m] = 2752; vw[m] = kr * 2752 + (tm + e) * 32 + (pc & 1) * 16; dw[m] = WOFF + 24576 + mi * 4096 + (w & 3) * 1024;
    }
  }
  const bool has_e = TA ? (w < 2) : (w < 4);
  const char* be_; unsigned lde, de;
  if (TA) {
    const int row = 8 * (w & 1) + (lane >> 3);
    int gr = row0 - 16 + row; gr = gr < 0 ? 0 : gr;
    be_ = p.x; lde = 2; ve = gr * 1024 + (((lane & 7) ^ ((row >> 1) & 7)) << 4); de = EOFF + w * 1024;
  } else {
    const int pos = 32 * (w & 1) + (lane >> 1), kr = pos ^ (((pos >> 3) & 1) << 2);
    be_ = (w & 2) ? p.w3 : p.w1; lde = 2752; ve = kr * 2752 + te * 32 + (lane & 1) * 16; de = EOFF + (w & 3) * 1024;
  }
  if (!has_e) { be_ = p.x; lde = 0; ve = 0; }
  unsigned vb;
  { const int row = 8 * nj + (lane >> 3), pc = lane & 7; const int c = (pc >> 1) ^ ((row >> 1) & 3);
    vb = row * 1024 + ct64 * 128 + (2 * c + (pc & 1)) * 16; }
  const unsigned emask = has_e ? 0xffffffffu : 0u;
  if (!has_e) de = DUMMY;
  const char* sx = p.x; const char* se = be_; const char* sw[4] = {bw[0], bw[1], bw[2], bw[3]};
  auto dma = [&](int i, int so) {
    if (i == 0) { glds16s(se, ve, ldsb + (so & emask) + de); se += 64u * lde; }
    else if (i <= 2) { glds16s(sx, vx[i - 1], ldsb + so + (2 * w + i - 1) * 1024); if (i == 2) sx += 128; }
    else { glds16s(sw[i - 3], vw[i - 3], ldsb + so + dw[i - 3]); sw[i - 3] += 64u * ldw[i - 3]; }
  };
  auto dmab = [&](int rr, int j) {
    const int ke = 4 * phys(rr) + 2 * j;
    const int ks2 = (ke + 1 > NKS - 1) ? ke : ke + mi;
    const char* src = (ke < 16) ? p.wo + ks2 * 32768 : p.w2 + (ks2 - 16) * 32768;
    glds16s(src, vb, ldsb + bslot(rr) + (2 * j + mi) * 4096 + nj * 1024);
  };
#pragma unroll
  for (int i = 0; i < 7; ++i) dma(i, 0);
#pragma unroll
  for (int i = 0; i < 7; ++i) dma(i, SLOT);
  STAMP(1);
  asm volatile("s_waitcnt vmcnt(14)" : "+v"(ev), "+v"(xrow) :: "memory");
  const unsigned tag = (unsigned)rfl((int)ev) + 1u;
  {
    float s1 = 0.f, s2 = 0.f;
#pragma unroll
    for (int i = 0; i < 4; ++i) {
      const float lo = __uint_as_float(xrow[i] << 16), hi = __uint_as_float(xrow[i] & 0xffff0000u);
      s1 += lo + hi; s2 = fmaf(lo, lo, fmaf(hi, hi, s2));
    }
    s1 = xg_sum(row_sum16(s1)); s2 = xg_sum(row_sum16(s2));
    const float m = s1 * (1.0f / 512.0f), var = fmaxf(s2 * (1.0f / 512.0f) - m * m, 0.f);
    const float rs = __builtin_amdgcn_rsqf(var + 1e-5f);
    const __amdgpu_buffer_rsrc_t str = __builtin_amdgcn_make_buffer_rsrc(p.ws + WS_STATS, 0, 2048 * 16, 0x00020000);
    if (lane == 0) st16(str, srow * 16, (u32x4){__float_as_uint(m), tag, __float_as_uint(rs), tag});
  }
  const char* pfp; unsigned pfs; const char* pfw; const char* pfw2;
  {
    const int l5 = lane & 31;
    const int L = w * 10 + (l5 < 10 ? l5 : 9);
    if (L < 32) { pfp = p.x + (size_t)(row0 + 32 * (idx >> 3) + L) * 1024; pfs = 128; }
    else {
      const int L2 = L - 32, ri = L2 / 6, sg = L2 - 6 * ri;
      const int kr = 8 * (idx & 7) + ri;
      if (TA) { pfp = (sg < 3) ? p.wqkv + (size_t)kr * 3072 + sg * 1024 + cg * 128 : (sg == 3 ? p.w1 : p.w3) + (size_t)kr * 2752 + tm * 32; pfs = (sg < 3) ? 64u * 3072 : 64u * 2752; }
      else { int cb = (tm < te ? tm : te) * 32 + (sg % 3) * 128; cb = cb > 2748 ? 2748 : cb; pfp = (sg < 3 ? p.w1 : p.w3) + (size_t)kr * 2752 + cb; pfs = 64u * 2752; }
    }
    { const int l0 = (idx * 8 + w) * 27 + (lane < 14 ? lane : 13), l1 = (idx * 8 + w) * 27 + 14 + (lane < 13 ? lane : 12);
      auto lineaddr = [&](int l) { return l < 4096 ? p.wo + (size_t)l * 128 : l < 6144 ? p.w2 + (size_t)(l - 4096) * 128 : p.w2 + (size_t)1280 * 1024 + (size_t)(l - 6144) * 128; };
      pfw = lineaddr(l0); pfw2 = lineaddr(l1); }
  }
  auto pf = [&](const char* a) { asm volatile("s_mov_b32 m0, %1\n\ts_nop 0\n\tglobal_load_lds_dword %0, off" :: "v"(a), "s"(ldsb + DUMMY) : "memory", "m0"); };
  {
    const char* a = pfp + (size_t)((lane < 32 ? 2u : 3u) * pfs);
    if (lane == 10) a = code0 + (size_t)((idx * 8 + w) & 255) * 128;
    pf(a);
  }
  const int xa0 = r16 * 128 + ((g ^ (r16 >> 1)) << 4);
  const int fl = q | ((g & 1) << 2);
  const int w256 = (8 * g + q) * 256 + (fl << 5) + pp * 8;
  const int w128 = (8 * g + q) * 128 + ((((q >> 1) & 1) | ((g & 1) << 1)) << 5) + pp * 8;
  const int w64 = (8 * g + q) * 64 + ((g & 1) << 5) + pp * 8;
  const int w32 = (8 * g + ((g & 1) << 2) + q) * 32 + pp * 8;
  const int dt32 = (g & 1) ? -128 : 128;
  const int tb0 = (w256 ^ (nj << 6)) + (mi ? 16384 : 0), tb2 = (w256 ^ (nj << 6)) + (mi ? 0 : 16384);
  const int tbx = EOFF + (nj & 1) * 2048 + w32;
  const int taq = w256 ^ (nj << 5), tak = w256 ^ ((4 + nj) << 5), tav = 16384 + (w128 ^ (nj << 5)), taf = 24576 + (nj & 1) * 4096 + (w64 ^ ((nj >> 1) << 5));
  const int fu = 4 + ((nj >> 1) | ((nj & 1) << 1));
  int xr[4];
#pragma unroll
  for (int i = 0; i < 4; ++i) xr[i] = mi * 8192 + (TA ? i : ((i + nj) & 3)) * 2048;
  const int bea = mi ? tav : tak, bes = mi ? 4096 : 8192, bet = mi ? 512 : 1024;
  const int e0a = mi ? taf : taq, e0s = mi ? 2048 : 8192, e0t = mi ? 256 : 1024;
  constexpr int NRD = TA ? 11 : 9;
  auto ldsB2 = [&](LAS char* p0, LAS char* p1) {
    s16x4 lo = __builtin_amdgcn_ds_read_tr16_b64_v4i16((LAS s16x4*)p0), hi = __builtin_amdgcn_ds_read_tr16_b64_v4i16((LAS s16x4*)p1);
    return __builtin_bit_cast(bf16x8, __builtin_shufflevector(lo, hi, 0, 1, 2, 3, 4, 5, 6, 7));
  };
  auto rdf = [&](Frags& F, int i, int so, int ks) {
    LAS char* sb = lds + so;
    const int xa = xa0 ^ (ks * 64);
    LAS char* wb = sb + WOFF;
    if (TA) {
      switch (i) {
        case 0: F.a[0] = ldsA(sb + xa + xr[0]); break;
        case 1: F.b[0] = ldsB(wb + taq + ks * 8192, 1024); break;
        case 2: F.b[1] = ldsB(wb + tak + ks * 8192, 1024); break;
        case 3: F.b[2] = ldsB(wb + tav + ks * 4096, 512); break;
        case 4: F.b[3] = ldsB(wb + taf + ks * 2048, 256); break;
        case 5: F.a[1] = ldsA(sb + xa + xr[1]); break;
        case 6: F.a[2] = ldsA(sb + xa + xr[2]); break;
        case 7: F.a[3] = ldsA(sb + xa + xr[3]); break;
        case 8: F.be = ldsB2(wb + bea + ks * bes, wb + bea + ks * bes + bet); break;
        case 9: F.ah = ldsA(sb + EOFF + xa); break;
        default: F.e0 = ldsB2(wb + e0a + ks * e0s, wb + e0a + ks * e0s + e0t); break;
      }
    } else {
      switch (i) {
        case 0: F.a[0] = ldsA(sb + xa + xr[0]); break;
        case 1: F.b[0] = ldsB(wb + tb0 + ks * 8192, 1024); break;
        case 2: F.b[1] = ldsB(wb + (tb0 ^ 32) + ks * 8192, 1024); break;
        case 3: F.b[2] = ldsB(wb + tb2 + ks * 8192, 1024); break;
        case 4: F.b[3] = ldsB(wb + (tb2 ^ 32) + ks * 8192, 1024); break;
        case 5: F.a[1] = ldsA(sb + xa + xr[1]); break;
        case 6: F.a[2] = ldsA(sb + xa + xr[2]); break;
        case 7: F.a[3] = ldsA(sb + xa + xr[3]); break;
        default: F.bx = ldsB2(sb + tbx + ks * 1024, sb + tbx + ks * 1024 + dt32); break;
      }
    }
  };
  f32x4 acc[4][4], accx[2], acch = {0, 0, 0, 0}, cs0 = {0, 0, 0, 0}, cs1 = {0, 0, 0, 0}, csx = {0, 0, 0, 0};
  accx[0] = (f32x4){0, 0, 0, 0}; accx[1] = (f32x4){0, 0, 0, 0};
#pragma unroll
  for (int c = 0; c < 4; ++c)
#pragma unroll
    for (int rt = 0; rt < 4; ++rt) acc[c][rt] = (f32x4){0, 0, 0, 0};
  const bf16x8 ones = {(__bf16)1.0f, (__bf16)1.0f, (__bf16)1.0f, (__bf16)1.0f, (__bf16)1.0f, (__bf16)1.0f, (__bf16)1.0f, (__bf16)1.0f};
  auto mm = [&](const Frags& F, int gi) {
    if (gi < 4) {
      acc[0][gi] = mfma32(F.b[0], F.a[gi], acc[0][gi]);
      acc[1][gi] = mfma32(F.b[1], F.a[gi], acc[1][gi]);
      acc[2][gi] = TA ? mfma32(F.a[gi], F.b[2], acc[2][gi]) : mfma32(F.b[2], F.a[gi], acc[2][gi]);
      acc[3][gi] = mfma32(F.b[3], F.a[gi], acc[3][gi]);
    } else if (TA) {
      acch = mfma32(F.be, F.ah, acch);
      cs0 = mfma32(F.e0, ones, cs0); cs1 = mfma32(F.be, ones, cs1);
    } else {
      accx[0] = mfma32(F.bx, F.a[0], accx[0]); accx[1] = mfma32(F.bx, F.a[1], accx[1]);
      cs0 = mfma32(F.b[0], ones, cs0); cs1 = mfma32(F.b[1], ones, cs1); csx = mfma32(F.bx, ones, csx);
    }
  };
  constexpr int RPG = (NRD + 4) / 5;
  auto half = [&](const Frags& Fc, Frags& Fn, int so_n, int ks_n, int mode, int d0, int nd, int so_d, int bt) {
#pragma unroll
    for (int gi = 0; gi < 5; ++gi) {
#define PRIO 3
      if (gi == 0) { if (ks_n == 0) __builtin_amdgcn_s_setprio(3); else __builtin_amdgcn_s_setprio(1); }
      if (gi == PRIO) { if (ks_n == 0) __builtin_amdgcn_s_setprio(2); else __builtin_amdgcn_s_setprio(0); }
#pragma unroll
      for (int i = 0; i < RPG; ++i) { const int idx = gi * RPG + i; if (idx < NRD) rdf(Fn, idx, so_n, ks_n); }
      mm(Fc, gi);
#pragma unroll
      for (int k = 0; k < 5; ++k) {
        __builtin_amdgcn_sched_group_barrier(0x8, 1, 0);
        __builtin_amdgcn_sched_group_barrier(0x100, 1, 0);
        __builtin_amdgcn_sched_group_barrier(0x2, 2, 0);
      }
      __builtin_amdgcn_sched_group_barrier(0x100, 8, 0);
      SB0();
      if (mode == 0 && gi < nd) { dma(d0 + gi, so_d); SB0(); }
      if (mode == 1 && gi < nd && d0 + gi < 6) { dmab(3 * bt + ((d0 + gi) >> 1), (d0 + gi) & 1); SB0(); }
    }
  };
  Frags F0, F1;
  WAITV(9); BAR();
  STAMP(2);
#pragma unroll
  for (int i = 0; i < NRD; ++i) rdf(F0, i, 0, 0);
#pragma unroll
  for (int i = 0; i < 3; ++i) dma(i, 2 * SLOT);
  half(F0, F1, 0, 1, 0, 3, 4, 2 * SLOT, 0);
  pf(pfp + (size_t)(4u * pfs));
  int sop = 0, so = SLOT;
#pragma unroll 1
  for (int t = 1; t < 6; ++t) {
    WAITV(9); WAITL0(); BAR();
    half(F1, F0, so, 0, 0, 0, 4, sop, 0);
    half(F0, F1, so, 1, 0, 4, 3, sop, 0);
    pf(t <= 3 ? pfp + (size_t)((unsigned)(t + 4) * pfs) : (t == 4 ? pfw : pfw2));
    sop = so; so = (so == 2 * SLOT) ? 0 : so + SLOT;
  }
  WAITV(9); WAITL0(); BAR();
  u32x4 stg;
  { int r = (w < 2) ? row0 + 64 * w + lane : (w == 2 ? row0 - 16 + (lane & 15) : row0 + lane); r = r < 0 ? 0 : r;
    asm volatile("global_load_dwordx4 %0, %1, %2 sc1" : "=v"(stg) : "v"((unsigned)(r * 16)), "s"(p.ws + WS_STATS) : "memory"); }
  half(F1, F0, 0, 0, 1, 0, 4, 0, 0);
  half(F0, F1, 0, 1, 1, 4, 3, 0, 0);
  WAITV(8); WAITL0(); BAR();
  half(F1, F0, SLOT, 0, 1, 0, 4, 0, 1);
  half(F0, F1, SLOT, 1, 1, 4, 3, 0, 1);
#pragma unroll
  for (int gi = 0; gi < 5; ++gi) mm(F1, gi);
  STAMP(4);
  {
    LAS f32x4* CSw = (LAS f32x4*)(lds + SC_CS);
    if (r16 == 0) {
      const int u0 = TA ? (mi ? 8 + fu : nj) : (mi * 8 + 2 * nj), u1 = TA ? (mi ? 8 + nj : 4 + nj) : (mi * 8 + 2 * nj + 1);
      CSw[u0 * 4 + g] = cs0; CSw[u1 * 4 + g] = cs1;
    }
    asm volatile("s_waitcnt vmcnt(12)" : "+v"(stg) :: "memory");
    if (w < 3) {
      int r = (w < 2) ? row0 + 64 * w + lane : row0 - 16 + (lane & 15); r = r < 0 ? 0 : r;
      if (!__all(stg[1] == tag && stg[3] == tag)) {
        gu64* gp = (gu64*)(p.ws + WS_STATS) + r * 2;
        for (unsigned spins = 0; spins < (1u << 20); ++spins) {
          const unsigned long long a = __hip_atomic_load(gp, RLX_AGENT), b = __hip_atomic_load(gp + 1, RLX_AGENT);
          stg = (u32x4){(unsigned)a, (unsigned)(a >> 32), (unsigned)b, (unsigned)(b >> 32)};
          if (__all(stg[1] == tag && stg[3] == tag)) break;
          __builtin_amdgcn_s_sleep(1);
        }
      }
      if (w < 2 || lane < 16) ((LAS f32x2*)(lds + SC_MR))[64 * w + lane] = (f32x2){__uint_as_float(stg[0]), __uint_as_float(stg[2])};
    }
  }
  WAITL0(); BAR();
  STAMP(5);
  LAS f32x4* CS = (LAS f32x4*)(lds + SC_CS);
  LAS f32x2* MR = (LAS f32x2*)(lds + SC_MR);
  const __amdgpu_buffer_rsrc_t ohr = __builtin_amdgcn_make_buffer_rsrc(p.ws + WS_OH, 0, 128 * NKS * 1024, 0x00020000);
  const unsigned ohl = lane * 16;
  if (!TA) {
    const f32x4 c0 = CS[(2 * nj) * 4 + g], c1 = CS[(2 * nj + 1) * 4 + g], c2 = CS[(8 + 2 * nj) * 4 + g], c3 = CS[(9 + 2 * nj) * 4 + g];
    const int tp = tm + 2 * nj;
    LAS char* fx = lds + SC_FX;
    { const f32x2 m0 = MR[64 * mi + 16 * nj + r16], m1 = MR[64 * mi + 16 * ((nj + 1) & 3) + r16];
      *(LAS s16x4*)(fx + ((mi * 2 + (nj & 1)) * 4 + nj) * 512 + lane * 8) = pack4(lnc(accx[0], m0, csx));
      *(LAS s16x4*)(fx + ((mi * 2 + (nj & 1)) * 4 + ((nj + 1) & 3)) * 512 + lane * 8) = pack4(lnc(accx[1], m1, csx)); }
    auto ffn = [&](const f32x4 (&G0)[4], const f32x4 (&U0)[4], const f32x4 (&G1)[4], const f32x4 (&U1)[4]) {
#pragma unroll
      for (int i = 0; i < 4; ++i) {
        const int rt = (i + nj) & 3;
        const f32x2 mr = MR[64 * mi + 16 * rt + r16];
        const u32x2 h0 = __builtin_bit_cast(u32x2, pack4(swiglu_ln(G0[i], U0[i], mr, c0, c2)));
        const u32x2 h1 = __builtin_bit_cast(u32x2, pack4(swiglu_ln(G1[i], U1[i], mr, c1, c3)));
        if (tp <= 85) st16(ohr, ohl + ((rg * 8 + 4 * mi + rt) * NKS + 16 + (tp >> 1)) * 1024, (u32x4){h0[0], h0[1], h1[0], h1[1]});
      }
    };
    if (mi) ffn(acc[2], acc[0], acc[3], acc[1]); else ffn(acc[0], acc[2], acc[1], acc[3]);
    WAITL0(); BAR();
    const f32x4 gx = unpack4(*(LAS s16x4*)(fx + ((mi * 2 + 0) * 4 + nj) * 512 + lane * 8)), ux = unpack4(*(LAS s16x4*)(fx + ((mi * 2 + 1) * 4 + nj) * 512 + lane * 8));
    st8(ohr, ohl + ((rg * 8 + w) * NKS + 16 + (te >> 1)) * 1024 + (te & 1) * 8, pack4(swiglu4(gx, ux)));
  } else {
    s16x4 Q[4], K[4], V[4]; u32x2 Fv[4];
    const f32x4 cq = CS[nj * 4 + g], ck = CS[(4 + nj) * 4 + g], cf = CS[(8 + fu) * 4 + g];
    const float cv = ((LAS float*)CS)[(8 + nj) * 16 + r16];
#pragma unroll
    for (int rt = 0; rt < 4; ++rt) {
      const f32x2 mr = MR[64 * mi + 16 * rt + r16];
      Q[rt] = pack4(lnc(acc[0][rt], mr, cq)); K[rt] = pack4(lnc(acc[1][rt], mr, ck)); Fv[rt] = __builtin_bit_cast(u32x2, pack4(lnc(acc[3][rt], mr, cf)));
      const f32x4 t01 = *(LAS f32x4*)(MR + 64 * mi + 16 * rt + 4 * g), t23 = *(LAS f32x4*)(MR + 64 * mi + 16 * rt + 4 * g + 2);
      const f32x4 mm4 = {t01[0], t01[2], t23[0], t23[2]}, rr4 = {t01[1], t01[3], t23[1], t23[3]};
      V[rt] = pack4((acc[2][rt] - mm4 * cv) * rr4);
    }
    LAS char* kvb = lds + SC_KV + nj * 1536;
    const s16x4 hv = pack4(lnc(acch, MR[128 + r16], CS[((mi ? 8 : 4) + nj) * 4 + g]));
    if (mi == 0) { *(LAS s16x4*)(kvb + 512 + lane * 8) = K[3]; *(LAS s16x4*)(kvb + 1024 + lane * 8) = V[3]; }
    else *(LAS s16x4*)(kvb + r16 * 32 + g * 8) = hv;
#pragma unroll
    for (int rt = 0; rt < 4; ++rt) *(LAS u32x2*)(lds + SC_FX + ((mi * 4 + rt) * 4 + nj) * 512 + lane * 8) = Fv[rt];
    f32x4 mkp, mkc;
#pragma unroll
    for (int r = 0; r < 4; ++r) {
      const int dp = r16 + 16 - (4 * g + r), dc = r16 - (4 * g + r);
      mkp[r] = (!(dp & 1) && dp <= 14) ? 0.f : -__builtin_inff();
      mkc[r] = (!(dc & 1) && dc >= 0 && dc <= 14) ? 0.f : -__builtin_inff();
    }
    const bool odd = r16 & 1;
    const unsigned psh = odd ? 16u : 0u;
    const int RT0 = rg * 8 + 4 * mi;
    const unsigned od = ohl + (RT0 * NKS + 2 * cg + (nj >> 1)) * 1024;
    const s16x4 ones4 = {0x3f80, 0x3f80, 0x3f80, 0x3f80};
    const bf16x8 ones8 = __builtin_bit_cast(bf16x8, __builtin_shufflevector(ones4, ones4, 0, 1, 2, 3, 4, 5, 6, 7));
    auto attn = [&](s16x4 kp, s16x4 vp, s16x4 kc, s16x4 vc, s16x4 qq, f32x4 mp, int rt) {
      const f32x4 z = {0, 0, 0, 0};
      const f32x4 sp = mfma16(kp, qq, mp), sc = mfma16(kc, qq, mkc);
      const float sc2 = 0.25f * 1.44269504089f;
      const float e0 = odd ? sp[1] : sp[0], e1 = odd ? sp[3] : sp[2], e2 = odd ? sc[1] : sc[0], e3 = odd ? sc[3] : sc[2];
      const float mx = xg_max(fmaxf(fmaxf(e0, e1), fmaxf(e2, e3))) * sc2;
      const float p0 = __builtin_amdgcn_exp2f(fmaf(e0, sc2, -mx)), p1 = __builtin_amdgcn_exp2f(fmaf(e1, sc2, -mx));
      const float p2 = __builtin_amdgcn_exp2f(fmaf(e2, sc2, -mx)), p3 = __builtin_amdgcn_exp2f(fmaf(e3, sc2, -mx));
      const u32x2 k01 = __builtin_bit_cast(u32x2, pack4((f32x4){p0, 0.f, p1, 0.f})), k23 = __builtin_bit_cast(u32x2, pack4((f32x4){p2, 0.f, p3, 0.f}));
      const u32x4 pw4 = {k01[0] << psh, k01[1] << psh, k23[0] << psh, k23[1] << psh};
      const bf16x8 pf = __builtin_bit_cast(bf16x8, pw4);
      const bf16x8 vf = __builtin_bit_cast(bf16x8, __builtin_shufflevector(vp, vc, 0, 1, 2, 3, 4, 5, 6, 7));
      const f32x4 o = mfma32(vf, pf, z), sm = mfma32(ones8, pf, z);
      return __builtin_bit_cast(u32x2, pack4(o * __builtin_amdgcn_rcpf(sm[0])));
    };
    u32x2 O[4];
#pragma unroll
    for (int rt = 1; rt < 4; ++rt) O[rt] = attn(K[rt - 1], V[rt - 1], K[rt], V[rt], Q[rt], mkp, rt);
    LAS char* ox = lds + SC_OX + w * 1024;
    *(LAS u32x2*)(ox + lane * 8) = (nj & 1) ? O[1] : O[3];
    if (nj & 1) *(LAS u32x2*)(ox + 512 + lane * 8) = O[2];
    WAITL0(); BAR();
    {
      LAS char* oo = lds + SC_OX + (w ^ 1) * 1024;
      const u32x2 p0 = *(LAS u32x2*)(oo + lane * 8);
      if (nj & 1) st16(ohr, od + 3 * NKS * 1024, (u32x4){p0[0], p0[1], O[3][0], O[3][1]});
      else {
        const u32x2 p1 = *(LAS u32x2*)(oo + 512 + lane * 8);
        st16(ohr, od + 1 * NKS * 1024, (u32x4){O[1][0], O[1][1], p0[0], p0[1]});
        st16(ohr, od + 2 * NKS * 1024, (u32x4){O[2][0], O[2][1], p1[0], p1[1]});
      }
    }
    {
      LAS char* fo = lds + SC_FX + (mi * 4 + nj) * 2048 + lane * 8;
      const s16x4 g0 = *(LAS s16x4*)fo, u0 = *(LAS s16x4*)(fo + 512), g1 = *(LAS s16x4*)(fo + 1024), u1 = *(LAS s16x4*)(fo + 1536);
      const u32x2 h0 = __builtin_bit_cast(u32x2, pack4(swiglu4(unpack4(g0), unpack4(u0)))), h1 = __builtin_bit_cast(u32x2, pack4(swiglu4(unpack4(g1), unpack4(u1))));
      st16(ohr, ohl + ((RT0 + nj) * NKS + 16 + cg) * 1024, (u32x4){h0[0], h0[1], h1[0], h1[1]});
    }
    s16x4 Kp, Vp;
    if (mi == 0) { Kp = hv; Vp = __builtin_amdgcn_ds_read_tr16_b64_v4i16((LAS s16x4*)(kvb + (4 * g + q) * 32 + pp * 8)); }
    else { Kp = *(LAS s16x4*)(kvb + 512 + lane * 8); Vp = *(LAS s16x4*)(kvb + 1024 + lane * 8); }
    const float ninf = -__builtin_inff();
    O[0] = attn(Kp, Vp, K[0], V[0], Q[0], RT0 == 0 ? (f32x4){ninf, ninf, ninf, ninf} : mkp, 0);
    st8(ohr, od + (nj & 1) * 8, __builtin_bit_cast(s16x4, O[0]));
  }
  STAMP(6);
  return tag;
}

constexpr int NR = (NKS + 3) / 4;
#define SC1 " sc1"
DI void gload16(u32x4& dst, const char* sbase, unsigned voff) { asm volatile("global_load_dwordx4 %0, %1, %2" SC1 : "=v"(dst) : "v"(voff), "s"(sbase) : "memory"); }
DI void gload8(u32x2& dst, const char* sbase, unsigned voff) { asm volatile("global_load_dwordx2 %0, %1, %2" SC1 : "=v"(dst) : "v"(voff), "s"(sbase) : "memory"); }
template <int N> DI void waitVn() { asm volatile("s_waitcnt vmcnt(%0)" :: "n"(N) : "memory"); }
#define P2AV 6
constexpr int P2A = P2AV;
constexpr int P2NA = P2A + 1;
constexpr int P2R = 4;
__host__ __device__ constexpr int bslot2(int r) { return r < 8 ? bslot(r) : r < 12 ? bslot(r - 8) : r == 12 ? SC_HS : bslot(r - 9); }
constexpr bool ksplit(int ks) { return false; }
constexpr bool psplit(int ke) { return ksplit(ke) || ksplit(ke + 1); }
constexpr int nA(int r) { return (psplit(4 * phys(r)) ? 2 : 1) + (psplit(4 * phys(r) + 2) ? 2 : 1); }
constexpr int p2issued(int r) { int n = 14; for (int q = 0; q < P2A; ++q) n += nA(q); for (int q = 0; q < r; ++q) n += (q + P2A < NR ? nA(q + P2A) : 0) + (q == 6 ? 4 : 0); return n; }
constexpr int p2posA(int r) { if (r < P2A) { int n = 14; for (int q = 0; q <= r; ++q) n += nA(q); return n; } return p2issued(r - P2A) + nA(r); }
constexpr int p2posB14() { return p2issued(6) + nA(6 + P2A) + 4; }
template <int R> struct P2W { static constexpr int n = p2issued(R) - ((R == 12 && p2posB14() > p2posA(R)) ? p2posB14() : p2posA(R)); };

DI void phase2(const Params& p, LAS char* lds, int rt64, int ct64, unsigned tag, bool boss) {
  const int tid = threadIdx.x, lane = tid & 63;
  const int w = rfl(tid >> 6), kh = w >> 2, m4 = w & 3;
  const int r16 = lane & 15, g = lane >> 4, q = r16 >> 2, pp = lane & 3;
  const unsigned ldsb = (unsigned)(uintptr_t)lds;
  constexpr int PH = 1; STAMP(0);
  const char* pa = p.ws + WS_OH + ((size_t)(rt64 * 4 + m4) * NKS + kh) * 1024;
  const unsigned va = lane * 16;
  unsigned vb;
  { const int row = 8 * m4 + (lane >> 3), pc = lane & 7; const int c = (pc >> 1) ^ ((row >> 1) & 3);
    vb = row * 1024 + ct64 * 128 + (2 * c + (pc & 1)) * 16; }
  const char* pb2 = p.w2 + (long)(kh - 16) * 32768;
  const char* pbo = p.wo + (long)kh * 32768;
  int bl[4];
#pragma unroll
  for (int c = 0; c < 4; ++c) bl[c] = (4 * g + q) * 128 + ((c ^ ((2 * g + (q >> 1)) & 3)) << 5) + pp * 8 + kh * 4096;
  auto readB = [&](bf16x8 (&dst)[2][4], int r) {
    LAS char* sb = lds + bslot2(r);
#pragma unroll
    for (int j = 0; j < 2; ++j)
#pragma unroll
      for (int c = 0; c < 4; ++c) dst[j][c] = ldsB(sb + j * 8192 + bl[c], 2048);
  };
  auto issueB = [&](int r) {
#pragma unroll
    for (int j = 0; j < 2; ++j) {
      const int ke = 4 * phys(r) + 2 * j;
      const bool over = (ke + 1 > NKS - 1);
      glds16s((ke < 16 ? pbo : pb2) + (over ? (long)ke - kh : (long)ke) * 32768, vb, ldsb + bslot2(r) + (2 * j) * 4096 + kh * 4096 + m4 * 1024);
    }
  };
  bf16x8 bp[P2R][2][4];
#pragma unroll
  for (int r = 0; r < P2R; ++r) readB(bp[r], r);
  WAITL0(); BAR();
#pragma unroll
  for (int r = 6; r <= 12; ++r) issueB(r);
  if (w == 0) {
    gu64* fl = (gu64*)(p.ws + WS_FLAGS) + (rt64 >> 1) * 16 + (lane & 15);
    const unsigned long long want = FMAGIC | tag;
    for (unsigned spins = 0; spins < (1u << 20); ++spins) {
      unsigned long long v;
      asm volatile("global_load_dwordx2 %0, %1, off sc1\n\ts_waitcnt vmcnt(0)" : "=v"(v) : "v"(fl) : "memory");
      if (__all(v == want)) break;
      __builtin_amdgcn_s_sleep(1);
    }
  }
  BAR();
  STAMP(1);
  u32x4 A4[P2NA][2]; u32x2 Al[P2NA][2], Ah[P2NA][2];
  const unsigned vs0 = lane * 8, vs1 = lane * 8 + 512, vn1 = lane * 16 + 8;
  auto issueA = [&](int r) {
#pragma unroll
    for (int j = 0; j < 2; ++j) {
      const int ke = 4 * phys(r) + 2 * j;
      const bool over = (ke + 1 > NKS - 1);
      const char* src = pa + (over ? (long)ke - kh : (long)ke) * 1024;
      if (!psplit(ke)) gload16(A4[r % P2NA][j], src, va);
      else if (ksplit(ke) && ksplit(ke + 1)) { gload8(Al[r % P2NA][j], src, vs0); gload8(Ah[r % P2NA][j], src, vs1); }
      else { const bool sp = ksplit(ke) ? (kh == 0) : (kh == 1); gload8(Al[r % P2NA][j], src, sp ? vs0 : va); gload8(Ah[r % P2NA][j], src, sp ? vs1 : vn1); }
    }
  };
  auto pinA = [&](int r) {
#pragma unroll
    for (int j = 0; j < 2; ++j) {
      if (psplit(4 * phys(r) + 2 * j)) asm volatile("" : "+v"(Al[r % P2NA][j]), "+v"(Ah[r % P2NA][j])); else asm volatile("" : "+v"(A4[r % P2NA][j]));
    }
  };
  auto getA = [&](int r, int j) {
    return psplit(4 * phys(r) + 2 * j) ? __builtin_bit_cast(bf16x8, __builtin_shufflevector(Al[r % P2NA][j], Ah[r % P2NA][j], 0, 1, 2, 3)) : __builtin_bit_cast(bf16x8, A4[r % P2NA][j]);
  };
#pragma unroll
  for (int r = 0; r < P2A; ++r) { issueA(r); if (r + 1 < P2A) BAR(); }
  f32x4 acc[4] = {{0, 0, 0, 0}, {0, 0, 0, 0}, {0, 0, 0, 0}, {0, 0, 0, 0}};
  const size_t ooff = (size_t)(rt64 * 64 + 16 * m4 + r16) * 1024 + (ct64 * 64 + 32 * kh + 4 * g) * 2;
  bf16x4 xv0, xv1;
  u32x4 fl0 = {0, 0, 0, 0}, fl1 = {0, 0, 0, 0};
  bf16x8 bf[2][2][4];
  auto round = [&](auto RC) {
    constexpr int r = decltype(RC)::value;
    waitVn<P2W<r>::n>();
    pinA(r);
    if (r == 5 || r == 6 || r == 12) BAR();
    if (r + P2A < NR) issueA(r + P2A);
    if (r == 6) { issueB(13); issueB(14); }
    if (r + 1 == NR) { xv0 = *(const bf16x4*)(p.x + ooff); xv1 = *(const bf16x4*)(p.x + ooff + 32); }
    if (r == 0) STAMP(2);
    if (r == 8) STAMP(3);
    if (r == 11 && boss) {
      const char* fb = p.ws + WS_FLAGS;
      asm volatile("global_load_dwordx4 %0, %2, %3 sc1\n\tglobal_load_dwordx4 %1, %2, %3 offset:1024 sc1" : "=&v"(fl0), "=&v"(fl1) : "v"(va), "s"(fb) : "memory");
    }
    SB0();
    if (r + 1 < NR && r + 1 >= P2R) readB(bf[(r + 1) & 1], r + 1);
#pragma unroll
    for (int j = 0; j < 2; ++j) {
      if (4 * phys(r) + 2 * j + 1 > NKS - 1 && kh) continue;
#pragma unroll
      for (int c = 0; c < 4; ++c) acc[c] = mfma32(r < P2R ? bp[r < P2R ? r : 0][j][c] : bf[r & 1][j][c], getA(r, j), acc[c]);
    }
#pragma unroll
    for (int k = 0; k < 8; ++k) {
      __builtin_amdgcn_sched_group_barrier(0x8, 1, 0);
      __builtin_amdgcn_sched_group_barrier(0x100, 2, 0);
    }
    SB0();
  };
  static_for<NR>(round);
  STAMP(4);
  {
    LAS f32x4* red = (LAS f32x4*)(lds + SC_RED);
    red[(w * 2 + 0) * 64 + lane] = kh ? acc[0] : acc[2];
    red[(w * 2 + 1) * 64 + lane] = kh ? acc[1] : acc[3];
    WAITL0(); BAR();
    const f32x4 o0 = red[((w ^ 4) * 2 + 0) * 64 + lane], o1 = red[((w ^ 4) * 2 + 1) * 64 + lane];
    const f32x4 s0 = (kh ? acc[2] : acc[0]) + o0, s1 = (kh ? acc[3] : acc[1]) + o1;
    const f32x4 x0 = __builtin_convertvector(xv0, f32x4), x1 = __builtin_convertvector(xv1, f32x4);
    const u32x2 pa2 = __builtin_bit_cast(u32x2, pack4(s0 + x0)), pb2_ = __builtin_bit_cast(u32x2, pack4(s1 + x1));
    const auto d0 = __builtin_amdgcn_permlane16_swap(pa2[0], pb2_[0], false, false), d1 = __builtin_amdgcn_permlane16_swap(pa2[1], pb2_[1], false, false);
    const __amdgpu_buffer_rsrc_t outr = __builtin_amdgcn_make_buffer_rsrc(p.out, 0, 2048 * 1024, 0x00020000);
    st16(outr, (unsigned)((rt64 * 64 + 16 * m4 + r16) * 1024 + (ct64 * 64 + 32 * kh + (g & 1) * 16 + (g >> 1) * 8) * 2), (u32x4){d0[0], d1[0], d0[1], d1[1]});
  }
  STAMP(5);
  if (boss) {
    asm volatile("s_waitcnt vmcnt(0)" : "+v"(fl0), "+v"(fl1) :: "memory");
    const unsigned hi = (unsigned)(FMAGIC >> 32);
    bool ok = fl0[0] == tag && fl0[1] == hi && fl0[2] == tag && fl0[3] == hi && fl1[0] == tag && fl1[1] == hi && fl1[2] == tag && fl1[3] == hi;
    if (!__all(ok)) {
      gu64* fl = (gu64*)(p.ws + WS_FLAGS) + lane;
      const unsigned long long want = FMAGIC | tag;
      for (unsigned spins = 0; spins < (1u << 20); ++spins) {
        ok = __hip_atomic_load(fl, RLX_AGENT) == want && __hip_atomic_load(fl + 64, RLX_AGENT) == want &&
             __hip_atomic_load(fl + 128, RLX_AGENT) == want && __hip_atomic_load(fl + 192, RLX_AGENT) == want;
        if (__all(ok)) break;
        __builtin_amdgcn_s_sleep(1);
      }
    }
    if (lane == 0) __hip_atomic_store((gu32*)p.ws, tag, RLX_AGENT);
  }
}

__global__ __launch_bounds__(512, 2) void k_fused(Params p) {
  extern __shared__ __attribute__((aligned(16))) char shm[];
  LAS char* lds = (LAS char*)shm;
  const int b = blockIdx.x, xcd = b & 7, idx = b >> 3;
  const int rg = (xcd & 1) * 8 + (idx & 7), cg = (xcd >> 1) * 4 + (idx >> 3);
  const int rt64 = xcd * 4 + (idx & 3), ct64 = idx >> 2;
  const char* code0 = (const char*)(__builtin_amdgcn_s_getpc() & ~(unsigned long long)255);
  const unsigned tag = (cg < 8) ? phase1<true>(p, lds, rg, cg, ct64, idx, code0) : phase1<false>(p, lds, rg, cg - 8, ct64, idx, code0);
  asm volatile("s_waitcnt vmcnt(0)" ::: "memory");
  BAR();
  if (threadIdx.x == 0) __hip_atomic_store((gu64*)(p.ws + WS_FLAGS) + rg * 16 + cg, FMAGIC | tag, RLX_AGENT);
  { constexpr int PH = 0; STAMP(7); }
  phase2(p, lds, rt64, ct64, tag, b == 0 && rfl(threadIdx.x >> 6) == 0);
  { constexpr int PH = 1; STAMP(6); STAMP(7); }
}

extern "C" void kernel_launch(void* const* d_in, const int* in_sizes, int n_in, void* d_out, int out_size, void* d_ws, size_t ws_size,
                              hipStream_t stream) {
  Params p;
  p.x = (const char*)d_in[0]; p.wqkv = (const char*)d_in[1]; p.wo = (const char*)d_in[2];
  p.w1 = (const char*)d_in[4]; p.w3 = (const char*)d_in[5]; p.w2 = (const char*)d_in[6];
  p.ws = (char*)d_ws; p.out = (char*)d_out; p.stamps = (unsigned long long*)((char*)d_ws + (128u << 20));
  (void)hipFuncSetAttribute((const void*)k_fused, hipFuncAttributeMaxDynamicSharedMemorySize, P1_LDS);
  k_fused<<<256, 512, P1_LDS, stream>>>(p);
}
```

```cpp
#include <hip/hip_runtime.h>
#include <stdint.h>

#define LAS __attribute__((address_space(3)))
#define DI __device__ __forceinline__
typedef __bf16 bf16x8 __attribute__((ext_vector_type(8)));
typedef __bf16 bf16x4 __attribute__((ext_vector_type(4)));
typedef short s16x4 __attribute__((ext_vector_type(4)));
typedef float f32x4 __attribute__((ext_vector_type(4)));
typedef float f32x2 __attribute__((ext_vector_type(2)));
typedef unsigned u32x2 __attribute__((ext_vector_type(2)));
typedef unsigned u32x4 __attribute__((ext_vector_type(4)));

constexpr int NKS = 59;
constexpr int SLOT = 53248;
constexpr int WOFF = 16384, EOFF = 49152;
constexpr int DUMMY = 3 * SLOT;
constexpr int SC_CS = DUMMY + 1024;
constexpr int SC_MR = SC_CS + 1152;
constexpr int P1_LDS = 163840;
constexpr int SC_HS = 86016;
constexpr int SC_KV = SLOT;
constexpr int SC_FX = SC_KV + 6144;
constexpr int SC_OX = SC_FX + 16384;
constexpr int SC_RED = SLOT;
__host__ __device__ constexpr int bslot(int r) { return (r & 7) < 3 ? 2 * SLOT + (r & 7) * 16384 : (r & 7) < 6 ? ((r & 7) - 3) * 16384 : SLOT + ((r & 7) - 6) * 16384; }
__host__ __device__ constexpr int phys(int r) { return (r + 6) % 15; }
constexpr unsigned long long FMAGIC = 0x5AC3E17Bull << 32;
constexpr size_t WS_FLAGS = 4096, WS_STATS = 8192, WS_OH = 65536;

#define STAMP(i) do {} while (0)

template <int V> struct IC { static constexpr int value = V; };
template <int N, int I = 0, class F> DI void static_for(F& f) { if constexpr (I < N) { f(IC<I>{}); static_for<N, I + 1>(f); } }
DI int rfl(int v) { return __builtin_amdgcn_readfirstlane(v); }
DI void glds16(const void* g, unsigned lds_dst) {
  unsigned keep;
  asm volatile("s_mov_b32 %0, m0\n\ts_mov_b32 m0, %2\n\ts_nop 0\n\tglobal_load_lds_dwordx4 %1, off\n\ts_mov_b32 m0, %0"
               : "=&s"(keep) : "v"(g), "s"(lds_dst) : "memory");
}
DI void glds16s(const char* sbase, unsigned voff, unsigned lds_dst) {
  asm volatile("s_mov_b32 m0, %2\n\ts_nop 0\n\tglobal_load_lds_dwordx4 %1, %0" :: "s"(sbase), "v"(voff), "s"(lds_dst) : "memory", "m0");
}
#define WAITV(n) asm volatile("s_waitcnt vmcnt(" #n ")" ::: "memory")
#define WAITL0() do { asm volatile("" ::: "memory"); __builtin_amdgcn_s_waitcnt(0xC07F); asm volatile("" ::: "memory"); } while (0)
#define BAR() do { asm volatile("" ::: "memory"); __builtin_amdgcn_s_barrier(); asm volatile("" ::: "memory"); } while (0)
#define SB0() __builtin_amdgcn_sched_barrier(0)

DI bf16x8 ldsA(LAS char* p) { return *(LAS bf16x8*)p; }
DI bf16x8 ldsB(LAS char* p, int toff) {
  s16x4 lo = __builtin_amdgcn_ds_read_tr16_b64_v4i16((LAS s16x4*)p);
  s16x4 hi = __builtin_amdgcn_ds_read_tr16_b64_v4i16((LAS s16x4*)(p + toff));
  return __builtin_bit_cast(bf16x8, __builtin_shufflevector(lo, hi, 0, 1, 2, 3, 4, 5, 6, 7));
}
DI f32x4 mfma32(bf16x8 a, bf16x8 b, f32x4 c) { return __builtin_amdgcn_mfma_f32_16x16x32_bf16(a, b, c, 0, 0, 0); }
DI f32x4 mfma16(s16x4 a, s16x4 b, f32x4 c) { return __builtin_amdgcn_mfma_f32_16x16x16bf16_1k(a, b, c, 0, 0, 0); }
DI s16x4 pack4(f32x4 v) { return __builtin_bit_cast(s16x4, __builtin_convertvector(v, bf16x4)); }
DI f32x4 unpack4(s16x4 v) { return __builtin_convertvector(__builtin_bit_cast(bf16x4, v), f32x4); }
DI float xg_sum(float v) {
  auto r = __builtin_amdgcn_permlane16_swap(__float_as_uint(v), __float_as_uint(v), false, false);
  v = __uint_as_float(r[0]) + __uint_as_float(r[1]);
  auto s = __builtin_amdgcn_permlane32_swap(__float_as_uint(v), __float_as_uint(v), false, false);
  return __uint_as_float(s[0]) + __uint_as_float(s[1]);
}
DI float xg_max(float v) {
  auto r = __builtin_amdgcn_permlane16_swap(__float_as_uint(v), __float_as_uint(v), false, false);
  v = fmaxf(__uint_as_float(r[0]), __uint_as_float(r[1]));
  auto s = __builtin_amdgcn_permlane32_swap(__float_as_uint(v), __float_as_uint(v), false, false);
  return fmaxf(__uint_as_float(s[0]), __uint_as_float(s[1]));
}
DI float row_sum16(float v) {
  v += __builtin_bit_cast(float, __builtin_amdgcn_update_dpp(0, __builtin_bit_cast(int, v), 0x128, 0xf, 0xf, false));
  v += __builtin_bit_cast(float, __builtin_amdgcn_update_dpp(0, __builtin_bit_cast(int, v), 0x124, 0xf, 0xf, false));
  v += __builtin_bit_cast(float, __builtin_amdgcn_update_dpp(0, __builtin_bit_cast(int, v), 0x122, 0xf, 0xf, false));
  v += __builtin_bit_cast(float, __builtin_amdgcn_update_dpp(0, __builtin_bit_cast(int, v), 0x121, 0xf, 0xf, false));
  return v;
}
DI f32x4 lnc(f32x4 a, f32x2 mr, f32x4 c) { return (a - c * mr[0]) * mr[1]; }
DI f32x4 swiglu_ln(f32x4 ag, f32x4 au, f32x2 mr, f32x4 cg, f32x4 cu) {
  const f32x4 g = ag - cg * mr[0], u = au - cu * mr[0];
  const f32x4 t = g * (mr[1] * -1.44269504089f);
  const float ir2 = __builtin_amdgcn_rcpf(mr[1] * mr[1]);
  f32x4 e, d;
#pragma unroll
  for (int r = 0; r < 4; ++r) e[r] = __builtin_amdgcn_exp2f(t[r]);
  e = e * ir2 + ir2;
#pragma unroll
  for (int r = 0; r < 4; ++r) d[r] = __builtin_amdgcn_rcpf(e[r]);
  return g * u * d;
}
DI f32x4 swiglu4(f32x4 g, f32x4 u) {
  const f32x4 t = g * -1.44269504089f;
  f32x4 d;
#pragma unroll
  for (int r = 0; r < 4; ++r) d[r] = __builtin_amdgcn_rcpf(1.0f + __builtin_amdgcn_exp2f(t[r]));
  return g * u * d;
}

struct Params {
  const char* x; const char* wqkv; const char* wo; const char* w1; const char* w3; const char* w2;
  char* ws; char* out; unsigned long long* stamps;
};
typedef __attribute__((address_space(1))) unsigned gu32;
typedef __attribute__((address_space(1))) unsigned long long gu64;
#define RLX_AGENT __ATOMIC_RELAXED, __HIP_MEMORY_SCOPE_AGENT
DI void st16(__amdgpu_buffer_rsrc_t r, unsigned off, u32x4 v) { __builtin_amdgcn_raw_buffer_store_b128(v, r, off, 0, 16); }
DI void st8(__amdgpu_buffer_rsrc_t r, unsigned off, s16x4 v) { __builtin_amdgcn_raw_buffer_store_b64(__builtin_bit_cast(u32x2, v), r, off, 0, 16); }

struct Frags { bf16x8 a[4], ah, b[4], bx, be, e0; };

template <bool TA>
DI unsigned phase1(const Params& p, LAS char* lds, int rg, int cg, int ct64, int idx, const char* code0) {
  const int tid = threadIdx.x, lane = tid & 63;
  const int w = rfl(tid >> 6), mi = w >> 2, nj = w & 3;
  const int r16 = lane & 15, g = lane >> 4, q = r16 >> 2, pp = lane & 3;
  constexpr int PH = 0; STAMP(0);
  const unsigned ldsb = (unsigned)(uintptr_t)lds;
  const int row0 = rg * 128;
  const int srow = row0 + (TA ? cg : 8 + cg) * 8 + w;
  unsigned ev; u32x4 xrow;
  asm volatile("global_load_dword %0, %1, %2 sc1" : "=v"(ev) : "v"(0u), "s"(p.ws) : "memory");
  asm volatile("global_load_dwordx4 %0, %1, %2" : "=v"(xrow) : "v"((unsigned)(srow * 1024 + lane * 16)), "s"(p.x) : "memory");
  const int tm = TA ? 2 * cg : 16 + 18 * (cg >> 1) + ((cg & 1) ? 10 : 0);
  const int te = 16 + 18 * (cg >> 1) + 8 + (cg & 1);
  unsigned vx[2], vw[4], ve;
#pragma unroll
  for (int m = 0; m < 2; ++m) {
    const int row = 16 * w + 8 * m + (lane >> 3);
    vx[m] = (unsigned)((row0 + row) * 1024 + (((lane & 7) ^ ((row >> 1) & 7)) << 4));
  }
  const char* bw[4]; unsigned ldw[4], dw[4];
#pragma unroll
  for (int m = 0; m < 4; ++m) {
    if (!TA || m < 2) {
      const int pi = TA ? (2 * w + m) : (2 * w + (m & 1));
      const int kr = 4 * pi + (lane >> 4), pc = lane & 15;
      const int c = (pc >> 1) ^ ((kr & 3) | (((kr >> 3) & 1) << 2));
      if (TA) { bw[m] = p.wqkv; ldw[m] = 3072; vw[m] = kr * 3072 + ((c >> 2) * 512 + (4 * cg + (c & 3)) * 16) * 2 + (pc & 1) * 16; dw[m] = WOFF + pi * 1024; }
      else { bw[m] = (m >> 1) ? p.w3 : p.w1; ldw[m] = 2752; int t = tm + c; t = t > 85 ? 85 : t; vw[m] = kr * 2752 + t * 32 + (pc & 1) * 16; dw[m] = WOFF + (m >> 1) * 16384 + pi * 1024; }
    } else if (m == 2) {
      const int kr = 8 * w + (lane >> 3), pc = lane & 7;
      const int c = (pc >> 1) ^ (((kr >> 1) & 1) | (((kr >> 3) & 1) << 1));
      bw[m] = p.wqkv; ldw[m] = 3072; vw[m] = kr * 3072 + (1024 + (4 * cg + c) * 16) * 2 + (pc & 1) * 16; dw[m] = WOFF + 16384 + w * 1024;
    } else {
      const int kr = 16 * (w & 3) + (lane >> 2), pc = lane & 3;
      const int e = (pc >> 1) ^ ((kr >> 3) & 1);
      bw[m] = mi ? p.w3 : p.w1; ldw[m] = 2752; vw[m] = kr * 2752 + (tm + e) * 32 + (pc & 1) * 16; dw[m] = WOFF + 24576 + mi * 4096 + (w & 3) * 1024;
    }
  }
  const bool has_e = TA ? (w < 2) : (w < 4);
  const char* be_; unsigned lde, de;
  if (TA) {
    const int row = 8 * (w & 1) + (lane >> 3);
    int gr = row0 - 16 + row; gr = gr < 0 ? 0 : gr;
    be_ = p.x; lde = 2; ve = gr * 1024 + (((lane & 7) ^ ((row >> 1) & 7)) << 4); de = EOFF + w * 1024;
  } else {
    const int pos = 32 * (w & 1) + (lane >> 1), kr = pos ^ (((pos >> 3) & 1) << 2);
    be_ = (w & 2) ? p.w3 : p.w1; lde = 2752; ve = kr * 2752 + te * 32 + (lane & 1) * 16; de = EOFF + (w & 3) * 1024;
  }
  if (!has_e) { be_ = p.x; lde = 0; ve = 0; }
  unsigned vb;
  { const int row = 8 * nj + (lane >> 3), pc = lane & 7; const int c = (pc >> 1) ^ ((row >> 1) & 3);
    vb = row * 1024 + ct64 * 128 + (2 * c + (pc & 1)) * 16; }
  const unsigned emask = has_e ? 0xffffffffu : 0u;
  if (!has_e) de = DUMMY;
  const char* sx = p.x; const char* se = be_; const char* sw[4] = {bw[0], bw[1], bw[2], bw[3]};
  auto dma = [&](int i, int so) {
    if (i == 0) { glds16s(se, ve, ldsb + (so & emask) + de); se += 64u * lde; }
    else if (i <= 2) { glds16s(sx, vx[i - 1], ldsb + so + (2 * w + i - 1) * 1024); if (i == 2) sx += 128; }
    else { glds16s(sw[i - 3], vw[i - 3], ldsb + so + dw[i - 3]); sw[i - 3] += 64u * ldw[i - 3]; }
  };
  auto dmab = [&](int rr, int j) {
    const int ke = 4 * phys(rr) + 2 * j;
    const int ks2 = (ke + 1 > NKS - 1) ? ke : ke + mi;
    const char* src = (ke < 16) ? p.wo + ks2 * 32768 : p.w2 + (ks2 - 16) * 32768;
    glds16s(src, vb, ldsb + bslot(rr) + (2 * j + mi) * 4096 + nj * 1024);
  };
#pragma unroll
  for (int i = 0; i < 7; ++i) dma(i, 0);
#pragma unroll
  for (int i = 0; i < 7; ++i) dma(i, SLOT);
  STAMP(1);
  const char* pfp; unsigned pfs; const char* pfw; const char* pfw2;
  {
    const int l5 = lane & 31;
    const int L = w * 10 + (l5 < 10 ? l5 : 9);
    if (L < 32) { pfp = p.x + (size_t)(row0 + 32 * (idx >> 3) + L) * 1024; pfs = 128; }
    else {
      const int L2 = L - 32, ri = L2 / 6, sg = L2 - 6 * ri;
      const int kr = 8 * (idx & 7) + ri;
      if (TA) { pfp = (sg < 3) ? p.wqkv + (size_t)kr * 3072 + sg * 1024 + cg * 128 : (sg == 3 ? p.w1 : p.w3) + (size_t)kr * 2752 + tm * 32; pfs = (sg < 3) ? 64u * 3072 : 64u * 2752; }
      else { int cb = (tm < te ? tm : te) * 32 + (sg % 3) * 128; cb = cb > 2748 ? 2748 : cb; pfp = (sg < 3 ? p.w1 : p.w3) + (size_t)kr * 2752 + cb; pfs = 64u * 2752; }
    }
    { const int l0 = (idx * 8 + w) * 27 + (lane < 14 ? lane : 13), l1 = (idx * 8 + w) * 27 + 14 + (lane < 13 ? lane : 12);
      auto lineaddr = [&](int l) { return l < 4096 ? p.wo + (size_t)l * 128 : l < 6144 ? p.w2 + (size_t)(l - 4096) * 128 : p.w2 + (size_t)1280 * 1024 + (size_t)(l - 6144) * 128; };
      pfw = lineaddr(l0); pfw2 = lineaddr(l1); }
  }
  auto pf = [&](const char* a) { asm volatile("s_mov_b32 m0, %1\n\ts_nop 0\n\tglobal_load_lds_dword %0, off" :: "v"(a), "s"(ldsb + DUMMY) : "memory", "m0"); };
  {
    const char* a = pfp + (size_t)((lane < 32 ? 2u : 3u) * pfs);
    if (lane == 10) a = code0 + (size_t)((idx * 8 + w) & 255) * 128;
    pf(a);
  }
  const int xa0 = r16 * 128 + ((g ^ (r16 >> 1)) << 4);
  const int fl = q | ((g & 1) << 2);
  const int w256 = (8 * g + q) * 256 + (fl << 5) + pp * 8;
  const int w128 = (8 * g + q) * 128 + ((((q >> 1) & 1) | ((g & 1) << 1)) << 5) + pp * 8;
  const int w64 = (8 * g + q) * 64 + ((g & 1) << 5) + pp * 8;
  const int w32 = (8 * g + ((g & 1) << 2) + q) * 32 + pp * 8;
  const int dt32 = (g & 1) ? -128 : 128;
  const int tb0 = (w256 ^ (nj << 6)) + (mi ? 16384 : 0), tb2 = (w256 ^ (nj << 6)) + (mi ? 0 : 16384);
  const int tbx = EOFF + (nj & 1) * 2048 + w32;
  const int taq = w256 ^ (nj << 5), tak = w256 ^ ((4 + nj) << 5), tav = 16384 + (w128 ^ (nj << 5)), taf = 24576 + (nj & 1) * 4096 + (w64 ^ ((nj >> 1) << 5));
  const int fu = 4 + ((nj >> 1) | ((nj & 1) << 1));
  int xr[4];
#pragma unroll
  for (int i = 0; i < 4; ++i) xr[i] = mi * 8192 + (TA ? i : ((i + nj) & 3)) * 2048;
  const int bea = mi ? tav : tak, bes = mi ? 4096 : 8192, bet = mi ? 512 : 1024;
  const int e0a = mi ? taf : taq, e0s = mi ? 2048 : 8192, e0t = mi ? 256 : 1024;
  constexpr int NRD = TA ? 11 : 9;
  auto ldsB2 = [&](LAS char* p0, LAS char* p1) {
    s16x4 lo = __builtin_amdgcn_ds_read_tr16_b64_v4i16((LAS s16x4*)p0), hi = __builtin_amdgcn_ds_read_tr16_b64_v4i16((LAS s16x4*)p1);
    return __builtin_bit_cast(bf16x8, __builtin_shufflevector(lo, hi, 0, 1, 2, 3, 4, 5, 6, 7));
  };
  auto rdf = [&](Frags& F, int i, int so, int ks) {
    LAS char* sb = lds + so;
    const int xa = xa0 ^ (ks * 64);
    LAS char* wb = sb + WOFF;
    if (TA) {
      switch (i) {
        case 0: F.a[0] = ldsA(sb + xa + xr[0]); break;
        case 1: F.b[0] = ldsB(wb + taq + ks * 8192, 1024); break;
        case 2: F.b[1] = ldsB(wb + tak + ks * 8192, 1024); break;
        case 3: F.b[2] = ldsB(wb + tav + ks * 4096, 512); break;
        case 4: F.b[3] = ldsB(wb + taf + ks * 2048, 256); break;
        case 5: F.a[1] = ldsA(sb + xa + xr[1]); break;
        case 6: F.a[2] = ldsA(sb + xa + xr[2]); break;
        case 7: F.a[3] = ldsA(sb + xa + xr[3]); break;
        case 8: F.be = ldsB2(wb + bea + ks * bes, wb + bea + ks * bes + bet); break;
        case 9: F.ah = ldsA(sb + EOFF + xa); break;
        default: F.e0 = ldsB2(wb + e0a + ks * e0s, wb + e0a + ks * e0s + e0t); break;
      }
    } else {
      switch (i) {
        case 0: F.a[0] = ldsA(sb + xa + xr[0]); break;
        case 1: F.b[0] = ldsB(wb + tb0 + ks * 8192, 1024); break;
        case 2: F.b[1] = ldsB(wb + (tb0 ^ 32) + ks * 8192, 1024); break;
        case 3: F.b[2] = ldsB(wb + tb2 + ks * 8192, 1024); break;
        case 4: F.b[3] = ldsB(wb + (tb2 ^ 32) + ks * 8192, 1024); break;
        case 5: F.a[1] = ldsA(sb + xa + xr[1]); break;
        case 6: F.a[2] = ldsA(sb + xa + xr[2]); break;
        case 7: F.a[3] = ldsA(sb + xa + xr[3]); break;
        default: F.bx = ldsB2(sb + tbx + ks * 1024, sb + tbx + ks * 1024 + dt32); break;
      }
    }
  };
  f32x4 acc[4][4], accx[2], acch = {0, 0, 0, 0}, cs0 = {0, 0, 0, 0}, cs1 = {0, 0, 0, 0}, csx = {0, 0, 0, 0};
  accx[0] = (f32x4){0, 0, 0, 0}; accx[1] = (f32x4){0, 0, 0, 0};
#pragma unroll
  for (int c = 0; c < 4; ++c)
#pragma unroll
    for (int rt = 0; rt < 4; ++rt) acc[c][rt] = (f32x4){0, 0, 0, 0};
  const bf16x8 ones = {(__bf16)1.0f, (__bf16)1.0f, (__bf16)1.0f, (__bf16)1.0f, (__bf16)1.0f, (__bf16)1.0f, (__bf16)1.0f, (__bf16)1.0f};
  auto mm = [&](const Frags& F, int gi) {
    if (gi < 4) {
      acc[0][gi] = mfma32(F.b[0], F.a[gi], acc[0][gi]);
      acc[1][gi] = mfma32(F.b[1], F.a[gi], acc[1][gi]);
      acc[2][gi] = TA ? mfma32(F.a[gi], F.b[2], acc[2][gi]) : mfma32(F.b[2], F.a[gi], acc[2][gi]);
      acc[3][gi] = mfma32(F.b[3], F.a[gi], acc[3][gi]);
    } else if (TA) {
      acch = mfma32(F.be, F.ah, acch);
      cs0 = mfma32(F.e0, ones, cs0); cs1 = mfma32(F.be, ones, cs1);
    } else {
      accx[0] = mfma32(F.bx, F.a[0], accx[0]); accx[1] = mfma32(F.bx, F.a[1], accx[1]);
      cs0 = mfma32(F.b[0], ones, cs0); cs1 = mfma32(F.b[1], ones, cs1); csx = mfma32(F.bx, ones, csx);
    }
  };
  constexpr int RPG = (NRD + 4) / 5;
  auto half = [&](const Frags& Fc, Frags& Fn, int so_n, int ks_n, int mode, int d0, int nd, int so_d, int bt) {
#pragma unroll
    for (int gi = 0; gi < 5; ++gi) {
#define PRIO 3
      if (gi == 0) { if (ks_n == 0) __builtin_amdgcn_s_setprio(3); else __builtin_amdgcn_s_setprio(1); }
      if (gi == PRIO) { if (ks_n == 0) __builtin_amdgcn_s_setprio(2); else __builtin_amdgcn_s_setprio(0); }
#pragma unroll
      for (int i = 0; i < RPG; ++i) { const int idx = gi * RPG + i; if (idx < NRD) rdf(Fn, idx, so_n, ks_n); }
      mm(Fc, gi);
#pragma unroll
      for (int k = 0; k < 5; ++k) {
        __builtin_amdgcn_sched_group_barrier(0x8, 1, 0);
        __builtin_amdgcn_sched_group_barrier(0x100, 1, 0);
        __builtin_amdgcn_sched_group_barrier(0x2, 2, 0);
      }
      __builtin_amdgcn_sched_group_barrier(0x100, 8, 0);
      SB0();
      if (mode == 0 && gi < nd) { dma(d0 + gi, so_d); SB0(); }
      if (mode == 1 && gi < nd && d0 + gi < 6) { dmab(3 * bt + ((d0 + gi) >> 1), (d0 + gi) & 1); SB0(); }
    }
  };
  Frags F0, F1;
  WAITV(8); BAR();
  STAMP(2);
#pragma unroll
  for (int i = 0; i < NRD; ++i) rdf(F0, i, 0, 0);
#pragma unroll
  for (int i = 0; i < 3; ++i) dma(i, 2 * SLOT);
  half(F0, F1, 0, 1, 0, 3, 4, 2 * SLOT, 0);
  pf(pfp + (size_t)(4u * pfs));
  asm volatile("" : "+v"(ev), "+v"(xrow));
  const unsigned tag = (unsigned)rfl((int)ev) + 1u;
  {
    float s1 = 0.f, s2 = 0.f;
#pragma unroll
    for (int i = 0; i < 4; ++i) {
      const float lo = __uint_as_float(xrow[i] << 16), hi = __uint_as_float(xrow[i] & 0xffff0000u);
      s1 += lo + hi; s2 = fmaf(lo, lo, fmaf(hi, hi, s2));
    }
    s1 = xg_sum(row_sum16(s1)); s2 = xg_sum(row_sum16(s2));
    const float m = s1 * (1.0f / 512.0f), var = fmaxf(s2 * (1.0f / 512.0f) - m * m, 0.f);
    const float rs = __builtin_amdgcn_rsqf(var + 1e-5f);
    const __amdgpu_buffer_rsrc_t str = __builtin_amdgcn_make_buffer_rsrc(p.ws + WS_STATS, 0, 2048 * 16, 0x00020000);
    if (lane == 0) st16(str, srow * 16, (u32x4){__float_as_uint(m), tag, __float_as_uint(rs), tag});
  }
  int sop = 0, so = SLOT;
#pragma unroll 1
  for (int t = 1; t < 6; ++t) {
    WAITV(9); WAITL0(); BAR();
    half(F1, F0, so, 0, 0, 0, 4, sop, 0);
    half(F0, F1, so, 1, 0, 4, 3, sop, 0);
    pf(t <= 3 ? pfp + (size_t)((unsigned)(t + 4) * pfs) : (t == 4 ? pfw : pfw2));
    sop = so; so = (so == 2 * SLOT) ? 0 : so + SLOT;
  }
  WAITV(9); WAITL0(); BAR();
  u32x4 stg;
  { int r = (w < 2) ? row0 + 64 * w + lane : (w == 2 ? row0 - 16 + (lane & 15) : row0 + lane); r = r < 0 ? 0 : r;
    asm volatile("global_load_dwordx4 %0, %1, %2 sc1" : "=v"(stg) : "v"((unsigned)(r * 16)), "s"(p.ws + WS_STATS) : "memory"); }
  half(F1, F0, 0, 0, 1, 0, 4, 0, 0);
  half(F0, F1, 0, 1, 1, 4, 3, 0, 0);
  WAITV(8); WAITL0(); BAR();
  half(F1, F0, SLOT, 0, 1, 0, 4, 0, 1);
  half(F0, F1, SLOT, 1, 1, 4, 3, 0, 1);
#pragma unroll
  for (int gi = 0; gi < 5; ++gi) mm(F1, gi);
  STAMP(4);
  {
    LAS f32x4* CSw = (LAS f32x4*)(lds + SC_CS);
    if (r16 == 0) {
      const int u0 = TA ? (mi ? 8 + fu : nj) : (mi * 8 + 2 * nj), u1 = TA ? (mi ? 8 + nj : 4 + nj) : (mi * 8 + 2 * nj + 1);
      CSw[u0 * 4 + g] = cs0; CSw[u1 * 4 + g] = cs1;
    }
    asm volatile("s_waitcnt vmcnt(12)" : "+v"(stg) :: "memory");
    if (w < 3) {
      int r = (w < 2) ? row0 + 64 * w + lane : row0 - 16 + (lane & 15); r = r < 0 ? 0 : r;
      if (!__all(stg[1] == tag && stg[3] == tag)) {
        gu64* gp = (gu64*)(p.ws + WS_STATS) + r * 2;
        for (unsigned spins = 0; spins < (1u << 20); ++spins) {
          const unsigned long long a = __hip_atomic_load(gp, RLX_AGENT), b = __hip_atomic_load(gp + 1, RLX_AGENT);
          stg = (u32x4){(unsigned)a, (unsigned)(a >> 32), (unsigned)b, (unsigned)(b >> 32)};
          if (__all(stg[1] == tag && stg[3] == tag)) break;
          __builtin_amdgcn_s_sleep(1);
        }
      }
      if (w < 2 || lane < 16) ((LAS f32x2*)(lds + SC_MR))[64 * w + lane] = (f32x2){__uint_as_float(stg[0]), __uint_as_float(stg[2])};
    }
  }
  WAITL0(); BAR();
  STAMP(5);
  LAS f32x4* CS = (LAS f32x4*)(lds + SC_CS);
  LAS f32x2* MR = (LAS f32x2*)(lds + SC_MR);
  const __amdgpu_buffer_rsrc_t ohr = __builtin_amdgcn_make_buffer_rsrc(p.ws + WS_OH, 0, 128 * NKS * 1024, 0x00020000);
  const unsigned ohl = lane * 16;
  if (!TA) {
    const f32x4 c0 = CS[(2 * nj) * 4 + g], c1 = CS[(2 * nj + 1) * 4 + g], c2 = CS[(8 + 2 * nj) * 4 + g], c3 = CS[(9 + 2 * nj) * 4 + g];
    const int tp = tm + 2 * nj;
    LAS char* fx = lds + SC_FX;
    { const f32x2 m0 = MR[64 * mi + 16 * nj + r16], m1 = MR[64 * mi + 16 * ((nj + 1) & 3) + r16];
      *(LAS s16x4*)(fx + ((mi * 2 + (nj & 1)) * 4 + nj) * 512 + lane * 8) = pack4(lnc(accx[0], m0, csx));
      *(LAS s16x4*)(fx + ((mi * 2 + (nj & 1)) * 4 + ((nj + 1) & 3)) * 512 + lane * 8) = pack4(lnc(accx[1], m1, csx)); }
    auto ffn = [&](const f32x4 (&G0)[4], const f32x4 (&U0)[4], const f32x4 (&G1)[4], const f32x4 (&U1)[4]) {
#pragma unroll
      for (int i = 0; i < 4; ++i) {
        const int rt = (i + nj) & 3;
        const f32x2 mr = MR[64 * mi + 16 * rt + r16];
        const u32x2 h0 = __builtin_bit_cast(u32x2, pack4(swiglu_ln(G0[i], U0[i], mr, c0, c2)));
        const u32x2 h1 = __builtin_bit_cast(u32x2, pack4(swiglu_ln(G1[i], U1[i], mr, c1, c3)));
        if (tp <= 85) st16(ohr, ohl + ((rg * 8 + 4 * mi + rt) * NKS + 16 + (tp >> 1)) * 1024, (u32x4){h0[0], h0[1], h1[0], h1[1]});
      }
    };
    if (mi) ffn(acc[2], acc[0], acc[3], acc[1]); else ffn(acc[0], acc[2], acc[1], acc[3]);
    WAITL0(); BAR();
    const f32x4 gx = unpack4(*(LAS s16x4*)(fx + ((mi * 2 + 0) * 4 + nj) * 512 + lane * 8)), ux = unpack4(*(LAS s16x4*)(fx + ((mi * 2 + 1) * 4 + nj) * 512 + lane * 8));
    st8(ohr, ohl + ((rg * 8 + w) * NKS + 16 + (te >> 1)) * 1024 + (te & 1) * 8, pack4(swiglu4(gx, ux)));
  } else {
    s16x4 Q[4], K[4], V[4]; u32x2 Fv[4];
    const f32x4 cq = CS[nj * 4 + g], ck = CS[(4 + nj) * 4 + g], cf = CS[(8 + fu) * 4 + g];
    const float cv = ((LAS float*)CS)[(8 + nj) * 16 + r16];
#pragma unroll
    for (int rt = 0; rt < 4; ++rt) {
      const f32x2 mr = MR[64 * mi + 16 * rt + r16];
      Q[rt] = pack4(lnc(acc[0][rt], mr, cq)); K[rt] = pack4(lnc(acc[1][rt], mr, ck)); Fv[rt] = __builtin_bit_cast(u32x2, pack4(lnc(acc[3][rt], mr, cf)));
      const f32x4 t01 = *(LAS f32x4*)(MR + 64 * mi + 16 * rt + 4 * g), t23 = *(LAS f32x4*)(MR + 64 * mi + 16 * rt + 4 * g + 2);
      const f32x4 mm4 = {t01[0], t01[2], t23[0], t23[2]}, rr4 = {t01[1], t01[3], t23[1], t23[3]};
      V[rt] = pack4((acc[2][rt] - mm4 * cv) * rr4);
    }
    LAS char* kvb = lds + SC_KV + nj * 1536;
    const s16x4 hv = pack4(lnc(acch, MR[128 + r16], CS[((mi ? 8 : 4) + nj) * 4 + g]));
    if (mi == 0) { *(LAS s16x4*)(kvb + 512 + lane * 8) = K[3]; *(LAS s16x4*)(kvb + 1024 + lane * 8) = V[3]; }
    else *(LAS s16x4*)(kvb + r16 * 32 + g * 8) = hv;
#pragma unroll
    for (int rt = 0; rt < 4; ++rt) *(LAS u32x2*)(lds + SC_FX + ((mi * 4 + rt) * 4 + nj) * 512 + lane * 8) = Fv[rt];
    f32x4 mkp, mkc;
#pragma unroll
    for (int r = 0; r < 4; ++r) {
      const int dp = r16 + 16 - (4 * g + r), dc = r16 - (4 * g + r);
      mkp[r] = (!(dp & 1) && dp <= 14) ? 0.f : -__builtin_inff();
      mkc[r] = (!(dc & 1) && dc >= 0 && dc <= 14) ? 0.f : -__builtin_inff();
    }
    const bool odd = r16 & 1;
    const unsigned psh = odd ? 16u : 0u;
    const int RT0 = rg * 8 + 4 * mi;
    const unsigned od = ohl + (RT0 * NKS + 2 * cg + (nj >> 1)) * 1024;
    const s16x4 ones4 = {0x3f80, 0x3f80, 0x3f80, 0x3f80};
    const bf16x8 ones8 = __builtin_bit_cast(bf16x8, __builtin_shufflevector(ones4, ones4, 0, 1, 2, 3, 4, 5, 6, 7));
    auto attn = [&](s16x4 kp, s16x4 vp, s16x4 kc, s16x4 vc, s16x4 qq, f32x4 mp, int rt) {
      const f32x4 z = {0, 0, 0, 0};
      const f32x4 sp = mfma16(kp, qq, mp), sc = mfma16(kc, qq, mkc);
      const float sc2 = 0.25f * 1.44269504089f;
      const float e0 = odd ? sp[1] : sp[0], e1 = odd ? sp[3] : sp[2], e2 = odd ? sc[1] : sc[0], e3 = odd ? sc[3] : sc[2];
      const float mx = xg_max(fmaxf(fmaxf(e0, e1), fmaxf(e2, e3))) * sc2;
      const float p0 = __builtin_amdgcn_exp2f(fmaf(e0, sc2, -mx)), p1 = __builtin_amdgcn_exp2f(fmaf(e1, sc2, -mx));
      const float p2 = __builtin_amdgcn_exp2f(fmaf(e2, sc2, -mx)), p3 = __builtin_amdgcn_exp2f(fmaf(e3, sc2, -mx));
      const u32x2 k01 = __builtin_bit_cast(u32x2, pack4((f32x4){p0, 0.f, p1, 0.f})), k23 = __builtin_bit_cast(u32x2, pack4((f32x4){p2, 0.f, p3, 0.f}));
      const u32x4 pw4 = {k01[0] << psh, k01[1] << psh, k23[0] << psh, k23[1] << psh};
      const bf16x8 pf = __builtin_bit_cast(bf16x8, pw4);
      const bf16x8 vf = __builtin_bit_cast(bf16x8, __builtin_shufflevector(vp, vc, 0, 1, 2, 3, 4, 5, 6, 7));
      const f32x4 o = mfma32(vf, pf, z), sm = mfma32(ones8, pf, z);
      return __builtin_bit_cast(u32x2, pack4(o * __builtin_amdgcn_rcpf(sm[0])));
    };
    u32x2 O[4];
#pragma unroll
    for (int rt = 1; rt < 4; ++rt) O[rt] = attn(K[rt - 1], V[rt - 1], K[rt], V[rt], Q[rt], mkp, rt);
    LAS char* ox = lds + SC_OX + w * 1024;
    *(LAS u32x2*)(ox + lane * 8) = (nj & 1) ? O[1] : O[3];
    if (nj & 1) *(LAS u32x2*)(ox + 512 + lane * 8) = O[2];
    WAITL0(); BAR();
    {
      LAS char* oo = lds + SC_OX + (w ^ 1) * 1024;
      const u32x2 p0 = *(LAS u32x2*)(oo + lane * 8);
      if (nj & 1) st16(ohr, od + 3 * NKS * 1024, (u32x4){p0[0], p0[1], O[3][0], O[3][1]});
      else {
        const u32x2 p1 = *(LAS u32x2*)(oo + 512 + lane * 8);
        st16(ohr, od + 1 * NKS * 1024, (u32x4){O[1][0], O[1][1], p0[0], p0[1]});
        st16(ohr, od + 2 * NKS * 1024, (u32x4){O[2][0], O[2][1], p1[0], p1[1]});
      }
    }
    {
      LAS char* fo = lds + SC_FX + (mi * 4 + nj) * 2048 + lane * 8;
      const s16x4 g0 = *(LAS s16x4*)fo, u0 = *(LAS s16x4*)(fo + 512), g1 = *(LAS s16x4*)(fo + 1024), u1 = *(LAS s16x4*)(fo + 1536);
      const u32x2 h0 = __builtin_bit_cast(u32x2, pack4(swiglu4(unpack4(g0), unpack4(u0)))), h1 = __builtin_bit_cast(u32x2, pack4(swiglu4(unpack4(g1), unpack4(u1))));
      st16(ohr, ohl + ((RT0 + nj) * NKS + 16 + cg) * 1024, (u32x4){h0[0], h0[1], h1[0], h1[1]});
    }
    s16x4 Kp, Vp;
    if (mi == 0) { Kp = hv; Vp = __builtin_amdgcn_ds_read_tr16_b64_v4i16((LAS s16x4*)(kvb + (4 * g + q) * 32 + pp * 8)); }
    else { Kp = *(LAS s16x4*)(kvb + 512 + lane * 8); Vp = *(LAS s16x4*)(kvb + 1024 + lane * 8); }
    const float ninf = -__builtin_inff();
    O[0] = attn(Kp, Vp, K[0], V[0], Q[0], RT0 == 0 ? (f32x4){ninf, ninf, ninf, ninf} : mkp, 0);
    st8(ohr, od + (nj & 1) * 8, __builtin_bit_cast(s16x4, O[0]));
  }
  STAMP(6);
  return tag;
}

constexpr int NR = (NKS + 3) / 4;
#define SC1 " sc1"
DI void gload16(u32x4& dst, const char* sbase, unsigned voff) { asm volatile("global_load_dwordx4 %0, %1, %2" SC1 : "=v"(dst) : "v"(voff), "s"(sbase) : "memory"); }
DI void gload8(u32x2& dst, const char* sbase, unsigned voff) { asm volatile("global_load_dwordx2 %0, %1, %2" SC1 : "=v"(dst) : "v"(voff), "s"(sbase) : "memory"); }
template <int N> DI void waitVn() { asm volatile("s_waitcnt vmcnt(%0)" :: "n"(N) : "memory"); }
#define P2AV 6
constexpr int P2A = P2AV;
constexpr int P2NA = P2A + 1;
constexpr int P2R = 4;
__host__ __device__ constexpr int bslot2(int r) { return r < 8 ? bslot(r) : r < 12 ? bslot(r - 8) : r == 12 ? SC_HS : bslot(r - 9); }
constexpr bool ksplit(int ks) { return false; }
constexpr bool psplit(int ke) { return ksplit(ke) || ksplit(ke + 1); }
constexpr int nA(int r) { return (psplit(4 * phys(r)) ? 2 : 1) + (psplit(4 * phys(r) + 2) ? 2 : 1); }
constexpr int p2issued(int r) { int n = 14; for (int q = 0; q < P2A; ++q) n += nA(q); for (int q = 0; q < r; ++q) n += (q + P2A < NR ? nA(q + P2A) : 0) + (q == 6 ? 4 : 0) + (q == 9 ? 2 : 0); return n; }
constexpr int p2posA(int r) { if (r < P2A) { int n = 14; for (int q = 0; q <= r; ++q) n += nA(q); return n; } return p2issued(r - P2A) + nA(r); }
constexpr int p2posB14() { return p2issued(6) + nA(6 + P2A) + 4; }
template <int R> struct P2W { static constexpr int n = p2issued(R) - ((R == 12 && p2posB14() > p2posA(R)) ? p2posB14() : p2posA(R)); };

DI void phase2(const Params& p, LAS char* lds, int rt64, int ct64, unsigned tag, bool boss) {
  const int tid = threadIdx.x, lane = tid & 63;
  const int w = rfl(tid >> 6), kh = w >> 2, m4 = w & 3;
  const int r16 = lane & 15, g = lane >> 4, q = r16 >> 2, pp = lane & 3;
  const unsigned ldsb = (unsigned)(uintptr_t)lds;
  constexpr int PH = 1; STAMP(0);
  const char* pa = p.ws + WS_OH + ((size_t)(rt64 * 4 + m4) * NKS + kh) * 1024;
  const unsigned va = lane * 16;
  unsigned vb;
  { const int row = 8 * m4 + (lane >> 3), pc = lane & 7; const int c = (pc >> 1) ^ ((row >> 1) & 3);
    vb = row * 1024 + ct64 * 128 + (2 * c + (pc & 1)) * 16; }
  const char* pb2 = p.w2 + (long)(kh - 16) * 32768;
  const char* pbo = p.wo + (long)kh * 32768;
  int bl[4];
#pragma unroll
  for (int c = 0; c < 4; ++c) bl[c] = (4 * g + q) * 128 + ((c ^ ((2 * g + (q >> 1)) & 3)) << 5) + pp * 8 + kh * 4096;
  auto readB = [&](bf16x8 (&dst)[2][4], int r) {
    LAS char* sb = lds + bslot2(r);
#pragma unroll
    for (int j = 0; j < 2; ++j)
#pragma unroll
      for (int c = 0; c < 4; ++c) dst[j][c] = ldsB(sb + j * 8192 + bl[c], 2048);
  };
  auto issueB = [&](int r) {
#pragma unroll
    for (int j = 0; j < 2; ++j) {
      const int ke = 4 * phys(r) + 2 * j;
      const bool over = (ke + 1 > NKS - 1);
      glds16s((ke < 16 ? pbo : pb2) + (over ? (long)ke - kh : (long)ke) * 32768, vb, ldsb + bslot2(r) + (2 * j) * 4096 + kh * 4096 + m4 * 1024);
    }
  };
  bf16x8 bp[P2R][2][4];
#pragma unroll
  for (int r = 0; r < P2R; ++r) readB(bp[r], r);
  WAITL0(); BAR();
#pragma unroll
  for (int r = 6; r <= 12; ++r) issueB(r);
  if (w == 0) {
    gu64* fl = (gu64*)(p.ws + WS_FLAGS) + (rt64 >> 1) * 16 + (lane & 15);
    const unsigned long long want = FMAGIC | tag;
    for (unsigned spins = 0; spins < (1u << 20); ++spins) {
      unsigned long long v;
      asm volatile("global_load_dwordx2 %0, %1, off sc1\n\ts_waitcnt vmcnt(0)" : "=v"(v) : "v"(fl) : "memory");
      if (__all(v == want)) break;
      __builtin_amdgcn_s_sleep(1);
    }
  }
  BAR();
  STAMP(1);
  u32x4 A4[P2NA][2]; u32x2 Al[P2NA][2], Ah[P2NA][2];
  const unsigned vs0 = lane * 8, vs1 = lane * 8 + 512, vn1 = lane * 16 + 8;
  auto issueA = [&](int r) {
#pragma unroll
    for (int j = 0; j < 2; ++j) {
      const int ke = 4 * phys(r) + 2 * j;
      const bool over = (ke + 1 > NKS - 1);
      const char* src = pa + (over ? (long)ke - kh : (long)ke) * 1024;
      if (!psplit(ke)) gload16(A4[r % P2NA][j], src, va);
      else if (ksplit(ke) && ksplit(ke + 1)) { gload8(Al[r % P2NA][j], src, vs0); gload8(Ah[r % P2NA][j], src, vs1); }
      else { const bool sp = ksplit(ke) ? (kh == 0) : (kh == 1); gload8(Al[r % P2NA][j], src, sp ? vs0 : va); gload8(Ah[r % P2NA][j], src, sp ? vs1 : vn1); }
    }
  };
  auto pinA = [&](int r) {
#pragma unroll
    for (int j = 0; j < 2; ++j) {
      if (psplit(4 * phys(r) + 2 * j)) asm volatile("" : "+v"(Al[r % P2NA][j]), "+v"(Ah[r % P2NA][j])); else asm volatile("" : "+v"(A4[r % P2NA][j]));
    }
  };
  auto getA = [&](int r, int j) {
    return psplit(4 * phys(r) + 2 * j) ? __builtin_bit_cast(bf16x8, __builtin_shufflevector(Al[r % P2NA][j], Ah[r % P2NA][j], 0, 1, 2, 3)) : __builtin_bit_cast(bf16x8, A4[r % P2NA][j]);
  };
#pragma unroll
  for (int r = 0; r < P2A; ++r) { issueA(r); if (r + 1 < P2A) BAR(); }
  f32x4 acc[4] = {{0, 0, 0, 0}, {0, 0, 0, 0}, {0, 0, 0, 0}, {0, 0, 0, 0}};
  const size_t ooff = (size_t)(rt64 * 64 + 16 * m4 + r16) * 1024 + (ct64 * 64 + 32 * kh + 4 * g) * 2;
  bf16x4 xv0, xv1;
  u32x4 fl0 = {0, 0, 0, 0}, fl1 = {0, 0, 0, 0};
  bf16x8 bf[2][2][4];
  auto round = [&](auto RC) {
    constexpr int r = decltype(RC)::value;
    waitVn<P2W<r>::n>();
    pinA(r);
    if (r == 5 || r == 6 || r == 12) BAR();
    if (r + P2A < NR) issueA(r + P2A);
    if (r == 6) { issueB(13); issueB(14); }
    if (r == 9) { asm volatile("global_load_dwordx2 %0, %2, %3\n\tglobal_load_dwordx2 %1, %2, %3 offset:32" : "=&v"(xv0), "=&v"(xv1) : "v"((unsigned)ooff), "s"(p.x) : "memory"); }
    if (r == 0) STAMP(2);
    if (r == 8) STAMP(3);
    if (r == 11 && boss) {
      const char* fb = p.ws + WS_FLAGS;
      asm volatile("global_load_dwordx4 %0, %2, %3 sc1\n\tglobal_load_dwordx4 %1, %2, %3 offset:1024 sc1" : "=&v"(fl0), "=&v"(fl1) : "v"(va), "s"(fb) : "memory");
    }
    SB0();
    if (r + 1 < NR && r + 1 >= P2R) readB(bf[(r + 1) & 1], r + 1);
#pragma unroll
    for (int j = 0; j < 2; ++j) {
      if (4 * phys(r) + 2 * j + 1 > NKS - 1 && kh) continue;
#pragma unroll
      for (int c = 0; c < 4; ++c) acc[c] = mfma32(r < P2R ? bp[r < P2R ? r : 0][j][c] : bf[r & 1][j][c], getA(r, j), acc[c]);
    }
#pragma unroll
    for (int k = 0; k < 8; ++k) {
      __builtin_amdgcn_sched_group_barrier(0x8, 1, 0);
      __builtin_amdgcn_sched_group_barrier(0x100, 2, 0);
    }
    SB0();
  };
  static_for<NR>(round);
  STAMP(4);
  {
    LAS f32x4* red = (LAS f32x4*)(lds + SC_RED);
    red[(w * 2 + 0) * 64 + lane] = kh ? acc[0] : acc[2];
    red[(w * 2 + 1) * 64 + lane] = kh ? acc[1] : acc[3];
    WAITL0(); BAR();
    const f32x4 o0 = red[((w ^ 4) * 2 + 0) * 64 + lane], o1 = red[((w ^ 4) * 2 + 1) * 64 + lane];
    const f32x4 s0 = (kh ? acc[2] : acc[0]) + o0, s1 = (kh ? acc[3] : acc[1]) + o1;
    asm volatile("s_waitcnt vmcnt(0)" : "+v"(xv0), "+v"(xv1) :: "memory");
    const f32x4 x0 = __builtin_convertvector(xv0, f32x4), x1 = __builtin_convertvector(xv1, f32x4);
    const u32x2 pa2 = __builtin_bit_cast(u32x2, pack4(s0 + x0)), pb2_ = __builtin_bit_cast(u32x2, pack4(s1 + x1));
    const auto d0 = __builtin_amdgcn_permlane16_swap(pa2[0], pb2_[0], false, false), d1 = __builtin_amdgcn_permlane16_swap(pa2[1], pb2_[1], false, false);
    const __amdgpu_buffer_rsrc_t outr = __builtin_amdgcn_make_buffer_rsrc(p.out, 0, 2048 * 1024, 0x00020000);
    st16(outr, (unsigned)((rt64 * 64 + 16 * m4 + r16) * 1024 + (ct64 * 64 + 32 * kh + (g & 1) * 16 + (g >> 1) * 8) * 2), (u32x4){d0[0], d1[0], d0[1], d1[1]});
  }
  STAMP(5);
  if (boss) {
    asm volatile("s_waitcnt vmcnt(0)" : "+v"(fl0), "+v"(fl1) :: "memory");
    const unsigned hi = (unsigned)(FMAGIC >> 32);
    bool ok = fl0[0] == tag && fl0[1] == hi && fl0[2] == tag && fl0[3] == hi && fl1[0] == tag && fl1[1] == hi && fl1[2] == tag && fl1[3] == hi;
    if (!__all(ok)) {
      gu64* fl = (gu64*)(p.ws + WS_FLAGS) + lane;
      const unsigned long long want = FMAGIC | tag;
      for (unsigned spins = 0; spins < (1u << 20); ++spins) {
        ok = __hip_atomic_load(fl, RLX_AGENT) == want && __hip_atomic_load(fl + 64, RLX_AGENT) == want &&
             __hip_atomic_load(fl + 128, RLX_AGENT) == want && __hip_atomic_load(fl + 192, RLX_AGENT) == want;
        if (__all(ok)) break;
        __builtin_amdgcn_s_sleep(1);
      }
    }
    if (lane == 0) __hip_atomic_store((gu32*)p.ws, tag, RLX_AGENT);
  }
}

__global__ __launch_bounds__(512, 2) void k_fused(Params p) {
  extern __shared__ __attribute__((aligned(16))) char shm[];
  LAS char* lds = (LAS char*)shm;
  const int b = blockIdx.x, xcd = b & 7, idx = b >> 3;
  const int rg = (xcd & 1) * 8 + (idx & 7), cg = (xcd >> 1) * 4 + (idx >> 3);
  const int rt64 = xcd * 4 + (idx & 3), ct64 = idx >> 2;
  const char* code0 = (const char*)(__builtin_amdgcn_s_getpc() & ~(unsigned long long)255);
  const unsigned tag = (cg < 8) ? phase1<true>(p, lds, rg, cg, ct64, idx, code0) : phase1<false>(p, lds, rg, cg - 8, ct64, idx, code0);
  asm volatile("s_waitcnt vmcnt(0)" ::: "memory");
  BAR();
  if (threadIdx.x == 0) __hip_atomic_store((gu64*)(p.ws + WS_FLAGS) + rg * 16 + cg, FMAGIC | tag, RLX_AGENT);
  { constexpr int PH = 0; STAMP(7); }
  phase2(p, lds, rt64, ct64, tag, b == 0 && rfl(threadIdx.x >> 6) == 0);
  { constexpr int PH = 1; STAMP(6); STAMP(7); }
}

extern "C" void kernel_launch(void* const* d_in, const int* in_sizes, int n_in, void* d_out, int out_size, void* d_ws, size_t ws_size,
                              hipStream_t stream) {
  Params p;
  p.x = (const char*)d_in[0]; p.wqkv = (const char*)d_in[1]; p.wo = (const char*)d_in[2];
  p.w1 = (const char*)d_in[4]; p.w3 = (const char*)d_in[5]; p.w2 = (const char*)d_in[6];
  p.ws = (char*)d_ws; p.out = (char*)d_out; p.stamps = (unsigned long long*)((char*)d_ws + (128u << 20));
  (void)hipFuncSetAttribute((const void*)k_fused, hipFuncAttributeMaxDynamicSharedMemorySize, P1_LDS);
  k_fused<<<256, 512, P1_LDS, stream>>>(p);
}
```

```cpp
#define ANCHOR 4
#include <hip/hip_runtime.h>
#include <stdint.h>

#define LAS __attribute__((address_space(3)))
#define DI __device__ __forceinline__
typedef __bf16 bf16x8 __attribute__((ext_vector_type(8)));
typedef __bf16 bf16x4 __attribute__((ext_vector_type(4)));
typedef short s16x4 __attribute__((ext_vector_type(4)));
typedef float f32x4 __attribute__((ext_vector_type(4)));
typedef float f32x2 __attribute__((ext_vector_type(2)));
typedef unsigned u32x2 __attribute__((ext_vector_type(2)));
typedef unsigned u32x4 __attribute__((ext_vector_type(4)));

constexpr int NKS = 59;
constexpr int SLOT = 53248;
constexpr int WOFF = 16384, EOFF = 49152;
constexpr int DUMMY = 3 * SLOT;
constexpr int SC_CS = DUMMY + 1024;
constexpr int SC_MR = SC_CS + 1152;
constexpr int P1_LDS = 163840;
constexpr int SC_HS = 86016;
constexpr int SC_KV = SLOT;
constexpr int SC_FX = SC_KV + 6144;
constexpr int SC_OX = SC_FX + 16384;
constexpr int SC_RED = SLOT;
__host__ __device__ constexpr int bslot(int r) { return (r & 7) < 3 ? 2 * SLOT + (r & 7) * 16384 : (r & 7) < 6 ? ((r & 7) - 3) * 16384 : SLOT + ((r & 7) - 6) * 16384; }
__host__ __device__ constexpr int phys(int r) { return (r + 6) % 15; }
constexpr unsigned long long FMAGIC = 0x5AC3E17Bull << 32;
constexpr size_t WS_FLAGS = 4096, WS_STATS = 8192, WS_OH = 65536;

#define STAMP(i) do {} while (0)

template <int V> struct IC { static constexpr int value = V; };
template <int N, int I = 0, class F> DI void static_for(F& f) { if constexpr (I < N) { f(IC<I>{}); static_for<N, I + 1>(f); } }
DI int rfl(int v) { return __builtin_amdgcn_readfirstlane(v); }
DI void glds16(const void* g, unsigned lds_dst) {
  unsigned keep;
  asm volatile("s_mov_b32 %0, m0\n\ts_mov_b32 m0, %2\n\ts_nop 0\n\tglobal_load_lds_dwordx4 %1, off\n\ts_mov_b32 m0, %0"
               : "=&s"(keep) : "v"(g), "s"(lds_dst) : "memory");
}
DI void glds16s(const char* sbase, unsigned voff, unsigned lds_dst) {
  asm volatile("s_mov_b32 m0, %2\n\ts_nop 0\n\tglobal_load_lds_dwordx4 %1, %0" :: "s"(sbase), "v"(voff), "s"(lds_dst) : "memory", "m0");
}
#define WAITV(n) asm volatile("s_waitcnt vmcnt(" #n ")" ::: "memory")
#define WAITL0() do { asm volatile("" ::: "memory"); __builtin_amdgcn_s_waitcnt(0xC07F); asm volatile("" ::: "memory"); } while (0)
#define BAR() do { asm volatile("" ::: "memory"); __builtin_amdgcn_s_barrier(); asm volatile("" ::: "memory"); } while (0)
#define SB0() __builtin_amdgcn_sched_barrier(0)

DI bf16x8 ldsA(LAS char* p) { return *(LAS bf16x8*)p; }
DI bf16x8 ldsB(LAS char* p, int toff) {
  s16x4 lo = __builtin_amdgcn_ds_read_tr16_b64_v4i16((LAS s16x4*)p);
  s16x4 hi = __builtin_amdgcn_ds_read_tr16_b64_v4i16((LAS s16x4*)(p + toff));
  return __builtin_bit_cast(bf16x8, __builtin_shufflevector(lo, hi, 0, 1, 2, 3, 4, 5, 6, 7));
}
DI f32x4 mfma32(bf16x8 a, bf16x8 b, f32x4 c) { return __builtin_amdgcn_mfma_f32_16x16x32_bf16(a, b, c, 0, 0, 0); }
DI f32x4 mfma16(s16x4 a, s16x4 b, f32x4 c) { return __builtin_amdgcn_mfma_f32_16x16x16bf16_1k(a, b, c, 0, 0, 0); }
DI s16x4 pack4(f32x4 v) { return __builtin_bit_cast(s16x4, __builtin_convertvector(v, bf16x4)); }
DI f32x4 unpack4(s16x4 v) { return __builtin_convertvector(__builtin_bit_cast(bf16x4, v), f32x4); }
DI float xg_sum(float v) {
  auto r = __builtin_amdgcn_permlane16_swap(__float_as_uint(v), __float_as_uint(v), false, false);
  v = __uint_as_float(r[0]) + __uint_as_float(r[1]);
  auto s = __builtin_amdgcn_permlane32_swap(__float_as_uint(v), __float_as_uint(v), false, false);
  return __uint_as_float(s[0]) + __uint_as_float(s[1]);
}
DI float xg_max(float v) {
  auto r = __builtin_amdgcn_permlane16_swap(__float_as_uint(v), __float_as_uint(v), false, false);
  v = fmaxf(__uint_as_float(r[0]), __uint_as_float(r[1]));
  auto s = __builtin_amdgcn_permlane32_swap(__float_as_uint(v), __float_as_uint(v), false, false);
  return fmaxf(__uint_as_float(s[0]), __uint_as_float(s[1]));
}
DI float row_sum16(float v) {
  v += __builtin_bit_cast(float, __builtin_amdgcn_update_dpp(0, __builtin_bit_cast(int, v), 0x128, 0xf, 0xf, false));
  v += __builtin_bit_cast(float, __builtin_amdgcn_update_dpp(0, __builtin_bit_cast(int, v), 0x124, 0xf, 0xf, false));
  v += __builtin_bit_cast(float, __builtin_amdgcn_update_dpp(0, __builtin_bit_cast(int, v), 0x122, 0xf, 0xf, false));
  v += __builtin_bit_cast(float, __builtin_amdgcn_update_dpp(0, __builtin_bit_cast(int, v), 0x121, 0xf, 0xf, false));
  return v;
}
DI f32x4 lnc(f32x4 a, f32x2 mr, f32x4 c) { return (a - c * mr[0]) * mr[1]; }
DI f32x4 swiglu_ln(f32x4 ag, f32x4 au, f32x2 mr, f32x4 cg, f32x4 cu) {
  const f32x4 g = ag - cg * mr[0], u = au - cu * mr[0];
  const f32x4 t = g * (mr[1] * -1.44269504089f);
  const float ir2 = __builtin_amdgcn_rcpf(mr[1] * mr[1]);
  f32x4 e, d;
#pragma unroll
  for (int r = 0; r < 4; ++r) e[r] = __builtin_amdgcn_exp2f(t[r]);
  e = e * ir2 + ir2;
#pragma unroll
  for (int r = 0; r < 4; ++r) d[r] = __builtin_amdgcn_rcpf(e[r]);
  return g * u * d;
}
DI f32x4 swiglu4(f32x4 g, f32x4 u) {
  const f32x4 t = g * -1.44269504089f;
  f32x4 d;
#pragma unroll
  for (int r = 0; r < 4; ++r) d[r] = __builtin_amdgcn_rcpf(1.0f + __builtin_amdgcn_exp2f(t[r]));
  return g * u * d;
}

struct Params {
  const char* x; const char* wqkv; const char* wo; const char* w1; const char* w3; const char* w2;
  char* ws; char* out; unsigned long long* stamps;
};
typedef __attribute__((address_space(1))) unsigned gu32;
typedef __attribute__((address_space(1))) unsigned long long gu64;
#define RLX_AGENT __ATOMIC_RELAXED, __HIP_MEMORY_SCOPE_AGENT
DI void st16(__amdgpu_buffer_rsrc_t r, unsigned off, u32x4 v) { __builtin_amdgcn_raw_buffer_store_b128(v, r, off, 0, 16); }
DI void st8(__amdgpu_buffer_rsrc_t r, unsigned off, s16x4 v) { __builtin_amdgcn_raw_buffer_store_b64(__builtin_bit_cast(u32x2, v), r, off, 0, 16); }

struct Frags { bf16x8 a[4], ah, b[4], bx, be, e0; };

template <bool TA>
DI unsigned phase1(const Params& p, LAS char* lds, int rg, int cg, int ct64, int idx, const char* code0) {
  const int tid = threadIdx.x, lane = tid & 63;
  const int w = rfl(tid >> 6), mi = w >> 2, nj = w & 3;
  const int r16 = lane & 15, g = lane >> 4, q = r16 >> 2, pp = lane & 3;
  constexpr int PH = 0; STAMP(0);
  const unsigned ldsb = (unsigned)(uintptr_t)lds;
  const int row0 = rg * 128;
  const int srow = row0 + (TA ? cg : 8 + cg) * 8 + w;
  unsigned ev; u32x4 xrow;
  asm volatile("global_load_dword %0, %1, %2 sc1" : "=v"(ev) : "v"(0u), "s"(p.ws) : "memory");
  asm volatile("global_load_dwordx4 %0, %1, %2" : "=v"(xrow) : "v"((unsigned)(srow * 1024 + lane * 16)), "s"(p.x) : "memory");
  const int tm = TA ? 2 * cg : 16 + 18 * (cg >> 1) + ((cg & 1) ? 10 : 0);
  const int te = 16 + 18 * (cg >> 1) + 8 + (cg & 1);
  unsigned vx[2], vw[4], ve;
#pragma unroll
  for (int m = 0; m < 2; ++m) {
    const int row = 16 * w + 8 * m + (lane >> 3);
    vx[m] = (unsigned)((row0 + row) * 1024 + (((lane & 7) ^ ((row >> 1) & 7)) << 4));
  }
  const char* bw[4]; unsigned ldw[4], dw[4];
#pragma unroll
  for (int m = 0; m < 4; ++m) {
    if (!TA || m < 2) {
      const int pi = TA ? (2 * w + m) : (2 * w + (m & 1));
      const int kr = 4 * pi + (lane >> 4), pc = lane & 15;
      const int c = (pc >> 1) ^ ((kr & 3) | (((kr >> 3) & 1) << 2));
      if (TA) { bw[m] = p.wqkv; ldw[m] = 3072; vw[m] = kr * 3072 + ((c >> 2) * 512 + (4 * cg + (c & 3)) * 16) * 2 + (pc & 1) * 16; dw[m] = WOFF + pi * 1024; }
      else { bw[m] = (m >> 1) ? p.w3 : p.w1; ldw[m] = 2752; int t = tm + c; t = t > 85 ? 85 : t; vw[m] = kr * 2752 + t * 32 + (pc & 1) * 16; dw[m] = WOFF + (m >> 1) * 16384 + pi * 1024; }
    } else if (m == 2) {
      const int kr = 8 * w + (lane >> 3), pc = lane & 7;
      const int c = (pc >> 1) ^ (((kr >> 1) & 1) | (((kr >> 3) & 1) << 1));
      bw[m] = p.wqkv; ldw[m] = 3072; vw[m] = kr * 3072 + (1024 + (4 * cg + c) * 16) * 2 + (pc & 1) * 16; dw[m] = WOFF + 16384 + w * 1024;
    } else {
      const int kr = 16 * (w & 3) + (lane >> 2), pc = lane & 3;
      const int e = (pc >> 1) ^ ((kr >> 3) & 1);
      bw[m] = mi ? p.w3 : p.w1; ldw[m] = 2752; vw[m] = kr * 2752 + (tm + e) * 32 + (pc & 1) * 16; dw[m] = WOFF + 24576 + mi * 4096 + (w & 3) * 1024;
    }
  }
  const bool has_e = TA ? (w < 2) : (w < 4);
  const char* be_; unsigned lde, de;
  if (TA) {
    const int row = 8 * (w & 1) + (lane >> 3);
    int gr = row0 - 16 + row; gr = gr < 0 ? 0 : gr;
    be_ = p.x; lde = 2; ve = gr * 1024 + (((lane & 7) ^ ((row >> 1) & 7)) << 4); de = EOFF + w * 1024;
  } else {
    const int pos = 32 * (w & 1) + (lane >> 1), kr = pos ^ (((pos >> 3) & 1) << 2);
    be_ = (w & 2) ? p.w3 : p.w1; lde = 2752; ve = kr * 2752 + te * 32 + (lane & 1) * 16; de = EOFF + (w & 3) * 1024;
  }
  if (!has_e) { be_ = p.x; lde = 0; ve = 0; }
  unsigned vb;
  { const int row = 8 * nj + (lane >> 3), pc = lane & 7; const int c = (pc >> 1) ^ ((row >> 1) & 3);
    vb = row * 1024 + ct64 * 128 + (2 * c + (pc & 1)) * 16; }
  const unsigned emask = has_e ? 0xffffffffu : 0u;
  if (!has_e) de = DUMMY;
  const char* sx = p.x; const char* se = be_; const char* sw[4] = {bw[0], bw[1], bw[2], bw[3]};
  auto dma = [&](int i, int so) {
    if (i == 0) { glds16s(se, ve, ldsb + (so & emask) + de); se += 64u * lde; }
    else if (i <= 2) { glds16s(sx, vx[i - 1], ldsb + so + (2 * w + i - 1) * 1024); if (i == 2) sx += 128; }
    else { glds16s(sw[i - 3], vw[i - 3], ldsb + so + dw[i - 3]); sw[i - 3] += 64u * ldw[i - 3]; }
  };
  auto dmab = [&](int rr, int j) {
    const int ke = 4 * phys(rr) + 2 * j;
    const int ks2 = (ke + 1 > NKS - 1) ? ke : ke + mi;
    const char* src = (ke < 16) ? p.wo + ks2 * 32768 : p.w2 + (ks2 - 16) * 32768;
    glds16s(src, vb, ldsb + bslot(rr) + (2 * j + mi) * 4096 + nj * 1024);
  };
#pragma unroll
  for (int i = 0; i < 7; ++i) dma(i, 0);
#pragma unroll
  for (int i = 0; i < 7; ++i) dma(i, SLOT);
  STAMP(1);
  const char* pfp; unsigned pfs; const char* pfw; const char* pfw2;
  {
    const int l5 = lane & 31;
    const int L = w * 10 + (l5 < 10 ? l5 : 9);
    if (L < 32) { pfp = p.x + (size_t)(row0 + 32 * (idx >> 3) + L) * 1024; pfs = 128; }
    else {
      const int L2 = L - 32, ri = L2 / 6, sg = L2 - 6 * ri;
      const int kr = 8 * (idx & 7) + ri;
      if (TA) { pfp = (sg < 3) ? p.wqkv + (size_t)kr * 3072 + sg * 1024 + cg * 128 : (sg == 3 ? p.w1 : p.w3) + (size_t)kr * 2752 + tm * 32; pfs = (sg < 3) ? 64u * 3072 : 64u * 2752; }
      else { int cb = (tm < te ? tm : te) * 32 + (sg % 3) * 128; cb = cb > 2748 ? 2748 : cb; pfp = (sg < 3 ? p.w1 : p.w3) + (size_t)kr * 2752 + cb; pfs = 64u * 2752; }
    }
    { const int l0 = (idx * 8 + w) * 27 + (lane < 14 ? lane : 13), l1 = (idx * 8 + w) * 27 + 14 + (lane < 13 ? lane : 12);
      auto lineaddr = [&](int l) { return l < 4096 ? p.wo + (size_t)l * 128 : l < 6144 ? p.w2 + (size_t)(l - 4096) * 128 : p.w2 + (size_t)1280 * 1024 + (size_t)(l - 6144) * 128; };
      pfw = lineaddr(l0); pfw2 = lineaddr(l1); }
  }
  auto pf = [&](const char* a) { asm volatile("s_mov_b32 m0, %1\n\ts_nop 0\n\tglobal_load_lds_dword %0, off" :: "v"(a), "s"(ldsb + DUMMY) : "memory", "m0"); };
  {
    const char* a = pfp + (size_t)((lane < 32 ? 2u : 3u) * pfs);
    if (lane == 10) a = code0 + (size_t)((idx * 8 + w) & 255) * 128;
    pf(a);
  }
  const int xa0 = r16 * 128 + ((g ^ (r16 >> 1)) << 4);
  const int fl = q | ((g & 1) << 2);
  const int w256 = (8 * g + q) * 256 + (fl << 5) + pp * 8;
  const int w128 = (8 * g + q) * 128 + ((((q >> 1) & 1) | ((g & 1) << 1)) << 5) + pp * 8;
  const int w64 = (8 * g + q) * 64 + ((g & 1) << 5) + pp * 8;
  const int w32 = (8 * g + ((g & 1) << 2) + q) * 32 + pp * 8;
  const int dt32 = (g & 1) ? -128 : 128;
  const int tb0 = (w256 ^ (nj << 6)) + (mi ? 16384 : 0), tb2 = (w256 ^ (nj << 6)) + (mi ? 0 : 16384);
  const int tbx = EOFF + (nj & 1) * 2048 + w32;
  const int taq = w256 ^ (nj << 5), tak = w256 ^ ((4 + nj) << 5), tav = 16384 + (w128 ^ (nj << 5)), taf = 24576 + (nj & 1) * 4096 + (w64 ^ ((nj >> 1) << 5));
  const int fu = 4 + ((nj >> 1) | ((nj & 1) << 1));
  int xr[4];
#pragma unroll
  for (int i = 0; i < 4; ++i) xr[i] = mi * 8192 + (TA ? i : ((i + nj) & 3)) * 2048;
  const int bea = mi ? tav : tak, bes = mi ? 4096 : 8192, bet = mi ? 512 : 1024;
  const int e0a = mi ? taf : taq, e0s = mi ? 2048 : 8192, e0t = mi ? 256 : 1024;
  constexpr int NRD = TA ? 11 : 9;
  auto ldsB2 = [&](LAS char* p0, LAS char* p1) {
    s16x4 lo = __builtin_amdgcn_ds_read_tr16_b64_v4i16((LAS s16x4*)p0), hi = __builtin_amdgcn_ds_read_tr16_b64_v4i16((LAS s16x4*)p1);
    return __builtin_bit_cast(bf16x8, __builtin_shufflevector(lo, hi, 0, 1, 2, 3, 4, 5, 6, 7));
  };
  auto rdf = [&](Frags& F, int i, int so, int ks) {
    LAS char* sb = lds + so;
    const int xa = xa0 ^ (ks * 64);
    LAS char* wb = sb + WOFF;
    if (TA) {
      switch (i) {
        case 0: F.a[0] = ldsA(sb + xa + xr[0]); break;
        case 1: F.b[0] = ldsB(wb + taq + ks * 8192, 1024); break;
        case 2: F.b[1] = ldsB(wb + tak + ks * 8192, 1024); break;
        case 3: F.b[2] = ldsB(wb + tav + ks * 4096, 512); break;
        case 4: F.b[3] = ldsB(wb + taf + ks * 2048, 256); break;
        case 5: F.a[1] = ldsA(sb + xa + xr[1]); break;
        case 6: F.a[2] = ldsA(sb + xa + xr[2]); break;
        case 7: F.a[3] = ldsA(sb + xa + xr[3]); break;
        case 8: F.be = ldsB2(wb + bea + ks * bes, wb + bea + ks * bes + bet); break;
        case 9: F.ah = ldsA(sb + EOFF + xa); break;
        default: F.e0 = ldsB2(wb + e0a + ks * e0s, wb + e0a + ks * e0s + e0t); break;
      }
    } else {
      switch (i) {
        case 0: F.a[0] = ldsA(sb + xa + xr[0]); break;
        case 1: F.b[0] = ldsB(wb + tb0 + ks * 8192, 1024); break;
        case 2: F.b[1] = ldsB(wb + (tb0 ^ 32) + ks * 8192, 1024); break;
        case 3: F.b[2] = ldsB(wb + tb2 + ks * 8192, 1024); break;
        case 4: F.b[3] = ldsB(wb + (tb2 ^ 32) + ks * 8192, 1024); break;
        case 5: F.a[1] = ldsA(sb + xa + xr[1]); break;
        case 6: F.a[2] = ldsA(sb + xa + xr[2]); break;
        case 7: F.a[3] = ldsA(sb + xa + xr[3]); break;
        default: F.bx = ldsB2(sb + tbx + ks * 1024, sb + tbx + ks * 1024 + dt32); break;
      }
    }
  };
  f32x4 acc[4][4], accx[2], acch = {0, 0, 0, 0}, cs0 = {0, 0, 0, 0}, cs1 = {0, 0, 0, 0}, csx = {0, 0, 0, 0};
  accx[0] = (f32x4){0, 0, 0, 0}; accx[1] = (f32x4){0, 0, 0, 0};
#pragma unroll
  for (int c = 0; c < 4; ++c)
#pragma unroll
    for (int rt = 0; rt < 4; ++rt) acc[c][rt] = (f32x4){0, 0, 0, 0};
  const bf16x8 ones = {(__bf16)1.0f, (__bf16)1.0f, (__bf16)1.0f, (__bf16)1.0f, (__bf16)1.0f, (__bf16)1.0f, (__bf16)1.0f, (__bf16)1.0f};
  auto mm = [&](const Frags& F, int gi) {
    if (gi < 4) {
      acc[0][gi] = mfma32(F.b[0], F.a[gi], acc[0][gi]);
      acc[1][gi] = mfma32(F.b[1], F.a[gi], acc[1][gi]);
      acc[2][gi] = TA ? mfma32(F.a[gi], F.b[2], acc[2][gi]) : mfma32(F.b[2], F.a[gi], acc[2][gi]);
      acc[3][gi] = mfma32(F.b[3], F.a[gi], acc[3][gi]);
    } else if (TA) {
      acch = mfma32(F.be, F.ah, acch);
      cs0 = mfma32(F.e0, ones, cs0); cs1 = mfma32(F.be, ones, cs1);
    } else {
      accx[0] = mfma32(F.bx, F.a[0], accx[0]); accx[1] = mfma32(F.bx, F.a[1], accx[1]);
      cs0 = mfma32(F.b[0], ones, cs0); cs1 = mfma32(F.b[1], ones, cs1); csx = mfma32(F.bx, ones, csx);
    }
  };
  constexpr int RPG = (NRD + 4) / 5;
  auto half = [&](const Frags& Fc, Frags& Fn, int so_n, int ks_n, int mode, int d0, int nd, int so_d, int bt) {
#pragma unroll
    for (int gi = 0; gi < 5; ++gi) {
#define PRIO 3
      if (gi == 0) { if (ks_n == 0) __builtin_amdgcn_s_setprio(3); else __builtin_amdgcn_s_setprio(1); }
      if (gi == PRIO) { if (ks_n == 0) __builtin_amdgcn_s_setprio(2); else __builtin_amdgcn_s_setprio(0); }
#pragma unroll
      for (int i = 0; i < RPG; ++i) { const int idx = gi * RPG + i; if (idx < NRD) rdf(Fn, idx, so_n, ks_n); }
      mm(Fc, gi);
#pragma unroll
      for (int k = 0; k < 5; ++k) {
        __builtin_amdgcn_sched_group_barrier(0x8, 1, 0);
        __builtin_amdgcn_sched_group_barrier(0x100, 1, 0);
        __builtin_amdgcn_sched_group_barrier(0x2, 2, 0);
      }
      __builtin_amdgcn_sched_group_barrier(0x100, 8, 0);
      SB0();
      if (mode == 0 && gi < nd) { dma(d0 + gi, so_d); SB0(); }
      if (mode == 1 && gi < nd && d0 + gi < 6) { dmab(3 * bt + ((d0 + gi) >> 1), (d0 + gi) & 1); SB0(); }
    }
  };
  Frags F0, F1;
  WAITV(8); BAR();
  STAMP(2);
#pragma unroll
  for (int i = 0; i < NRD; ++i) rdf(F0, i, 0, 0);
#pragma unroll
  for (int i = 0; i < 3; ++i) dma(i, 2 * SLOT);
  half(F0, F1, 0, 1, 0, 3, 4, 2 * SLOT, 0);
  pf(pfp + (size_t)(4u * pfs));
  asm volatile("" : "+v"(ev), "+v"(xrow));
  const unsigned tag = (unsigned)rfl((int)ev) + 1u;
  {
    float s1 = 0.f, s2 = 0.f;
#pragma unroll
    for (int i = 0; i < 4; ++i) {
      const float lo = __uint_as_float(xrow[i] << 16), hi = __uint_as_float(xrow[i] & 0xffff0000u);
      s1 += lo + hi; s2 = fmaf(lo, lo, fmaf(hi, hi, s2));
    }
    s1 = xg_sum(row_sum16(s1)); s2 = xg_sum(row_sum16(s2));
    const float m = s1 * (1.0f / 512.0f), var = fmaxf(s2 * (1.0f / 512.0f) - m * m, 0.f);
    const float rs = __builtin_amdgcn_rsqf(var + 1e-5f);
    const __amdgpu_buffer_rsrc_t str = __builtin_amdgcn_make_buffer_rsrc(p.ws + WS_STATS, 0, 2048 * 16, 0x00020000);
    if (lane == 0) st16(str, srow * 16, (u32x4){__float_as_uint(m), tag, __float_as_uint(rs), tag});
  }
  int sop = 0, so = SLOT;
#pragma unroll 1
  for (int t = 1; t < 6; ++t) {
    WAITV(9); WAITL0(); BAR();
    half(F1, F0, so, 0, 0, 0, 4, sop, 0);
    half(F0, F1, so, 1, 0, 4, 3, sop, 0);
    pf(t <= 3 ? pfp + (size_t)((unsigned)(t + 4) * pfs) : (t == 4 ? pfw : pfw2));
    sop = so; so = (so == 2 * SLOT) ? 0 : so + SLOT;
  }
  WAITV(9); WAITL0(); BAR();
  u32x4 stg;
  { int r = (w < 2) ? row0 + 64 * w + lane : (w == 2 ? row0 - 16 + (lane & 15) : row0 + lane); r = r < 0 ? 0 : r;
    asm volatile("global_load_dwordx4 %0, %1, %2 sc1" : "=v"(stg) : "v"((unsigned)(r * 16)), "s"(p.ws + WS_STATS) : "memory"); }
  half(F1, F0, 0, 0, 1, 0, 4, 0, 0);
  half(F0, F1, 0, 1, 1, 4, 3, 0, 0);
  WAITV(8); WAITL0(); BAR();
  half(F1, F0, SLOT, 0, 1, 0, 4, 0, 1);
  half(F0, F1, SLOT, 1, 1, 4, 3, 0, 1);
#pragma unroll
  for (int gi = 0; gi < 5; ++gi) mm(F1, gi);
  STAMP(4);
  {
    LAS f32x4* CSw = (LAS f32x4*)(lds + SC_CS);
    if (r16 == 0) {
      const int u0 = TA ? (mi ? 8 + fu : nj) : (mi * 8 + 2 * nj), u1 = TA ? (mi ? 8 + nj : 4 + nj) : (mi * 8 + 2 * nj + 1);
      CSw[u0 * 4 + g] = cs0; CSw[u1 * 4 + g] = cs1;
    }
    asm volatile("s_waitcnt vmcnt(12)" : "+v"(stg) :: "memory");
    if (w < 3) {
      int r = (w < 2) ? row0 + 64 * w + lane : row0 - 16 + (lane & 15); r = r < 0 ? 0 : r;
      if (!__all(stg[1] == tag && stg[3] == tag)) {
        gu64* gp = (gu64*)(p.ws + WS_STATS) + r * 2;
        for (unsigned spins = 0; spins < (1u << 20); ++spins) {
          const unsigned long long a = __hip_atomic_load(gp, RLX_AGENT), b = __hip_atomic_load(gp + 1, RLX_AGENT);
          stg = (u32x4){(unsigned)a, (unsigned)(a >> 32), (unsigned)b, (unsigned)(b >> 32)};
          if (__all(stg[1] == tag && stg[3] == tag)) break;
          __builtin_amdgcn_s_sleep(1);
        }
      }
      if (w < 2 || lane < 16) ((LAS f32x2*)(lds + SC_MR))[64 * w + lane] = (f32x2){__uint_as_float(stg[0]), __uint_as_float(stg[2])};
    }
  }
  WAITL0(); BAR();
  STAMP(5);
  LAS f32x4* CS = (LAS f32x4*)(lds + SC_CS);
  LAS f32x2* MR = (LAS f32x2*)(lds + SC_MR);
  const __amdgpu_buffer_rsrc_t ohr = __builtin_amdgcn_make_buffer_rsrc(p.ws + WS_OH, 0, 128 * NKS * 1024, 0x00020000);
  const unsigned ohl = lane * 16;
  if (!TA) {
    const f32x4 c0 = CS[(2 * nj) * 4 + g], c1 = CS[(2 * nj + 1) * 4 + g], c2 = CS[(8 + 2 * nj) * 4 + g], c3 = CS[(9 + 2 * nj) * 4 + g];
    const int tp = tm + 2 * nj;
    LAS char* fx = lds + SC_FX;
    { const f32x2 m0 = MR[64 * mi + 16 * nj + r16], m1 = MR[64 * mi + 16 * ((nj + 1) & 3) + r16];
      *(LAS s16x4*)(fx + ((mi * 2 + (nj & 1)) * 4 + nj) * 512 + lane * 8) = pack4(lnc(accx[0], m0, csx));
      *(LAS s16x4*)(fx + ((mi * 2 + (nj & 1)) * 4 + ((nj + 1) & 3)) * 512 + lane * 8) = pack4(lnc(accx[1], m1, csx)); }
    auto ffn = [&](const f32x4 (&G0)[4], const f32x4 (&U0)[4], const f32x4 (&G1)[4], const f32x4 (&U1)[4]) {
#pragma unroll
      for (int i = 0; i < 4; ++i) {
        const int rt = (i + nj) & 3;
        const f32x2 mr = MR[64 * mi + 16 * rt + r16];
        const u32x2 h0 = __builtin_bit_cast(u32x2, pack4(swiglu_ln(G0[i], U0[i], mr, c0, c2)));
        const u32x2 h1 = __builtin_bit_cast(u32x2, pack4(swiglu_ln(G1[i], U1[i], mr, c1, c3)));
        if (tp <= 85) st16(ohr, ohl + ((rg * 8 + 4 * mi + rt) * NKS + 16 + (tp >> 1)) * 1024, (u32x4){h0[0], h0[1], h1[0], h1[1]});
      }
    };
    if (mi) ffn(acc[2], acc[0], acc[3], acc[1]); else ffn(acc[0], acc[2], acc[1], acc[3]);
    WAITL0(); BAR();
    const f32x4 gx = unpack4(*(LAS s16x4*)(fx + ((mi * 2 + 0) * 4 + nj) * 512 + lane * 8)), ux = unpack4(*(LAS s16x4*)(fx + ((mi * 2 + 1) * 4 + nj) * 512 + lane * 8));
    st8(ohr, ohl + ((rg * 8 + w) * NKS + 16 + (te >> 1)) * 1024 + (te & 1) * 8, pack4(swiglu4(gx, ux)));
  } else {
    s16x4 Q[4], K[4], V[4]; u32x2 Fv[4];
    const f32x4 cq = CS[nj * 4 + g], ck = CS[(4 + nj) * 4 + g], cf = CS[(8 + fu) * 4 + g];
    const float cv = ((LAS float*)CS)[(8 + nj) * 16 + r16];
#pragma unroll
    for (int rt = 0; rt < 4; ++rt) {
      const f32x2 mr = MR[64 * mi + 16 * rt + r16];
      Q[rt] = pack4(lnc(acc[0][rt], mr, cq)); K[rt] = pack4(lnc(acc[1][rt], mr, ck)); Fv[rt] = __builtin_bit_cast(u32x2, pack4(lnc(acc[3][rt], mr, cf)));
      const f32x4 t01 = *(LAS f32x4*)(MR + 64 * mi + 16 * rt + 4 * g), t23 = *(LAS f32x4*)(MR + 64 * mi + 16 * rt + 4 * g + 2);
      const f32x4 mm4 = {t01[0], t01[2], t23[0], t23[2]}, rr4 = {t01[1], t01[3], t23[1], t23[3]};
      V[rt] = pack4((acc[2][rt] - mm4 * cv) * rr4);
    }
    LAS char* kvb = lds + SC_KV + nj * 1536;
    const s16x4 hv = pack4(lnc(acch, MR[128 + r16], CS[((mi ? 8 : 4) + nj) * 4 + g]));
    if (mi == 0) { *(LAS s16x4*)(kvb + 512 + lane * 8) = K[3]; *(LAS s16x4*)(kvb + 1024 + lane * 8) = V[3]; }
    else *(LAS s16x4*)(kvb + r16 * 32 + g * 8) = hv;
#pragma unroll
    for (int rt = 0; rt < 4; ++rt) *(LAS u32x2*)(lds + SC_FX + ((mi * 4 + rt) * 4 + nj) * 512 + lane * 8) = Fv[rt];
    f32x4 mkp, mkc;
#pragma unroll
    for (int r = 0; r < 4; ++r) {
      const int dp = r16 + 16 - (4 * g + r), dc = r16 - (4 * g + r);
      mkp[r] = (!(dp & 1) && dp <= 14) ? 0.f : -__builtin_inff();
      mkc[r] = (!(dc & 1) && dc >= 0 && dc <= 14) ? 0.f : -__builtin_inff();
    }
    const bool odd = r16 & 1;
    const unsigned psh = odd ? 16u : 0u;
    const int RT0 = rg * 8 + 4 * mi;
    const unsigned od = ohl + (RT0 * NKS + 2 * cg + (nj >> 1)) * 1024;
    const s16x4 ones4 = {0x3f80, 0x3f80, 0x3f80, 0x3f80};
    const bf16x8 ones8 = __builtin_bit_cast(bf16x8, __builtin_shufflevector(ones4, ones4, 0, 1, 2, 3, 4, 5, 6, 7));
    auto attn = [&](s16x4 kp, s16x4 vp, s16x4 kc, s16x4 vc, s16x4 qq, f32x4 mp, int rt) {
      const f32x4 z = {0, 0, 0, 0};
      const f32x4 sp = mfma16(kp, qq, mp), sc = mfma16(kc, qq, mkc);
      const float sc2 = 0.25f * 1.44269504089f;
      const float e0 = odd ? sp[1] : sp[0], e1 = odd ? sp[3] : sp[2], e2 = odd ? sc[1] : sc[0], e3 = odd ? sc[3] : sc[2];
      const float mx = xg_max(fmaxf(fmaxf(e0, e1), fmaxf(e2, e3))) * sc2;
      const float p0 = __builtin_amdgcn_exp2f(fmaf(e0, sc2, -mx)), p1 = __builtin_amdgcn_exp2f(fmaf(e1, sc2, -mx));
      const float p2 = __builtin_amdgcn_exp2f(fmaf(e2, sc2, -mx)), p3 = __builtin_amdgcn_exp2f(fmaf(e3, sc2, -mx));
      const u32x2 k01 = __builtin_bit_cast(u32x2, pack4((f32x4){p0, 0.f, p1, 0.f})), k23 = __builtin_bit_cast(u32x2, pack4((f32x4){p2, 0.f, p3, 0.f}));
      const u32x4 pw4 = {k01[0] << psh, k01[1] << psh, k23[0] << psh, k23[1] << psh};
      const bf16x8 pf = __builtin_bit_cast(bf16x8, pw4);
      const bf16x8 vf = __builtin_bit_cast(bf16x8, __builtin_shufflevector(vp, vc, 0, 1, 2, 3, 4, 5, 6, 7));
      const f32x4 o = mfma32(vf, pf, z), sm = mfma32(ones8, pf, z);
      return __builtin_bit_cast(u32x2, pack4(o * __builtin_amdgcn_rcpf(sm[0])));
    };
    u32x2 O[4];
#pragma unroll
    for (int rt = 1; rt < 4; ++rt) O[rt] = attn(K[rt - 1], V[rt - 1], K[rt], V[rt], Q[rt], mkp, rt);
    LAS char* ox = lds + SC_OX + w * 1024;
    *(LAS u32x2*)(ox + lane * 8) = (nj & 1) ? O[1] : O[3];
    if (nj & 1) *(LAS u32x2*)(ox + 512 + lane * 8) = O[2];
    WAITL0(); BAR();
    {
      LAS char* oo = lds + SC_OX + (w ^ 1) * 1024;
      const u32x2 p0 = *(LAS u32x2*)(oo + lane * 8);
      if (nj & 1) st16(ohr, od + 3 * NKS * 1024, (u32x4){p0[0], p0[1], O[3][0], O[3][1]});
      else {
        const u32x2 p1 = *(LAS u32x2*)(oo + 512 + lane * 8);
        st16(ohr, od + 1 * NKS * 1024, (u32x4){O[1][0], O[1][1], p0[0], p0[1]});
        st16(ohr, od + 2 * NKS * 1024, (u32x4){O[2][0], O[2][1], p1[0], p1[1]});
      }
    }
    {
      LAS char* fo = lds + SC_FX + (mi * 4 + nj) * 2048 + lane * 8;
      const s16x4 g0 = *(LAS s16x4*)fo, u0 = *(LAS s16x4*)(fo + 512), g1 = *(LAS s16x4*)(fo + 1024), u1 = *(LAS s16x4*)(fo + 1536);
      const u32x2 h0 = __builtin_bit_cast(u32x2, pack4(swiglu4(unpack4(g0), unpack4(u0)))), h1 = __builtin_bit_cast(u32x2, pack4(swiglu4(unpack4(g1), unpack4(u1))));
      st16(ohr, ohl + ((RT0 + nj) * NKS + 16 + cg) * 1024, (u32x4){h0[0], h0[1], h1[0], h1[1]});
    }
    s16x4 Kp, Vp;
    if (mi == 0) { Kp = hv; Vp = __builtin_amdgcn_ds_read_tr16_b64_v4i16((LAS s16x4*)(kvb + (4 * g + q) * 32 + pp * 8)); }
    else { Kp = *(LAS s16x4*)(kvb + 512 + lane * 8); Vp = *(LAS s16x4*)(kvb + 1024 + lane * 8); }
    const float ninf = -__builtin_inff();
    O[0] = attn(Kp, Vp, K[0], V[0], Q[0], RT0 == 0 ? (f32x4){ninf, ninf, ninf, ninf} : mkp, 0);
    st8(ohr, od + (nj & 1) * 8, __builtin_bit_cast(s16x4, O[0]));
  }
  STAMP(6);
  return tag;
}

constexpr int NR = (NKS + 3) / 4;
#define SC1 " sc1"
DI void gload16(u32x4& dst, const char* sbase, unsigned voff) { asm volatile("global_load_dwordx4 %0, %1, %2" SC1 : "=v"(dst) : "v"(voff), "s"(sbase) : "memory"); }
DI void gload8(u32x2& dst, const char* sbase, unsigned voff) { asm volatile("global_load_dwordx2 %0, %1, %2" SC1 : "=v"(dst) : "v"(voff), "s"(sbase) : "memory"); }
template <int N> DI void waitVn() { asm volatile("s_waitcnt vmcnt(%0)" :: "n"(N) : "memory"); }
#define P2AV 6
constexpr int P2A = P2AV;
constexpr int P2NA = P2A + 1;
constexpr int P2R = 4;
__host__ __device__ constexpr int bslot2(int r) { return r < 8 ? bslot(r) : r < 12 ? bslot(r - 8) : r == 12 ? SC_HS : bslot(r - 9); }
constexpr bool ksplit(int ks) { return false; }
constexpr bool psplit(int ke) { return ksplit(ke) || ksplit(ke + 1); }
constexpr int nA(int r) { return (psplit(4 * phys(r)) ? 2 : 1) + (psplit(4 * phys(r) + 2) ? 2 : 1); }
constexpr int p2issued(int r) { int n = 14; for (int q = 0; q < P2A; ++q) n += nA(q); for (int q = 0; q < r; ++q) n += (q + P2A < NR ? nA(q + P2A) : 0) + (q == 6 ? 4 : 0) + (q == 9 ? 2 : 0); return n; }
constexpr int p2posA(int r) { if (r < P2A) { int n = 14; for (int q = 0; q <= r; ++q) n += nA(q); return n; } return p2issued(r - P2A) + nA(r); }
constexpr int p2posB14() { return p2issued(6) + nA(6 + P2A) + 4; }
template <int R> struct P2W { static constexpr int n = p2issued(R) - ((R == 12 && p2posB14() > p2posA(R)) ? p2posB14() : p2posA(R)); };

DI void phase2(const Params& p, LAS char* lds, int rt64, int ct64, unsigned tag, bool boss) {
  const int tid = threadIdx.x, lane = tid & 63;
  const int w = rfl(tid >> 6), kh = w >> 2, m4 = w & 3;
  const int r16 = lane & 15, g = lane >> 4, q = r16 >> 2, pp = lane & 3;
  const unsigned ldsb = (unsigned)(uintptr_t)lds;
  constexpr int PH = 1; STAMP(0);
  const char* pa = p.ws + WS_OH + ((size_t)(rt64 * 4 + m4) * NKS + kh) * 1024;
  const unsigned va = lane * 16;
  unsigned vb;
  { const int row = 8 * m4 + (lane >> 3), pc = lane & 7; const int c = (pc >> 1) ^ ((row >> 1) & 3);
    vb = row * 1024 + ct64 * 128 + (2 * c + (pc & 1)) * 16; }
  const char* pb2 = p.w2 + (long)(kh - 16) * 32768;
  const char* pbo = p.wo + (long)kh * 32768;
  int bl[4];
#pragma unroll
  for (int c = 0; c < 4; ++c) bl[c] = (4 * g + q) * 128 + ((c ^ ((2 * g + (q >> 1)) & 3)) << 5) + pp * 8 + kh * 4096;
  auto readB = [&](bf16x8 (&dst)[2][4], int r) {
    LAS char* sb = lds + bslot2(r);
#pragma unroll
    for (int j = 0; j < 2; ++j)
#pragma unroll
      for (int c = 0; c < 4; ++c) dst[j][c] = ldsB(sb + j * 8192 + bl[c], 2048);
  };
  auto issueB = [&](int r) {
#pragma unroll
    for (int j = 0; j < 2; ++j) {
      const int ke = 4 * phys(r) + 2 * j;
      const bool over = (ke + 1 > NKS - 1);
      glds16s((ke < 16 ? pbo : pb2) + (over ? (long)ke - kh : (long)ke) * 32768, vb, ldsb + bslot2(r) + (2 * j) * 4096 + kh * 4096 + m4 * 1024);
    }
  };
  bf16x8 bp[P2R][2][4];
#pragma unroll
  for (int r = 0; r < P2R; ++r) readB(bp[r], r);
  WAITL0(); BAR();
#pragma unroll
  for (int r = 6; r <= 12; ++r) issueB(r);
  if (w == 0) {
    gu64* fl = (gu64*)(p.ws + WS_FLAGS) + (rt64 >> 1) * 16 + (lane & 15);
    const unsigned long long want = FMAGIC | tag;
    for (unsigned spins = 0; spins < (1u << 20); ++spins) {
      unsigned long long v;
      asm volatile("global_load_dwordx2 %0, %1, off sc1\n\ts_waitcnt vmcnt(0)" : "=v"(v) : "v"(fl) : "memory");
      if (__all(v == want)) break;
      __builtin_amdgcn_s_sleep(1);
    }
  }
  BAR();
  STAMP(1);
  u32x4 A4[P2NA][2]; u32x2 Al[P2NA][2], Ah[P2NA][2];
  const unsigned vs0 = lane * 8, vs1 = lane * 8 + 512, vn1 = lane * 16 + 8;
  auto issueA = [&](int r) {
#pragma unroll
    for (int j = 0; j < 2; ++j) {
      const int ke = 4 * phys(r) + 2 * j;
      const bool over = (ke + 1 > NKS - 1);
      const char* src = pa + (over ? (long)ke - kh : (long)ke) * 1024;
      if (!psplit(ke)) gload16(A4[r % P2NA][j], src, va);
      else if (ksplit(ke) && ksplit(ke + 1)) { gload8(Al[r % P2NA][j], src, vs0); gload8(Ah[r % P2NA][j], src, vs1); }
      else { const bool sp = ksplit(ke) ? (kh == 0) : (kh == 1); gload8(Al[r % P2NA][j], src, sp ? vs0 : va); gload8(Ah[r % P2NA][j], src, sp ? vs1 : vn1); }
    }
  };
  auto pinA = [&](int r) {
#pragma unroll
    for (int j = 0; j < 2; ++j) {
      if (psplit(4 * phys(r) + 2 * j)) asm volatile("" : "+v"(Al[r % P2NA][j]), "+v"(Ah[r % P2NA][j])); else asm volatile("" : "+v"(A4[r % P2NA][j]));
    }
  };
  auto getA = [&](int r, int j) {
    return psplit(4 * phys(r) + 2 * j) ? __builtin_bit_cast(bf16x8, __builtin_shufflevector(Al[r % P2NA][j], Ah[r % P2NA][j], 0, 1, 2, 3)) : __builtin_bit_cast(bf16x8, A4[r % P2NA][j]);
  };
#pragma unroll
  for (int r = 0; r < P2A; ++r) { issueA(r); if (r + 1 < P2A) BAR(); }
  f32x4 acc[4] = {{0, 0, 0, 0}, {0, 0, 0, 0}, {0, 0, 0, 0}, {0, 0, 0, 0}};
  const size_t ooff = (size_t)(rt64 * 64 + 16 * m4 + r16) * 1024 + (ct64 * 64 + 32 * kh + 4 * g) * 2;
  bf16x4 xv0, xv1;
  u32x4 fl0 = {0, 0, 0, 0}, fl1 = {0, 0, 0, 0};
  bf16x8 bf[2][2][4];
  auto round = [&](auto RC) {
    constexpr int r = decltype(RC)::value;
    waitVn<P2W<r>::n>();
    pinA(r);
    if (r == 5 || r == 6 || r == 12) BAR();
    if (r + P2A < NR) issueA(r + P2A);
    if (r == 6) { issueB(13); issueB(14); }
    if (r == 9) { asm volatile("global_load_dwordx2 %0, %2, %3\n\tglobal_load_dwordx2 %1, %2, %3 offset:32" : "=&v"(xv0), "=&v"(xv1) : "v"((unsigned)ooff), "s"(p.x) : "memory"); }
    if (r == 0) STAMP(2);
    if (r == 8) STAMP(3);
    if (r == 11 && boss) {
      const char* fb = p.ws + WS_FLAGS;
      asm volatile("global_load_dwordx4 %0, %2, %3 sc1\n\tglobal_load_dwordx4 %1, %2, %3 offset:1024 sc1" : "=&v"(fl0), "=&v"(fl1) : "v"(va), "s"(fb) : "memory");
    }
    SB0();
    if (r + 1 < NR && r + 1 >= P2R) readB(bf[(r + 1) & 1], r + 1);
#pragma unroll
    for (int j = 0; j < 2; ++j) {
      if (4 * phys(r) + 2 * j + 1 > NKS - 1 && kh) continue;
#pragma unroll
      for (int c = 0; c < 4; ++c) acc[c] = mfma32(r < P2R ? bp[r < P2R ? r : 0][j][c] : bf[r & 1][j][c], getA(r, j), acc[c]);
    }
#pragma unroll
    for (int k = 0; k < 8; ++k) {
      __builtin_amdgcn_sched_group_barrier(0x8, 1, 0);
      __builtin_amdgcn_sched_group_barrier(0x100, 2, 0);
    }
    SB0();
  };
  static_for<NR>(round);
  STAMP(4);
  {
    LAS f32x4* red = (LAS f32x4*)(lds + SC_RED);
    red[(w * 2 + 0) * 64 + lane] = kh ? acc[0] : acc[2];
    red[(w * 2 + 1) * 64 + lane] = kh ? acc[1] : acc[3];
    WAITL0(); BAR();
    const f32x4 o0 = red[((w ^ 4) * 2 + 0) * 64 + lane], o1 = red[((w ^ 4) * 2 + 1) * 64 + lane];
    const f32x4 s0 = (kh ? acc[2] : acc[0]) + o0, s1 = (kh ? acc[3] : acc[1]) + o1;
    asm volatile("s_waitcnt vmcnt(0)" : "+v"(xv0), "+v"(xv1) :: "memory");
    const f32x4 x0 = __builtin_convertvector(xv0, f32x4), x1 = __builtin_convertvector(xv1, f32x4);
    const u32x2 pa2 = __builtin_bit_cast(u32x2, pack4(s0 + x0)), pb2_ = __builtin_bit_cast(u32x2, pack4(s1 + x1));
    const auto d0 = __builtin_amdgcn_permlane16_swap(pa2[0], pb2_[0], false, false), d1 = __builtin_amdgcn_permlane16_swap(pa2[1], pb2_[1], false, false);
    const __amdgpu_buffer_rsrc_t outr = __builtin_amdgcn_make_buffer_rsrc(p.out, 0, 2048 * 1024, 0x00020000);
    st16(outr, (unsigned)((rt64 * 64 + 16 * m4 + r16) * 1024 + (ct64 * 64 + 32 * kh + (g & 1) * 16 + (g >> 1) * 8) * 2), (u32x4){d0[0], d1[0], d0[1], d1[1]});
  }
  STAMP(5);
  if (boss) {
    asm volatile("s_waitcnt vmcnt(0)" : "+v"(fl0), "+v"(fl1) :: "memory");
    const unsigned hi = (unsigned)(FMAGIC >> 32);
    bool ok = fl0[0] == tag && fl0[1] == hi && fl0[2] == tag && fl0[3] == hi && fl1[0] == tag && fl1[1] == hi && fl1[2] == tag && fl1[3] == hi;
    if (!__all(ok)) {
      gu64* fl = (gu64*)(p.ws + WS_FLAGS) + lane;
      const unsigned long long want = FMAGIC | tag;
      for (unsigned spins = 0; spins < (1u << 20); ++spins) {
        ok = __hip_atomic_load(fl, RLX_AGENT) == want && __hip_atomic_load(fl + 64, RLX_AGENT) == want &&
             __hip_atomic_load(fl + 128, RLX_AGENT) == want && __hip_atomic_load(fl + 192, RLX_AGENT) == want;
        if (__all(ok)) break;
        __builtin_amdgcn_s_sleep(1);
      }
    }
    if (lane == 0) __hip_atomic_store((gu32*)p.ws, tag, RLX_AGENT);
  }
}

__global__ __launch_bounds__(512, 2) void k_fused(Params p) {
  extern __shared__ __attribute__((aligned(16))) char shm[];
  LAS char* lds = (LAS char*)shm;
  const unsigned long long t00 = __builtin_amdgcn_s_memrealtime();
  if (blockIdx.x == ANCHOR && threadIdx.x == 0) __hip_atomic_store((gu64*)(p.ws + 2048), t00, RLX_AGENT);
  const int b = blockIdx.x, xcd = b & 7, idx = b >> 3;
  const int rg = (xcd & 1) * 8 + (idx & 7), cg = (xcd >> 1) * 4 + (idx >> 3);
  const int rt64 = xcd * 4 + (idx & 3), ct64 = idx >> 2;
  const char* code0 = (const char*)(__builtin_amdgcn_s_getpc() & ~(unsigned long long)255);
  const unsigned tag = (cg < 8) ? phase1<true>(p, lds, rg, cg, ct64, idx, code0) : phase1<false>(p, lds, rg, cg - 8, ct64, idx, code0);
  asm volatile("s_waitcnt vmcnt(0)" ::: "memory");
  BAR();
  if (threadIdx.x == 0) __hip_atomic_store((gu64*)(p.ws + WS_FLAGS) + rg * 16 + cg, FMAGIC | tag, RLX_AGENT);
  { constexpr int PH = 0; STAMP(7); }
  phase2(p, lds, rt64, ct64, tag, b == 0 && rfl(threadIdx.x >> 6) == 0);
  { const unsigned long long ta = __hip_atomic_load((gu64*)(p.ws + 2048), RLX_AGENT); while (__builtin_amdgcn_s_memrealtime() - ta < 4000ull) __builtin_amdgcn_s_sleep(8); }
}

extern "C" void kernel_launch(void* const* d_in, const int* in_sizes, int n_in, void* d_out, int out_size, void* d_ws, size_t ws_size,
                              hipStream_t stream) {
  Params p;
  p.x = (const char*)d_in[0]; p.wqkv = (const char*)d_in[1]; p.wo = (const char*)d_in[2];
  p.w1 = (const char*)d_in[4]; p.w3 = (const char*)d_in[5]; p.w2 = (const char*)d_in[6];
  p.ws = (char*)d_ws; p.out = (char*)d_out; p.stamps = (unsigned long long*)((char*)d_ws + (128u << 20));
  (void)hipFuncSetAttribute((const void*)k_fused, hipFuncAttributeMaxDynamicSharedMemorySize, P1_LDS);
  k_fused<<<256, 512, P1_LDS, stream>>>(p);
}
```

```cpp
#define P2AV 5
#include <hip/hip_runtime.h>
#include <stdint.h>

#define LAS __attribute__((address_space(3)))
#define DI __device__ __forceinline__
typedef __bf16 bf16x8 __attribute__((ext_vector_type(8)));
typedef __bf16 bf16x4 __attribute__((ext_vector_type(4)));
typedef short s16x4 __attribute__((ext_vector_type(4)));
typedef float f32x4 __attribute__((ext_vector_type(4)));
typedef float f32x2 __attribute__((ext_vector_type(2)));
typedef unsigned u32x2 __attribute__((ext_vector_type(2)));
typedef unsigned u32x4 __attribute__((ext_vector_type(4)));

constexpr int NKS = 59;
constexpr int SLOT = 53248;
constexpr int WOFF = 16384, EOFF = 49152;
constexpr int DUMMY = 3 * SLOT;
constexpr int SC_CS = DUMMY + 1024;
constexpr int SC_MR = SC_CS + 1152;
constexpr int P1_LDS = 163840;
constexpr int SC_HS = 86016;
constexpr int SC_KV = SLOT;
constexpr int SC_FX = SC_KV + 6144;
constexpr int SC_OX = SC_FX + 16384;
constexpr int SC_RED = SLOT;
__host__ __device__ constexpr int bslot(int r) { return (r & 7) < 3 ? 2 * SLOT + (r & 7) * 16384 : (r & 7) < 6 ? ((r & 7) - 3) * 16384 : SLOT + ((r & 7) - 6) * 16384; }
__host__ __device__ constexpr int phys(int r) { return (r + 6) % 15; }
constexpr unsigned long long FMAGIC = 0x5AC3E17Bull << 32;
constexpr size_t WS_FLAGS = 4096, WS_STATS = 8192, WS_OH = 65536;

#define STAMP(i) do {} while (0)

template <int V> struct IC { static constexpr int value = V; };
template <int N, int I = 0, class F> DI void static_for(F& f) { if constexpr (I < N) { f(IC<I>{}); static_for<N, I + 1>(f); } }
DI int rfl(int v) { return __builtin_amdgcn_readfirstlane(v); }
DI void glds16(const void* g, unsigned lds_dst) {
  unsigned keep;
  asm volatile("s_mov_b32 %0, m0\n\ts_mov_b32 m0, %2\n\ts_nop 0\n\tglobal_load_lds_dwordx4 %1, off\n\ts_mov_b32 m0, %0"
               : "=&s"(keep) : "v"(g), "s"(lds_dst) : "memory");
}
DI void glds16s(const char* sbase, unsigned voff, unsigned lds_dst) {
  asm volatile("s_mov_b32 m0, %2\n\ts_nop 0\n\tglobal_load_lds_dwordx4 %1, %0" :: "s"(sbase), "v"(voff), "s"(lds_dst) : "memory", "m0");
}
#define WAITV(n) asm volatile("s_waitcnt vmcnt(" #n ")" ::: "memory")
#define WAITL0() do { asm volatile("" ::: "memory"); __builtin_amdgcn_s_waitcnt(0xC07F); asm volatile("" ::: "memory"); } while (0)
#define BAR() do { asm volatile("" ::: "memory"); __builtin_amdgcn_s_barrier(); asm volatile("" ::: "memory"); } while (0)
#define SB0() __builtin_amdgcn_sched_barrier(0)

DI bf16x8 ldsA(LAS char* p) { return *(LAS bf16x8*)p; }
DI bf16x8 ldsB(LAS char* p, int toff) {
  s16x4 lo = __builtin_amdgcn_ds_read_tr16_b64_v4i16((LAS s16x4*)p);
  s16x4 hi = __builtin_amdgcn_ds_read_tr16_b64_v4i16((LAS s16x4*)(p + toff));
  return __builtin_bit_cast(bf16x8, __builtin_shufflevector(lo, hi, 0, 1, 2, 3, 4, 5, 6, 7));
}
DI f32x4 mfma32(bf16x8 a, bf16x8 b, f32x4 c) { return __builtin_amdgcn_mfma_f32_16x16x32_bf16(a, b, c, 0, 0, 0); }
DI f32x4 mfma16(s16x4 a, s16x4 b, f32x4 c) { return __builtin_amdgcn_mfma_f32_16x16x16bf16_1k(a, b, c, 0, 0, 0); }
DI s16x4 pack4(f32x4 v) { return __builtin_bit_cast(s16x4, __builtin_convertvector(v, bf16x4)); }
DI f32x4 unpack4(s16x4 v) { return __builtin_convertvector(__builtin_bit_cast(bf16x4, v), f32x4); }
DI float xg_sum(float v) {
  auto r = __builtin_amdgcn_permlane16_swap(__float_as_uint(v), __float_as_uint(v), false, false);
  v = __uint_as_float(r[0]) + __uint_as_float(r[1]);
  auto s = __builtin_amdgcn_permlane32_swap(__float_as_uint(v), __float_as_uint(v), false, false);
  return __uint_as_float(s[0]) + __uint_as_float(s[1]);
}
DI float xg_max(float v) {
  auto r = __builtin_amdgcn_permlane16_swap(__float_as_uint(v), __float_as_uint(v), false, false);
  v = fmaxf(__uint_as_float(r[0]), __uint_as_float(r[1]));
  auto s = __builtin_amdgcn_permlane32_swap(__float_as_uint(v), __float_as_uint(v), false, false);
  return fmaxf(__uint_as_float(s[0]), __uint_as_float(s[1]));
}
DI float row_sum16(float v) {
  v += __builtin_bit_cast(float, __builtin_amdgcn_update_dpp(0, __builtin_bit_cast(int, v), 0x128, 0xf, 0xf, false));
  v += __builtin_bit_cast(float, __builtin_amdgcn_update_dpp(0, __builtin_bit_cast(int, v), 0x124, 0xf, 0xf, false));
  v += __builtin_bit_cast(float, __builtin_amdgcn_update_dpp(0, __builtin_bit_cast(int, v), 0x122, 0xf, 0xf, false));
  v += __builtin_bit_cast(float, __builtin_amdgcn_update_dpp(0, __builtin_bit_cast(int, v), 0x121, 0xf, 0xf, false));
  return v;
}
DI f32x4 lnc(f32x4 a, f32x2 mr, f32x4 c) { return (a - c * mr[0]) * mr[1]; }
DI f32x4 swiglu_ln(f32x4 ag, f32x4 au, f32x2 mr, f32x4 cg, f32x4 cu) {
  const f32x4 g = ag - cg * mr[0], u = au - cu * mr[0];
  const f32x4 t = g * (mr[1] * -1.44269504089f);
  const float ir2 = __builtin_amdgcn_rcpf(mr[1] * mr[1]);
  f32x4 e, d;
#pragma unroll
  for (int r = 0; r < 4; ++r) e[r] = __builtin_amdgcn_exp2f(t[r]);
  e = e * ir2 + ir2;
#pragma unroll
  for (int r = 0; r < 4; ++r) d[r] = __builtin_amdgcn_rcpf(e[r]);
  return g * u * d;
}
DI f32x4 swiglu4(f32x4 g, f32x4 u) {
  const f32x4 t = g * -1.44269504089f;
  f32x4 d;
#pragma unroll
  for (int r = 0; r < 4; ++r) d[r] = __builtin_amdgcn_rcpf(1.0f + __builtin_amdgcn_exp2f(t[r]));
  return g * u * d;
}

struct Params {
  const char* x; const char* wqkv; const char* wo; const char* w1; const char* w3; const char* w2;
  char* ws; char* out; unsigned long long* stamps;
};
typedef __attribute__((address_space(1))) unsigned gu32;
typedef __attribute__((address_space(1))) unsigned long long gu64;
#define RLX_AGENT __ATOMIC_RELAXED, __HIP_MEMORY_SCOPE_AGENT
DI void st16(__amdgpu_buffer_rsrc_t r, unsigned off, u32x4 v) { __builtin_amdgcn_raw_buffer_store_b128(v, r, off, 0, 16); }
DI void st8(__amdgpu_buffer_rsrc_t r, unsigned off, s16x4 v) { __builtin_amdgcn_raw_buffer_store_b64(__builtin_bit_cast(u32x2, v), r, off, 0, 16); }

struct Frags { bf16x8 a[4], ah, b[4], bx, be, e0; };

template <bool TA>
DI unsigned phase1(const Params& p, LAS char* lds, int rg, int cg, int ct64, int idx, const char* code0) {
  const int tid = threadIdx.x, lane = tid & 63;
  const int w = rfl(tid >> 6), mi = w >> 2, nj = w & 3;
  const int r16 = lane & 15, g = lane >> 4, q = r16 >> 2, pp = lane & 3;
  constexpr int PH = 0; STAMP(0);
  const unsigned ldsb = (unsigned)(uintptr_t)lds;
  const int row0 = rg * 128;
  const int srow = row0 + (TA ? cg : 8 + cg) * 8 + w;
  unsigned ev; u32x4 xrow;
  asm volatile("global_load_dword %0, %1, %2 sc1" : "=v"(ev) : "v"(0u), "s"(p.ws) : "memory");
  asm volatile("global_load_dwordx4 %0, %1, %2" : "=v"(xrow) : "v"((unsigned)(srow * 1024 + lane * 16)), "s"(p.x) : "memory");
  const int tm = TA ? 2 * cg : 16 + 18 * (cg >> 1) + ((cg & 1) ? 10 : 0);
  const int te = 16 + 18 * (cg >> 1) + 8 + (cg & 1);
  unsigned vx[2], vw[4], ve;
#pragma unroll
  for (int m = 0; m < 2; ++m) {
    const int row = 16 * w + 8 * m + (lane >> 3);
    vx[m] = (unsigned)((row0 + row) * 1024 + (((lane & 7) ^ ((row >> 1) & 7)) << 4));
  }
  const char* bw[4]; unsigned ldw[4], dw[4];
#pragma unroll
  for (int m = 0; m < 4; ++m) {
    if (!TA || m < 2) {
      const int pi = TA ? (2 * w + m) : (2 * w + (m & 1));
      const int kr = 4 * pi + (lane >> 4), pc = lane & 15;
      const int c = (pc >> 1) ^ ((kr & 3) | (((kr >> 3) & 1) << 2));
      if (TA) { bw[m] = p.wqkv; ldw[m] = 3072; vw[m] = kr * 3072 + ((c >> 2) * 512 + (4 * cg + (c & 3)) * 16) * 2 + (pc & 1) * 16; dw[m] = WOFF + pi * 1024; }
      else { bw[m] = (m >> 1) ? p.w3 : p.w1; ldw[m] = 2752; int t = tm + c; t = t > 85 ? 85 : t; vw[m] = kr * 2752 + t * 32 + (pc & 1) * 16; dw[m] = WOFF + (m >> 1) * 16384 + pi * 1024; }
    } else if (m == 2) {
      const int kr = 8 * w + (lane >> 3), pc = lane & 7;
      const int c = (pc >> 1) ^ (((kr >> 1) & 1) | (((kr >> 3) & 1) << 1));
      bw[m] = p.wqkv; ldw[m] = 3072; vw[m] = kr * 3072 + (1024 + (4 * cg + c) * 16) * 2 + (pc & 1) * 16; dw[m] = WOFF + 16384 + w * 1024;
    } else {
      const int kr = 16 * (w & 3) + (lane >> 2), pc = lane & 3;
      const int e = (pc >> 1) ^ ((kr >> 3) & 1);
      bw[m] = mi ? p.w3 : p.w1; ldw[m] = 2752; vw[m] = kr * 2752 + (tm + e) * 32 + (pc & 1) * 16; dw[m] = WOFF + 24576 + mi * 4096 + (w & 3) * 1024;
    }
  }
  const bool has_e = TA ? (w < 2) : (w < 4);
  const char* be_; unsigned lde, de;
  if (TA) {
    const int row = 8 * (w & 1) + (lane >> 3);
    int gr = row0 - 16 + row; gr = gr < 0 ? 0 : gr;
    be_ = p.x; lde = 2; ve = gr * 1024 + (((lane & 7) ^ ((row >> 1) & 7)) << 4); de = EOFF + w * 1024;
  } else {
    const int pos = 32 * (w & 1) + (lane >> 1), kr = pos ^ (((pos >> 3) & 1) << 2);
    be_ = (w & 2) ? p.w3 : p.w1; lde = 2752; ve = kr * 2752 + te * 32 + (lane & 1) * 16; de = EOFF + (w & 3) * 1024;
  }
  if (!has_e) { be_ = p.x; lde = 0; ve = 0; }
  unsigned vb;
  { const int row = 8 * nj + (lane >> 3), pc = lane & 7; const int c = (pc >> 1) ^ ((row >> 1) & 3);
    vb = row * 1024 + ct64 * 128 + (2 * c + (pc & 1)) * 16; }
  const unsigned emask = has_e ? 0xffffffffu : 0u;
  if (!has_e) de = DUMMY;
  const char* sx = p.x; const char* se = be_; const char* sw[4] = {bw[0], bw[1], bw[2], bw[3]};
  auto dma = [&](int i, int so) {
    if (i == 0) { glds16s(se, ve, ldsb + (so & emask) + de); se += 64u * lde; }
    else if (i <= 2) { glds16s(sx, vx[i - 1], ldsb + so + (2 * w + i - 1) * 1024); if (i == 2) sx += 128; }
    else { glds16s(sw[i - 3], vw[i - 3], ldsb + so + dw[i - 3]); sw[i - 3] += 64u * ldw[i - 3]; }
  };
  auto dmab = [&](int rr, int j) {
    const int ke = 4 * phys(rr) + 2 * j;
    const int ks2 = (ke + 1 > NKS - 1) ? ke : ke + mi;
    const char* src = (ke < 16) ? p.wo + ks2 * 32768 : p.w2 + (ks2 - 16) * 32768;
    glds16s(src, vb, ldsb + bslot(rr) + (2 * j + mi) * 4096 + nj * 1024);
  };
#pragma unroll
  for (int i = 0; i < 7; ++i) dma(i, 0);
#pragma unroll
  for (int i = 0; i < 7; ++i) dma(i, SLOT);
  STAMP(1);
  const char* pfp; unsigned pfs; const char* pfw; const char* pfw2;
  {
    const int l5 = lane & 31;
    const int L = w * 10 + (l5 < 10 ? l5 : 9);
    if (L < 32) { pfp = p.x + (size_t)(row0 + 32 * (idx >> 3) + L) * 1024; pfs = 128; }
    else {
      const int L2 = L - 32, ri = L2 / 6, sg = L2 - 6 * ri;
      const int kr = 8 * (idx & 7) + ri;
      if (TA) { pfp = (sg < 3) ? p.wqkv + (size_t)kr * 3072 + sg * 1024 + cg * 128 : (sg == 3 ? p.w1 : p.w3) + (size_t)kr * 2752 + tm * 32; pfs = (sg < 3) ? 64u * 3072 : 64u * 2752; }
      else { int cb = (tm < te ? tm : te) * 32 + (sg % 3) * 128; cb = cb > 2748 ? 2748 : cb; pfp = (sg < 3 ? p.w1 : p.w3) + (size_t)kr * 2752 + cb; pfs = 64u * 2752; }
    }
    { const int l0 = (idx * 8 + w) * 27 + (lane < 14 ? lane : 13), l1 = (idx * 8 + w) * 27 + 14 + (lane < 13 ? lane : 12);
      auto lineaddr = [&](int l) { return l < 4096 ? p.wo + (size_t)l * 128 : l < 6144 ? p.w2 + (size_t)(l - 4096) * 128 : p.w2 + (size_t)1280 * 1024 + (size_t)(l - 6144) * 128; };
      pfw = lineaddr(l0); pfw2 = lineaddr(l1); }
  }
  auto pf = [&](const char* a) { asm volatile("s_mov_b32 m0, %1\n\ts_nop 0\n\tglobal_load_lds_dword %0, off" :: "v"(a), "s"(ldsb + DUMMY) : "memory", "m0"); };
  {
    const char* a = pfp + (size_t)((lane < 32 ? 2u : 3u) * pfs);
    if (lane == 10) a = code0 + (size_t)((idx * 8 + w) & 255) * 128;
    pf(a);
  }
  const int xa0 = r16 * 128 + ((g ^ (r16 >> 1)) << 4);
  const int fl = q | ((g & 1) << 2);
  const int w256 = (8 * g + q) * 256 + (fl << 5) + pp * 8;
  const int w128 = (8 * g + q) * 128 + ((((q >> 1) & 1) | ((g & 1) << 1)) << 5) + pp * 8;
  const int w64 = (8 * g + q) * 64 + ((g & 1) << 5) + pp * 8;
  const int w32 = (8 * g + ((g & 1) << 2) + q) * 32 + pp * 8;
  const int dt32 = (g & 1) ? -128 : 128;
  const int tb0 = (w256 ^ (nj << 6)) + (mi ? 16384 : 0), tb2 = (w256 ^ (nj << 6)) + (mi ? 0 : 16384);
  const int tbx = EOFF + (nj & 1) * 2048 + w32;
  const int taq = w256 ^ (nj << 5), tak = w256 ^ ((4 + nj) << 5), tav = 16384 + (w128 ^ (nj << 5)), taf = 24576 + (nj & 1) * 4096 + (w64 ^ ((nj >> 1) << 5));
  const int fu = 4 + ((nj >> 1) | ((nj & 1) << 1));
  int xr[4];
#pragma unroll
  for (int i = 0; i < 4; ++i) xr[i] = mi * 8192 + (TA ? i : ((i + nj) & 3)) * 2048;
  const int bea = mi ? tav : tak, bes = mi ? 4096 : 8192, bet = mi ? 512 : 1024;
  const int e0a = mi ? taf : taq, e0s = mi ? 2048 : 8192, e0t = mi ? 256 : 1024;
  constexpr int NRD = TA ? 11 : 9;
  auto ldsB2 = [&](LAS char* p0, LAS char* p1) {
    s16x4 lo = __builtin_amdgcn_ds_read_tr16_b64_v4i16((LAS s16x4*)p0), hi = __builtin_amdgcn_ds_read_tr16_b64_v4i16((LAS s16x4*)p1);
    return __builtin_bit_cast(bf16x8, __builtin_shufflevector(lo, hi, 0, 1, 2, 3, 4, 5, 6, 7));
  };
  auto rdf = [&](Frags& F, int i, int so, int ks) {
    LAS char* sb = lds + so;
    const int xa = xa0 ^ (ks * 64);
    LAS char* wb = sb + WOFF;
    if (TA) {
      switch (i) {
        case 0: F.a[0] = ldsA(sb + xa + xr[0]); break;
        case 1: F.b[0] = ldsB(wb + taq + ks * 8192, 1024); break;
        case 2: F.b[1] = ldsB(wb + tak + ks * 8192, 1024); break;
        case 3: F.b[2] = ldsB(wb + tav + ks * 4096, 512); break;
        case 4: F.b[3] = ldsB(wb + taf + ks * 2048, 256); break;
        case 5: F.a[1] = ldsA(sb + xa + xr[1]); break;
        case 6: F.a[2] = ldsA(sb + xa + xr[2]); break;
        case 7: F.a[3] = ldsA(sb + xa + xr[3]); break;
        case 8: F.be = ldsB2(wb + bea + ks * bes, wb + bea + ks * bes + bet); break;
        case 9: F.ah = ldsA(sb + EOFF + xa); break;
        default: F.e0 = ldsB2(wb + e0a + ks * e0s, wb + e0a + ks * e0s + e0t); break;
      }
    } else {
      switch (i) {
        case 0: F.a[0] = ldsA(sb + xa + xr[0]); break;
        case 1: F.b[0] = ldsB(wb + tb0 + ks * 8192, 1024); break;
        case 2: F.b[1] = ldsB(wb + (tb0 ^ 32) + ks * 8192, 1024); break;
        case 3: F.b[2] = ldsB(wb + tb2 + ks * 8192, 1024); break;
        case 4: F.b[3] = ldsB(wb + (tb2 ^ 32) + ks * 8192, 1024); break;
        case 5: F.a[1] = ldsA(sb + xa + xr[1]); break;
        case 6: F.a[2] = ldsA(sb + xa + xr[2]); break;
        case 7: F.a[3] = ldsA(sb + xa + xr[3]); break;
        default: F.bx = ldsB2(sb + tbx + ks * 1024, sb + tbx + ks * 1024 + dt32); break;
      }
    }
  };
  f32x4 acc[4][4], accx[2], acch = {0, 0, 0, 0}, cs0 = {0, 0, 0, 0}, cs1 = {0, 0, 0, 0}, csx = {0, 0, 0, 0};
  accx[0] = (f32x4){0, 0, 0, 0}; accx[1] = (f32x4){0, 0, 0, 0};
#pragma unroll
  for (int c = 0; c < 4; ++c)
#pragma unroll
    for (int rt = 0; rt < 4; ++rt) acc[c][rt] = (f32x4){0, 0, 0, 0};
  const bf16x8 ones = {(__bf16)1.0f, (__bf16)1.0f, (__bf16)1.0f, (__bf16)1.0f, (__bf16)1.0f, (__bf16)1.0f, (__bf16)1.0f, (__bf16)1.0f};
  auto mm = [&](const Frags& F, int gi) {
    if (gi < 4) {
      acc[0][gi] = mfma32(F.b[0], F.a[gi], acc[0][gi]);
      acc[1][gi] = mfma32(F.b[1], F.a[gi], acc[1][gi]);
      acc[2][gi] = TA ? mfma32(F.a[gi], F.b[2], acc[2][gi]) : mfma32(F.b[2], F.a[gi], acc[2][gi]);
      acc[3][gi] = mfma32(F.b[3], F.a[gi], acc[3][gi]);
    } else if (TA) {
      acch = mfma32(F.be, F.ah, acch);
      cs0 = mfma32(F.e0, ones, cs0); cs1 = mfma32(F.be, ones, cs1);
    } else {
      accx[0] = mfma32(F.bx, F.a[0], accx[0]); accx[1] = mfma32(F.bx, F.a[1], accx[1]);
      cs0 = mfma32(F.b[0], ones, cs0); cs1 = mfma32(F.b[1], ones, cs1); csx = mfma32(F.bx, ones, csx);
    }
  };
  constexpr int RPG = (NRD + 4) / 5;
  auto half = [&](const Frags& Fc, Frags& Fn, int so_n, int ks_n, int mode, int d0, int nd, int so_d, int bt) {
#pragma unroll
    for (int gi = 0; gi < 5; ++gi) {
#define PRIO 3
      if (gi == 0) { if (ks_n == 0) __builtin_amdgcn_s_setprio(3); else __builtin_amdgcn_s_setprio(1); }
      if (gi == PRIO) { if (ks_n == 0) __builtin_amdgcn_s_setprio(2); else __builtin_amdgcn_s_setprio(0); }
#pragma unroll
      for (int i = 0; i < RPG; ++i) { const int idx = gi * RPG + i; if (idx < NRD) rdf(Fn, idx, so_n, ks_n); }
      mm(Fc, gi);
#pragma unroll
      for (int k = 0; k < 5; ++k) {
        __builtin_amdgcn_sched_group_barrier(0x8, 1, 0);
        __builtin_amdgcn_sched_group_barrier(0x100, 1, 0);
        __builtin_amdgcn_sched_group_barrier(0x2, 2, 0);
      }
      __builtin_amdgcn_sched_group_barrier(0x100, 8, 0);
      SB0();
      if (mode == 0 && gi < nd) { dma(d0 + gi, so_d); SB0(); }
      if (mode == 1 && gi < nd && d0 + gi < 6) { dmab(3 * bt + ((d0 + gi) >> 1), (d0 + gi) & 1); SB0(); }
    }
  };
  Frags F0, F1;
  WAITV(8); BAR();
  STAMP(2);
#pragma unroll
  for (int i = 0; i < NRD; ++i) rdf(F0, i, 0, 0);
#pragma unroll
  for (int i = 0; i < 3; ++i) dma(i, 2 * SLOT);
  half(F0, F1, 0, 1, 0, 3, 4, 2 * SLOT, 0);
  pf(pfp + (size_t)(4u * pfs));
  asm volatile("" : "+v"(ev), "+v"(xrow));
  const unsigned tag = (unsigned)rfl((int)ev) + 1u;
  {
    float s1 = 0.f, s2 = 0.f;
#pragma unroll
    for (int i = 0; i < 4; ++i) {
      const float lo = __uint_as_float(xrow[i] << 16), hi = __uint_as_float(xrow[i] & 0xffff0000u);
      s1 += lo + hi; s2 = fmaf(lo, lo, fmaf(hi, hi, s2));
    }
    s1 = xg_sum(row_sum16(s1)); s2 = xg_sum(row_sum16(s2));
    const float m = s1 * (1.0f / 512.0f), var = fmaxf(s2 * (1.0f / 512.0f) - m * m, 0.f);
    const float rs = __builtin_amdgcn_rsqf(var + 1e-5f);
    const __amdgpu_buffer_rsrc_t str = __builtin_amdgcn_make_buffer_rsrc(p.ws + WS_STATS, 0, 2048 * 16, 0x00020000);
    if (lane == 0) st16(str, srow * 16, (u32x4){__float_as_uint(m), tag, __float_as_uint(rs), tag});
  }
  int sop = 0, so = SLOT;
#pragma unroll 1
  for (int t = 1; t < 6; ++t) {
    WAITV(9); WAITL0(); BAR();
    half(F1, F0, so, 0, 0, 0, 4, sop, 0);
    half(F0, F1, so, 1, 0, 4, 3, sop, 0);
    pf(t <= 3 ? pfp + (size_t)((unsigned)(t + 4) * pfs) : (t == 4 ? pfw : pfw2));
    sop = so; so = (so == 2 * SLOT) ? 0 : so + SLOT;
  }
  WAITV(9); WAITL0(); BAR();
  u32x4 stg;
  { int r = (w < 2) ? row0 + 64 * w + lane : (w == 2 ? row0 - 16 + (lane & 15) : row0 + lane); r = r < 0 ? 0 : r;
    asm volatile("global_load_dwordx4 %0, %1, %2 sc1" : "=v"(stg) : "v"((unsigned)(r * 16)), "s"(p.ws + WS_STATS) : "memory"); }
  half(F1, F0, 0, 0, 1, 0, 4, 0, 0);
  half(F0, F1, 0, 1, 1, 4, 3, 0, 0);
  WAITV(8); WAITL0(); BAR();
  half(F1, F0, SLOT, 0, 1, 0, 4, 0, 1);
  half(F0, F1, SLOT, 1, 1, 4, 3, 0, 1);
#pragma unroll
  for (int gi = 0; gi < 5; ++gi) mm(F1, gi);
  STAMP(4);
  {
    LAS f32x4* CSw = (LAS f32x4*)(lds + SC_CS);
    if (r16 == 0) {
      const int u0 = TA ? (mi ? 8 + fu : nj) : (mi * 8 + 2 * nj), u1 = TA ? (mi ? 8 + nj : 4 + nj) : (mi * 8 + 2 * nj + 1);
      CSw[u0 * 4 + g] = cs0; CSw[u1 * 4 + g] = cs1;
    }
    asm volatile("s_waitcnt vmcnt(12)" : "+v"(stg) :: "memory");
    if (w < 3) {
      int r = (w < 2) ? row0 + 64 * w + lane : row0 - 16 + (lane & 15); r = r < 0 ? 0 : r;
      if (!__all(stg[1] == tag && stg[3] == tag)) {
        gu64* gp = (gu64*)(p.ws + WS_STATS) + r * 2;
        for (unsigned spins = 0; spins < (1u << 20); ++spins) {
          const unsigned long long a = __hip_atomic_load(gp, RLX_AGENT), b = __hip_atomic_load(gp + 1, RLX_AGENT);
          stg = (u32x4){(unsigned)a, (unsigned)(a >> 32), (unsigned)b, (unsigned)(b >> 32)};
          if (__all(stg[1] == tag && stg[3] == tag)) break;
          __builtin_amdgcn_s_sleep(1);
        }
      }
      if (w < 2 || lane < 16) ((LAS f32x2*)(lds + SC_MR))[64 * w + lane] = (f32x2){__uint_as_float(stg[0]), __uint_as_float(stg[2])};
    }
  }
  WAITL0(); BAR();
  STAMP(5);
  LAS f32x4* CS = (LAS f32x4*)(lds + SC_CS);
  LAS f32x2* MR = (LAS f32x2*)(lds + SC_MR);
  const __amdgpu_buffer_rsrc_t ohr = __builtin_amdgcn_make_buffer_rsrc(p.ws + WS_OH, 0, 128 * NKS * 1024, 0x00020000);
  const unsigned ohl = lane * 16;
  if (!TA) {
    const f32x4 c0 = CS[(2 * nj) * 4 + g], c1 = CS[(2 * nj + 1) * 4 + g], c2 = CS[(8 + 2 * nj) * 4 + g], c3 = CS[(9 + 2 * nj) * 4 + g];
    const int tp = tm + 2 * nj;
    LAS char* fx = lds + SC_FX;
    { const f32x2 m0 = MR[64 * mi + 16 * nj + r16], m1 = MR[64 * mi + 16 * ((nj + 1) & 3) + r16];
      *(LAS s16x4*)(fx + ((mi * 2 + (nj & 1)) * 4 + nj) * 512 + lane * 8) = pack4(lnc(accx[0], m0, csx));
      *(LAS s16x4*)(fx + ((mi * 2 + (nj & 1)) * 4 + ((nj + 1) & 3)) * 512 + lane * 8) = pack4(lnc(accx[1], m1, csx)); }
    auto ffn = [&](const f32x4 (&G0)[4], const f32x4 (&U0)[4], const f32x4 (&G1)[4], const f32x4 (&U1)[4]) {
#pragma unroll
      for (int i = 0; i < 4; ++i) {
        const int rt = (i + nj) & 3;
        const f32x2 mr = MR[64 * mi + 16 * rt + r16];
        const u32x2 h0 = __builtin_bit_cast(u32x2, pack4(swiglu_ln(G0[i], U0[i], mr, c0, c2)));
        const u32x2 h1 = __builtin_bit_cast(u32x2, pack4(swiglu_ln(G1[i], U1[i], mr, c1, c3)));
        if (tp <= 85) st16(ohr, ohl + ((rg * 8 + 4 * mi + rt) * NKS + 16 + (tp >> 1)) * 1024, (u32x4){h0[0], h0[1], h1[0], h1[1]});
      }
    };
    if (mi) ffn(acc[2], acc[0], acc[3], acc[1]); else ffn(acc[0], acc[2], acc[1], acc[3]);
    WAITL0(); BAR();
    const f32x4 gx = unpack4(*(LAS s16x4*)(fx + ((mi * 2 + 0) * 4 + nj) * 512 + lane * 8)), ux = unpack4(*(LAS s16x4*)(fx + ((mi * 2 + 1) * 4 + nj) * 512 + lane * 8));
    st8(ohr, ohl + ((rg * 8 + w) * NKS + 16 + (te >> 1)) * 1024 + (te & 1) * 8, pack4(swiglu4(gx, ux)));
  } else {
    s16x4 Q[4], K[4], V[4]; u32x2 Fv[4];
    const f32x4 cq = CS[nj * 4 + g], ck = CS[(4 + nj) * 4 + g], cf = CS[(8 + fu) * 4 + g];
    const float cv = ((LAS float*)CS)[(8 + nj) * 16 + r16];
#pragma unroll
    for (int rt = 0; rt < 4; ++rt) {
      const f32x2 mr = MR[64 * mi + 16 * rt + r16];
      Q[rt] = pack4(lnc(acc[0][rt], mr, cq)); K[rt] = pack4(lnc(acc[1][rt], mr, ck)); Fv[rt] = __builtin_bit_cast(u32x2, pack4(lnc(acc[3][rt], mr, cf)));
      const f32x4 t01 = *(LAS f32x4*)(MR + 64 * mi + 16 * rt + 4 * g), t23 = *(LAS f32x4*)(MR + 64 * mi + 16 * rt + 4 * g + 2);
      const f32x4 mm4 = {t01[0], t01[2], t23[0], t23[2]}, rr4 = {t01[1], t01[3], t23[1], t23[3]};
      V[rt] = pack4((acc[2][rt] - mm4 * cv) * rr4);
    }
    LAS char* kvb = lds + SC_KV + nj * 1536;
    const s16x4 hv = pack4(lnc(acch, MR[128 + r16], CS[((mi ? 8 : 4) + nj) * 4 + g]));
    if (mi == 0) { *(LAS s16x4*)(kvb + 512 + lane * 8) = K[3]; *(LAS s16x4*)(kvb + 1024 + lane * 8) = V[3]; }
    else *(LAS s16x4*)(kvb + r16 * 32 + g * 8) = hv;
#pragma unroll
    for (int rt = 0; rt < 4; ++rt) *(LAS u32x2*)(lds + SC_FX + ((mi * 4 + rt) * 4 + nj) * 512 + lane * 8) = Fv[rt];
    f32x4 mkp, mkc;
#pragma unroll
    for (int r = 0; r < 4; ++r) {
      const int dp = r16 + 16 - (4 * g + r), dc = r16 - (4 * g + r);
      mkp[r] = (!(dp & 1) && dp <= 14) ? 0.f : -__builtin_inff();
      mkc[r] = (!(dc & 1) && dc >= 0 && dc <= 14) ? 0.f : -__builtin_inff();
    }
    const bool odd = r16 & 1;
    const unsigned psh = odd ? 16u : 0u;
    const int RT0 = rg * 8 + 4 * mi;
    const unsigned od = ohl + (RT0 * NKS + 2 * cg + (nj >> 1)) * 1024;
    const s16x4 ones4 = {0x3f80, 0x3f80, 0x3f80, 0x3f80};
    const bf16x8 ones8 = __builtin_bit_cast(bf16x8, __builtin_shufflevector(ones4, ones4, 0, 1, 2, 3, 4, 5, 6, 7));
    auto attn = [&](s16x4 kp, s16x4 vp, s16x4 kc, s16x4 vc, s16x4 qq, f32x4 mp, int rt) {
      const f32x4 z = {0, 0, 0, 0};
      const f32x4 sp = mfma16(kp, qq, mp), sc = mfma16(kc, qq, mkc);
      const float sc2 = 0.25f * 1.44269504089f;
      const float e0 = odd ? sp[1] : sp[0], e1 = odd ? sp[3] : sp[2], e2 = odd ? sc[1] : sc[0], e3 = odd ? sc[3] : sc[2];
      const float mx = xg_max(fmaxf(fmaxf(e0, e1), fmaxf(e2, e3))) * sc2;
      const float p0 = __builtin_amdgcn_exp2f(fmaf(e0, sc2, -mx)), p1 = __builtin_amdgcn_exp2f(fmaf(e1, sc2, -mx));
      const float p2 = __builtin_amdgcn_exp2f(fmaf(e2, sc2, -mx)), p3 = __builtin_amdgcn_exp2f(fmaf(e3, sc2, -mx));
      const u32x2 k01 = __builtin_bit_cast(u32x2, pack4((f32x4){p0, 0.f, p1, 0.f})), k23 = __builtin_bit_cast(u32x2, pack4((f32x4){p2, 0.f, p3, 0.f}));
      const u32x4 pw4 = {k01[0] << psh, k01[1] << psh, k23[0] << psh, k23[1] << psh};
      const bf16x8 pf = __builtin_bit_cast(bf16x8, pw4);
      const bf16x8 vf = __builtin_bit_cast(bf16x8, __builtin_shufflevector(vp, vc, 0, 1, 2, 3, 4, 5, 6, 7));
      const f32x4 o = mfma32(vf, pf, z), sm = mfma32(ones8, pf, z);
      return __builtin_bit_cast(u32x2, pack4(o * __builtin_amdgcn_rcpf(sm[0])));
    };
    u32x2 O[4];
#pragma unroll
    for (int rt = 1; rt < 4; ++rt) O[rt] = attn(K[rt - 1], V[rt - 1], K[rt], V[rt], Q[rt], mkp, rt);
    LAS char* ox = lds + SC_OX + w * 1024;
    *(LAS u32x2*)(ox + lane * 8) = (nj & 1) ? O[1] : O[3];
    if (nj & 1) *(LAS u32x2*)(ox + 512 + lane * 8) = O[2];
    WAITL0(); BAR();
    {
      LAS char* oo = lds + SC_OX + (w ^ 1) * 1024;
      const u32x2 p0 = *(LAS u32x2*)(oo + lane * 8);
      if (nj & 1) st16(ohr, od + 3 * NKS * 1024, (u32x4){p0[0], p0[1], O[3][0], O[3][1]});
      else {
        const u32x2 p1 = *(LAS u32x2*)(oo + 512 + lane * 8);
        st16(ohr, od + 1 * NKS * 1024, (u32x4){O[1][0], O[1][1], p0[0], p0[1]});
        st16(ohr, od + 2 * NKS * 1024, (u32x4){O[2][0], O[2][1], p1[0], p1[1]});
      }
    }
    {
      LAS char* fo = lds + SC_FX + (mi * 4 + nj) * 2048 + lane * 8;
      const s16x4 g0 = *(LAS s16x4*)fo, u0 = *(LAS s16x4*)(fo + 512), g1 = *(LAS s16x4*)(fo + 1024), u1 = *(LAS s16x4*)(fo + 1536);
      const u32x2 h0 = __builtin_bit_cast(u32x2, pack4(swiglu4(unpack4(g0), unpack4(u0)))), h1 = __builtin_bit_cast(u32x2, pack4(swiglu4(unpack4(g1), unpack4(u1))));
      st16(ohr, ohl + ((RT0 + nj) * NKS + 16 + cg) * 1024, (u32x4){h0[0], h0[1], h1[0], h1[1]});
    }
    s16x4 Kp, Vp;
    if (mi == 0) { Kp = hv; Vp = __builtin_amdgcn_ds_read_tr16_b64_v4i16((LAS s16x4*)(kvb + (4 * g + q) * 32 + pp * 8)); }
    else { Kp = *(LAS s16x4*)(kvb + 512 + lane * 8); Vp = *(LAS s16x4*)(kvb + 1024 + lane * 8); }
    const float ninf = -__builtin_inff();
    O[0] = attn(Kp, Vp, K[0], V[0], Q[0], RT0 == 0 ? (f32x4){ninf, ninf, ninf, ninf} : mkp, 0);
    st8(ohr, od + (nj & 1) * 8, __builtin_bit_cast(s16x4, O[0]));
  }
  STAMP(6);
  return tag;
}

constexpr int NR = (NKS + 3) / 4;
#define SC1 " sc1"
DI void gload16(u32x4& dst, const char* sbase, unsigned voff) { asm volatile("global_load_dwordx4 %0, %1, %2" SC1 : "=v"(dst) : "v"(voff), "s"(sbase) : "memory"); }
DI void gload8(u32x2& dst, const char* sbase, unsigned voff) { asm volatile("global_load_dwordx2 %0, %1, %2" SC1 : "=v"(dst) : "v"(voff), "s"(sbase) : "memory"); }
template <int N> DI void waitVn() { asm volatile("s_waitcnt vmcnt(%0)" :: "n"(N) : "memory"); }
constexpr int P2A = P2AV;
constexpr int P2NA = P2A + 1;
constexpr int P2R = 4;
__host__ __device__ constexpr int bslot2(int r) { return r < 8 ? bslot(r) : r < 12 ? bslot(r - 8) : r == 12 ? SC_HS : bslot(r - 9); }
constexpr bool ksplit(int ks) { return false; }
constexpr bool psplit(int ke) { return ksplit(ke) || ksplit(ke + 1); }
constexpr int nA(int r) { return (psplit(4 * phys(r)) ? 2 : 1) + (psplit(4 * phys(r) + 2) ? 2 : 1); }
constexpr int p2issued(int r) { int n = 14; for (int q = 0; q < P2A; ++q) n += nA(q); for (int q = 0; q < r; ++q) n += (q + P2A < NR ? nA(q + P2A) : 0) + (q == 6 ? 4 : 0) + (q == 9 ? 2 : 0); return n; }
constexpr int p2posA(int r) { if (r < P2A) { int n = 14; for (int q = 0; q <= r; ++q) n += nA(q); return n; } return p2issued(r - P2A) + nA(r); }
constexpr int p2posB14() { return p2issued(6) + nA(6 + P2A) + 4; }
template <int R> struct P2W { static constexpr int n = p2issued(R) - ((R == 12 && p2posB14() > p2posA(R)) ? p2posB14() : p2posA(R)); };

DI void phase2(const Params& p, LAS char* lds, int rt64, int ct64, unsigned tag, bool boss) {
  const int tid = threadIdx.x, lane = tid & 63;
  const int w = rfl(tid >> 6), kh = w >> 2, m4 = w & 3;
  const int r16 = lane & 15, g = lane >> 4, q = r16 >> 2, pp = lane & 3;
  const unsigned ldsb = (unsigned)(uintptr_t)lds;
  constexpr int PH = 1; STAMP(0);
  const char* pa = p.ws + WS_OH + ((size_t)(rt64 * 4 + m4) * NKS + kh) * 1024;
  const unsigned va = lane * 16;
  unsigned vb;
  { const int row = 8 * m4 + (lane >> 3), pc = lane & 7; const int c = (pc >> 1) ^ ((row >> 1) & 3);
    vb = row * 1024 + ct64 * 128 + (2 * c + (pc & 1)) * 16; }
  const char* pb2 = p.w2 + (long)(kh - 16) * 32768;
  const char* pbo = p.wo + (long)kh * 32768;
  int bl[4];
#pragma unroll
  for (int c = 0; c < 4; ++c) bl[c] = (4 * g + q) * 128 + ((c ^ ((2 * g + (q >> 1)) & 3)) << 5) + pp * 8 + kh * 4096;
  auto readB = [&](bf16x8 (&dst)[2][4], int r) {
    LAS char* sb = lds + bslot2(r);
#pragma unroll
    for (int j = 0; j < 2; ++j)
#pragma unroll
      for (int c = 0; c < 4; ++c) dst[j][c] = ldsB(sb + j * 8192 + bl[c], 2048);
  };
  auto issueB = [&](int r) {
#pragma unroll
    for (int j = 0; j < 2; ++j) {
      const int ke = 4 * phys(r) + 2 * j;
      const bool over = (ke + 1 > NKS - 1);
      glds16s((ke < 16 ? pbo : pb2) + (over ? (long)ke - kh : (long)ke) * 32768, vb, ldsb + bslot2(r) + (2 * j) * 4096 + kh * 4096 + m4 * 1024);
    }
  };
  bf16x8 bp[P2R][2][4];
#pragma unroll
  for (int r = 0; r < P2R; ++r) readB(bp[r], r);
  WAITL0(); BAR();
#pragma unroll
  for (int r = 6; r <= 12; ++r) issueB(r);
  if (w == 0) {
    gu64* fl = (gu64*)(p.ws + WS_FLAGS) + (rt64 >> 1) * 16 + (lane & 15);
    const unsigned long long want = FMAGIC | tag;
    for (unsigned spins = 0; spins < (1u << 20); ++spins) {
      unsigned long long v;
      asm volatile("global_load_dwordx2 %0, %1, off sc1\n\ts_waitcnt vmcnt(0)" : "=v"(v) : "v"(fl) : "memory");
      if (__all(v == want)) break;
      __builtin_amdgcn_s_sleep(1);
    }
  }
  BAR();
  STAMP(1);
  u32x4 A4[P2NA][2]; u32x2 Al[P2NA][2], Ah[P2NA][2];
  const unsigned vs0 = lane * 8, vs1 = lane * 8 + 512, vn1 = lane * 16 + 8;
  auto issueA = [&](int r) {
#pragma unroll
    for (int j = 0; j < 2; ++j) {
      const int ke = 4 * phys(r) + 2 * j;
      const bool over = (ke + 1 > NKS - 1);
      const char* src = pa + (over ? (long)ke - kh : (long)ke) * 1024;
      if (!psplit(ke)) gload16(A4[r % P2NA][j], src, va);
      else if (ksplit(ke) && ksplit(ke + 1)) { gload8(Al[r % P2NA][j], src, vs0); gload8(Ah[r % P2NA][j], src, vs1); }
      else { const bool sp = ksplit(ke) ? (kh == 0) : (kh == 1); gload8(Al[r % P2NA][j], src, sp ? vs0 : va); gload8(Ah[r % P2NA][j], src, sp ? vs1 : vn1); }
    }
  };
  auto pinA = [&](int r) {
#pragma unroll
    for (int j = 0; j < 2; ++j) {
      if (psplit(4 * phys(r) + 2 * j)) asm volatile("" : "+v"(Al[r % P2NA][j]), "+v"(Ah[r % P2NA][j])); else asm volatile("" : "+v"(A4[r % P2NA][j]));
    }
  };
  auto getA = [&](int r, int j) {
    return psplit(4 * phys(r) + 2 * j) ? __builtin_bit_cast(bf16x8, __builtin_shufflevector(Al[r % P2NA][j], Ah[r % P2NA][j], 0, 1, 2, 3)) : __builtin_bit_cast(bf16x8, A4[r % P2NA][j]);
  };
#pragma unroll
  for (int r = 0; r < P2A; ++r) { issueA(r); if (r + 1 < P2A) BAR(); }
  f32x4 acc[4] = {{0, 0, 0, 0}, {0, 0, 0, 0}, {0, 0, 0, 0}, {0, 0, 0, 0}};
  const size_t ooff = (size_t)(rt64 * 64 + 16 * m4 + r16) * 1024 + (ct64 * 64 + 32 * kh + 4 * g) * 2;
  bf16x4 xv0, xv1;
  u32x4 fl0 = {0, 0, 0, 0}, fl1 = {0, 0, 0, 0};
  bf16x8 bf[2][2][4];
  auto round = [&](auto RC) {
    constexpr int r = decltype(RC)::value;
    waitVn<P2W<r>::n>();
    pinA(r);
    if (r == 5 || r == 6 || r == 12) BAR();
    if (r + P2A < NR) issueA(r + P2A);
    if (r == 6) { issueB(13); issueB(14); }
    if (r == 9) { asm volatile("global_load_dwordx2 %0, %2, %3\n\tglobal_load_dwordx2 %1, %2, %3 offset:32" : "=&v"(xv0), "=&v"(xv1) : "v"((unsigned)ooff), "s"(p.x) : "memory"); }
    if (r == 0) STAMP(2);
    if (r == 8) STAMP(3);
    if (r == 11 && boss) {
      const char* fb = p.ws + WS_FLAGS;
      asm volatile("global_load_dwordx4 %0, %2, %3 sc1\n\tglobal_load_dwordx4 %1, %2, %3 offset:1024 sc1" : "=&v"(fl0), "=&v"(fl1) : "v"(va), "s"(fb) : "memory");
    }
    SB0();
    if (r + 1 < NR && r + 1 >= P2R) readB(bf[(r + 1) & 1], r + 1);
#pragma unroll
    for (int j = 0; j < 2; ++j) {
      if (4 * phys(r) + 2 * j + 1 > NKS - 1 && kh) continue;
#pragma unroll
      for (int c = 0; c < 4; ++c) acc[c] = mfma32(r < P2R ? bp[r < P2R ? r : 0][j][c] : bf[r & 1][j][c], getA(r, j), acc[c]);
    }
#pragma unroll
    for (int k = 0; k < 8; ++k) {
      __builtin_amdgcn_sched_group_barrier(0x8, 1, 0);
      __builtin_amdgcn_sched_group_barrier(0x100, 2, 0);
    }
    SB0();
  };
  static_for<NR>(round);
  STAMP(4);
  {
    LAS f32x4* red = (LAS f32x4*)(lds + SC_RED);
    red[(w * 2 + 0) * 64 + lane] = kh ? acc[0] : acc[2];
    red[(w * 2 + 1) * 64 + lane] = kh ? acc[1] : acc[3];
    WAITL0(); BAR();
    const f32x4 o0 = red[((w ^ 4) * 2 + 0) * 64 + lane], o1 = red[((w ^ 4) * 2 + 1) * 64 + lane];
    const f32x4 s0 = (kh ? acc[2] : acc[0]) + o0, s1 = (kh ? acc[3] : acc[1]) + o1;
    asm volatile("s_waitcnt vmcnt(0)" : "+v"(xv0), "+v"(xv1) :: "memory");
    const f32x4 x0 = __builtin_convertvector(xv0, f32x4), x1 = __builtin_convertvector(xv1, f32x4);
    const u32x2 pa2 = __builtin_bit_cast(u32x2, pack4(s0 + x0)), pb2_ = __builtin_bit_cast(u32x2, pack4(s1 + x1));
    const auto d0 = __builtin_amdgcn_permlane16_swap(pa2[0], pb2_[0], false, false), d1 = __builtin_amdgcn_permlane16_swap(pa2[1], pb2_[1], false, false);
    const __amdgpu_buffer_rsrc_t outr = __builtin_amdgcn_make_buffer_rsrc(p.out, 0, 2048 * 1024, 0x00020000);
    st16(outr, (unsigned)((rt64 * 64 + 16 * m4 + r16) * 1024 + (ct64 * 64 + 32 * kh + (g & 1) * 16 + (g >> 1) * 8) * 2), (u32x4){d0[0], d1[0], d0[1], d1[1]});
  }
  STAMP(5);
  if (boss) {
    asm volatile("s_waitcnt vmcnt(0)" : "+v"(fl0), "+v"(fl1) :: "memory");
    const unsigned hi = (unsigned)(FMAGIC >> 32);
    bool ok = fl0[0] == tag && fl0[1] == hi && fl0[2] == tag && fl0[3] == hi && fl1[0] == tag && fl1[1] == hi && fl1[2] == tag && fl1[3] == hi;
    if (!__all(ok)) {
      gu64* fl = (gu64*)(p.ws + WS_FLAGS) + lane;
      const unsigned long long want = FMAGIC | tag;
      for (unsigned spins = 0; spins < (1u << 20); ++spins) {
        ok = __hip_atomic_load(fl, RLX_AGENT) == want && __hip_atomic_load(fl + 64, RLX_AGENT) == want &&
             __hip_atomic_load(fl + 128, RLX_AGENT) == want && __hip_atomic_load(fl + 192, RLX_AGENT) == want;
        if (__all(ok)) break;
        __builtin_amdgcn_s_sleep(1);
      }
    }
    if (lane == 0) __hip_atomic_store((gu32*)p.ws, tag, RLX_AGENT);
  }
}

__global__ __launch_bounds__(512, 2) void k_fused(Params p) {
  extern __shared__ __attribute__((aligned(16))) char shm[];
  LAS char* lds = (LAS char*)shm;
  const int b = blockIdx.x, xcd = b & 7, idx = b >> 3;
  const int rg = (xcd & 1) * 8 + (idx & 7), cg = (xcd >> 1) * 4 + (idx >> 3);
  const int rt64 = xcd * 4 + (idx & 3), ct64 = idx >> 2;
  const char* code0 = (const char*)(__builtin_amdgcn_s_getpc() & ~(unsigned long long)255);
  const unsigned tag = (cg < 8) ? phase1<true>(p, lds, rg, cg, ct64, idx, code0) : phase1<false>(p, lds, rg, cg - 8, ct64, idx, code0);
  asm volatile("s_waitcnt vmcnt(0)" ::: "memory");
  BAR();
  if (threadIdx.x == 0) __hip_atomic_store((gu64*)(p.ws + WS_FLAGS) + rg * 16 + cg, FMAGIC | tag, RLX_AGENT);
  { constexpr int PH = 0; STAMP(7); }
  phase2(p, lds, rt64, ct64, tag, b == 0 && rfl(threadIdx.x >> 6) == 0);
  { constexpr int PH = 1; STAMP(6); STAMP(7); }
}

extern "C" void kernel_launch(void* const* d_in, const int* in_sizes, int n_in, void* d_out, int out_size, void* d_ws, size_t ws_size,
                              hipStream_t stream) {
  Params p;
  p.x = (const char*)d_in[0]; p.wqkv = (const char*)d_in[1]; p.wo = (const char*)d_in[2];
  p.w1 = (const char*)d_in[4]; p.w3 = (const char*)d_in[5]; p.w2 = (const char*)d_in[6];
  p.ws = (char*)d_ws; p.out = (char*)d_out; p.stamps = (unsigned long long*)((char*)d_ws + (128u << 20));
  (void)hipFuncSetAttribute((const void*)k_fused, hipFuncAttributeMaxDynamicSharedMemorySize, P1_LDS);
  k_fused<<<256, 512, P1_LDS, stream>>>(p);
}
```

```cpp
#include <hip/hip_runtime.h>
#include <stdint.h>

#define LAS __attribute__((address_space(3)))
#define DI __device__ __forceinline__
typedef __bf16 bf16x8 __attribute__((ext_vector_type(8)));
typedef __bf16 bf16x4 __attribute__((ext_vector_type(4)));
typedef short s16x4 __attribute__((ext_vector_type(4)));
typedef float f32x4 __attribute__((ext_vector_type(4)));
typedef float f32x2 __attribute__((ext_vector_type(2)));
typedef unsigned u32x2 __attribute__((ext_vector_type(2)));
typedef unsigned u32x4 __attribute__((ext_vector_type(4)));

constexpr int NKS = 59;
constexpr int SLOT = 53248;
constexpr int WOFF = 16384, EOFF = 49152;
constexpr int DUMMY = 3 * SLOT;
constexpr int SC_CS = DUMMY + 1024;
constexpr int SC_MR = SC_CS + 1152;
constexpr int P1_LDS = 163840;
constexpr int SC_HS = 86016;
constexpr int SC_KV = SLOT;
constexpr int SC_FX = SC_KV + 6144;
constexpr int SC_OX = SC_FX + 16384;
constexpr int SC_RED = SLOT;
__host__ __device__ constexpr int bslot(int r) { return (r & 7) < 3 ? 2 * SLOT + (r & 7) * 16384 : (r & 7) < 6 ? ((r & 7) - 3) * 16384 : SLOT + ((r & 7) - 6) * 16384; }
__host__ __device__ constexpr int phys(int r) { return (r + 6) % 15; }
constexpr unsigned long long FMAGIC = 0x5AC3E17Bull << 32;
constexpr size_t WS_FLAGS = 4096, WS_STATS = 8192, WS_OH = 65536;

#define STAMP(i) do {} while (0)

template <int V> struct IC { static constexpr int value = V; };
template <int N, int I = 0, class F> DI void static_for(F& f) { if constexpr (I < N) { f(IC<I>{}); static_for<N, I + 1>(f); } }
DI int rfl(int v) { return __builtin_amdgcn_readfirstlane(v); }
DI void glds16(const void* g, unsigned lds_dst) {
  unsigned keep;
  asm volatile("s_mov_b32 %0, m0\n\ts_mov_b32 m0, %2\n\ts_nop 0\n\tglobal_load_lds_dwordx4 %1, off\n\ts_mov_b32 m0, %0"
               : "=&s"(keep) : "v"(g), "s"(lds_dst) : "memory");
}
DI void glds16s(const char* sbase, unsigned voff, unsigned lds_dst) {
  asm volatile("s_mov_b32 m0, %2\n\ts_nop 0\n\tglobal_load_lds_dwordx4 %1, %0" :: "s"(sbase), "v"(voff), "s"(lds_dst) : "memory", "m0");
}
#define WAITV(n) asm volatile("s_waitcnt vmcnt(" #n ")" ::: "memory")
#define WAITL0() do { asm volatile("" ::: "memory"); __builtin_amdgcn_s_waitcnt(0xC07F); asm volatile("" ::: "memory"); } while (0)
#define BAR() do { asm volatile("" ::: "memory"); __builtin_amdgcn_s_barrier(); asm volatile("" ::: "memory"); } while (0)
#define SB0() __builtin_amdgcn_sched_barrier(0)

DI bf16x8 ldsA(LAS char* p) { return *(LAS bf16x8*)p; }
DI bf16x8 ldsB(LAS char* p, int toff) {
  s16x4 lo = __builtin_amdgcn_ds_read_tr16_b64_v4i16((LAS s16x4*)p);
  s16x4 hi = __builtin_amdgcn_ds_read_tr16_b64_v4i16((LAS s16x4*)(p + toff));
  return __builtin_bit_cast(bf16x8, __builtin_shufflevector(lo, hi, 0, 1, 2, 3, 4, 5, 6, 7));
}
DI f32x4 mfma32(bf16x8 a, bf16x8 b, f32x4 c) { return __builtin_amdgcn_mfma_f32_16x16x32_bf16(a, b, c, 0, 0, 0); }
DI f32x4 mfma16(s16x4 a, s16x4 b, f32x4 c) { return __builtin_amdgcn_mfma_f32_16x16x16bf16_1k(a, b, c, 0, 0, 0); }
DI s16x4 pack4(f32x4 v) { return __builtin_bit_cast(s16x4, __builtin_convertvector(v, bf16x4)); }
DI f32x4 unpack4(s16x4 v) { return __builtin_convertvector(__builtin_bit_cast(bf16x4, v), f32x4); }
DI float xg_sum(float v) {
  auto r = __builtin_amdgcn_permlane16_swap(__float_as_uint(v), __float_as_uint(v), false, false);
  v = __uint_as_float(r[0]) + __uint_as_float(r[1]);
  auto s = __builtin_amdgcn_permlane32_swap(__float_as_uint(v), __float_as_uint(v), false, false);
  return __uint_as_float(s[0]) + __uint_as_float(s[1]);
}
DI float xg_max(float v) {
  auto r = __builtin_amdgcn_permlane16_swap(__float_as_uint(v), __float_as_uint(v), false, false);
  v = fmaxf(__uint_as_float(r[0]), __uint_as_float(r[1]));
  auto s = __builtin_amdgcn_permlane32_swap(__float_as_uint(v), __float_as_uint(v), false, false);
  return fmaxf(__uint_as_float(s[0]), __uint_as_float(s[1]));
}
DI float row_sum16(float v) {
  v += __builtin_bit_cast(float, __builtin_amdgcn_update_dpp(0, __builtin_bit_cast(int, v), 0x128, 0xf, 0xf, false));
  v += __builtin_bit_cast(float, __builtin_amdgcn_update_dpp(0, __builtin_bit_cast(int, v), 0x124, 0xf, 0xf, false));
  v += __builtin_bit_cast(float, __builtin_amdgcn_update_dpp(0, __builtin_bit_cast(int, v), 0x122, 0xf, 0xf, false));
  v += __builtin_bit_cast(float, __builtin_amdgcn_update_dpp(0, __builtin_bit_cast(int, v), 0x121, 0xf, 0xf, false));
  return v;
}
DI f32x4 lnc(f32x4 a, f32x2 mr, f32x4 c) { return (a - c * mr[0]) * mr[1]; }
DI f32x4 swiglu_ln(f32x4 ag, f32x4 au, f32x2 mr, f32x4 cg, f32x4 cu) {
  const f32x4 g = ag - cg * mr[0], u = au - cu * mr[0];
  const f32x4 t = g * (mr[1] * -1.44269504089f);
  const float ir2 = __builtin_amdgcn_rcpf(mr[1] * mr[1]);
  f32x4 e, d;
#pragma unroll
  for (int r = 0; r < 4; ++r) e[r] = __builtin_amdgcn_exp2f(t[r]);
  e = e * ir2 + ir2;
#pragma unroll
  for (int r = 0; r < 4; ++r) d[r] = __builtin_amdgcn_rcpf(e[r]);
  return g * u * d;
}
DI f32x4 swiglu4(f32x4 g, f32x4 u) {
  const f32x4 t = g * -1.44269504089f;
  f32x4 d;
#pragma unroll
  for (int r = 0; r < 4; ++r) d[r] = __builtin_amdgcn_rcpf(1.0f + __builtin_amdgcn_exp2f(t[r]));
  return g * u * d;
}

struct Params {
  const char* x; const char* wqkv; const char* wo; const char* w1; const char* w3; const char* w2;
  char* ws; char* out; unsigned long long* stamps;
};
typedef __attribute__((address_space(1))) unsigned gu32;
typedef __attribute__((address_space(1))) unsigned long long gu64;
#define RLX_AGENT __ATOMIC_RELAXED, __HIP_MEMORY_SCOPE_AGENT
DI void st16(__amdgpu_buffer_rsrc_t r, unsigned off, u32x4 v) { __builtin_amdgcn_raw_buffer_store_b128(v, r, off, 0, 16); }
DI void st8(__amdgpu_buffer_rsrc_t r, unsigned off, s16x4 v) { __builtin_amdgcn_raw_buffer_store_b64(__builtin_bit_cast(u32x2, v), r, off, 0, 16); }

struct Frags { bf16x8 a[4], ah, b[4], bx, be, e0; };

template <bool TA>
DI unsigned phase1(const Params& p, LAS char* lds, int rg, int cg, int ct64, int idx, const char* code0) {
  const int tid = threadIdx.x, lane = tid & 63;
  const int w = rfl(tid >> 6), mi = w >> 2, nj = w & 3;
  const int r16 = lane & 15, g = lane >> 4, q = r16 >> 2, pp = lane & 3;
  constexpr int PH = 0; STAMP(0);
  const unsigned ldsb = (unsigned)(uintptr_t)lds;
  const int row0 = rg * 128;
  const int srow = row0 + (TA ? cg : 8 + cg) * 8 + w;
  unsigned ev; u32x4 xrow;
  asm volatile("global_load_dword %0, %1, %2 sc1" : "=v"(ev) : "v"(0u), "s"(p.ws) : "memory");
  asm volatile("global_load_dwordx4 %0, %1, %2" : "=v"(xrow) : "v"((unsigned)(srow * 1024 + lane * 16)), "s"(p.x) : "memory");
  const int tm = TA ? 2 * cg : 16 + 18 * (cg >> 1) + ((cg & 1) ? 10 : 0);
  const int te = 16 + 18 * (cg >> 1) + 8 + (cg & 1);
  unsigned vx[2], vw[4], ve;
#pragma unroll
  for (int m = 0; m < 2; ++m) {
    const int row = 16 * w + 8 * m + (lane >> 3);
    vx[m] = (unsigned)((row0 + row) * 1024 + (((lane & 7) ^ ((row >> 1) & 7)) << 4));
  }
  const char* bw[4]; unsigned ldw[4], dw[4];
#pragma unroll
  for (int m = 0; m < 4; ++m) {
    if (!TA || m < 2) {
      const int pi = TA ? (2 * w + m) : (2 * w + (m & 1));
      const int kr = 4 * pi + (lane >> 4), pc = lane & 15;
      const int c = (pc >> 1) ^ ((kr & 3) | (((kr >> 3) & 1) << 2));
      if (TA) { bw[m] = p.wqkv; ldw[m] = 3072; vw[m] = kr * 3072 + ((c >> 2) * 512 + (4 * cg + (c & 3)) * 16) * 2 + (pc & 1) * 16; dw[m] = WOFF + pi * 1024; }
      else { bw[m] = (m >> 1) ? p.w3 : p.w1; ldw[m] = 2752; int t = tm + c; t = t > 85 ? 85 : t; vw[m] = kr * 2752 + t * 32 + (pc & 1) * 16; dw[m] = WOFF + (m >> 1) * 16384 + pi * 1024; }
    } else if (m == 2) {
      const int kr = 8 * w + (lane >> 3), pc = lane & 7;
      const int c = (pc >> 1) ^ (((kr >> 1) & 1) | (((kr >> 3) & 1) << 1));
      bw[m] = p.wqkv; ldw[m] = 3072; vw[m] = kr * 3072 + (1024 + (4 * cg + c) * 16) * 2 + (pc & 1) * 16; dw[m] = WOFF + 16384 + w * 1024;
    } else {
      const int kr = 16 * (w & 3) + (lane >> 2), pc = lane & 3;
      const int e = (pc >> 1) ^ ((kr >> 3) & 1);
      bw[m] = mi ? p.w3 : p.w1; ldw[m] = 2752; vw[m] = kr * 2752 + (tm + e) * 32 + (pc & 1) * 16; dw[m] = WOFF + 24576 + mi * 4096 + (w & 3) * 1024;
    }
  }
  const bool has_e = TA ? (w < 2) : (w < 4);
  const char* be_; unsigned lde, de;
  if (TA) {
    const int row = 8 * (w & 1) + (lane >> 3);
    int gr = row0 - 16 + row; gr = gr < 0 ? 0 : gr;
    be_ = p.x; lde = 2; ve = gr * 1024 + (((lane & 7) ^ ((row >> 1) & 7)) << 4); de = EOFF + w * 1024;
  } else {
    const int pos = 32 * (w & 1) + (lane >> 1), kr = pos ^ (((pos >> 3) & 1) << 2);
    be_ = (w & 2) ? p.w3 : p.w1; lde = 2752; ve = kr * 2752 + te * 32 + (lane & 1) * 16; de = EOFF + (w & 3) * 1024;
  }
  if (!has_e) { be_ = p.x; lde = 0; ve = 0; }
  unsigned vb;
  { const int row = 8 * nj + (lane >> 3), pc = lane & 7; const int c = (pc >> 1) ^ ((row >> 1) & 3);
    vb = row * 1024 + ct64 * 128 + (2 * c + (pc & 1)) * 16; }
  const unsigned emask = has_e ? 0xffffffffu : 0u;
  if (!has_e) de = DUMMY;
  const char* sx = p.x; const char* se = be_; const char* sw[4] = {bw[0], bw[1], bw[2], bw[3]};
  auto dma = [&](int i, int so) {
    if (i == 0) { glds16s(se, ve, ldsb + (so & emask) + de); se += 64u * lde; }
    else if (i <= 2) { glds16s(sx, vx[i - 1], ldsb + so + (2 * w + i - 1) * 1024); if (i == 2) sx += 128; }
    else { glds16s(sw[i - 3], vw[i - 3], ldsb + so + dw[i - 3]); sw[i - 3] += 64u * ldw[i - 3]; }
  };
  auto dmab = [&](int rr, int j) {
    const int ke = 4 * phys(rr) + 2 * j;
    const int ks2 = (ke + 1 > NKS - 1) ? ke : ke + mi;
    const char* src = (ke < 16) ? p.wo + ks2 * 32768 : p.w2 + (ks2 - 16) * 32768;
    glds16s(src, vb, ldsb + bslot(rr) + (2 * j + mi) * 4096 + nj * 1024);
  };
#pragma unroll
  for (int i = 0; i < 7; ++i) dma(i, 0);
#pragma unroll
  for (int i = 0; i < 7; ++i) dma(i, SLOT);
  STAMP(1);
  const char* pfp; unsigned pfs; const char* pfw; const char* pfw2;
  {
    const int l5 = lane & 31;
    const int L = w * 10 + (l5 < 10 ? l5 : 9);
    if (L < 32) { pfp = p.x + (size_t)(row0 + 32 * (idx >> 3) + L) * 1024; pfs = 128; }
    else {
      const int L2 = L - 32, ri = L2 / 6, sg = L2 - 6 * ri;
      const int kr = 8 * (idx & 7) + ri;
      if (TA) { pfp = (sg < 3) ? p.wqkv + (size_t)kr * 3072 + sg * 1024 + cg * 128 : (sg == 3 ? p.w1 : p.w3) + (size_t)kr * 2752 + tm * 32; pfs = (sg < 3) ? 64u * 3072 : 64u * 2752; }
      else { int cb = (tm < te ? tm : te) * 32 + (sg % 3) * 128; cb = cb > 2748 ? 2748 : cb; pfp = (sg < 3 ? p.w1 : p.w3) + (size_t)kr * 2752 + cb; pfs = 64u * 2752; }
    }
    { const int l0 = (idx * 8 + w) * 27 + (lane < 14 ? lane : 13), l1 = (idx * 8 + w) * 27 + 14 + (lane < 13 ? lane : 12);
      auto lineaddr = [&](int l) { return l < 4096 ? p.wo + (size_t)l * 128 : l < 6144 ? p.w2 + (size_t)(l - 4096) * 128 : p.w2 + (size_t)1280 * 1024 + (size_t)(l - 6144) * 128; };
      pfw = lineaddr(l0); pfw2 = lineaddr(l1); }
  }
  auto pf = [&](const char* a) { asm volatile("s_mov_b32 m0, %1\n\ts_nop 0\n\tglobal_load_lds_dword %0, off" :: "v"(a), "s"(ldsb + DUMMY) : "memory", "m0"); };
  {
    const char* a = pfp + (size_t)((lane < 32 ? 2u : 3u) * pfs);
    pf(a);
  }
  const int xa0 = r16 * 128 + ((g ^ (r16 >> 1)) << 4);
  const int fl = q | ((g & 1) << 2);
  const int w256 = (8 * g + q) * 256 + (fl << 5) + pp * 8;
  const int w128 = (8 * g + q) * 128 + ((((q >> 1) & 1) | ((g & 1) << 1)) << 5) + pp * 8;
  const int w64 = (8 * g + q) * 64 + ((g & 1) << 5) + pp * 8;
  const int w32 = (8 * g + ((g & 1) << 2) + q) * 32 + pp * 8;
  const int dt32 = (g & 1) ? -128 : 128;
  const int tb0 = (w256 ^ (nj << 6)) + (mi ? 16384 : 0), tb2 = (w256 ^ (nj << 6)) + (mi ? 0 : 16384);
  const int tbx = EOFF + (nj & 1) * 2048 + w32;
  const int taq = w256 ^ (nj << 5), tak = w256 ^ ((4 + nj) << 5), tav = 16384 + (w128 ^ (nj << 5)), taf = 24576 + (nj & 1) * 4096 + (w64 ^ ((nj >> 1) << 5));
  const int fu = 4 + ((nj >> 1) | ((nj & 1) << 1));
  int xr[4];
#pragma unroll
  for (int i = 0; i < 4; ++i) xr[i] = mi * 8192 + (TA ? i : ((i + nj) & 3)) * 2048;
  const int bea = mi ? tav : tak, bes = mi ? 4096 : 8192, bet = mi ? 512 : 1024;
  const int e0a = mi ? taf : taq, e0s = mi ? 2048 : 8192, e0t = mi ? 256 : 1024;
  constexpr int NRD = TA ? 11 : 9;
  auto ldsB2 = [&](LAS char* p0, LAS char* p1) {
    s16x4 lo = __builtin_amdgcn_ds_read_tr16_b64_v4i16((LAS s16x4*)p0), hi = __builtin_amdgcn_ds_read_tr16_b64_v4i16((LAS s16x4*)p1);
    return __builtin_bit_cast(bf16x8, __builtin_shufflevector(lo, hi, 0, 1, 2, 3, 4, 5, 6, 7));
  };
  auto rdf = [&](Frags& F, int i, int so, int ks) {
    LAS char* sb = lds + so;
    const int xa = xa0 ^ (ks * 64);
    LAS char* wb = sb + WOFF;
    if (TA) {
      switch (i) {
        case 0: F.a[0] = ldsA(sb + xa + xr[0]); break;
        case 1: F.b[0] = ldsB(wb + taq + ks * 8192, 1024); break;
        case 2: F.b[1] = ldsB(wb + tak + ks * 8192, 1024); break;
        case 3: F.b[2] = ldsB(wb + tav + ks * 4096, 512); break;
        case 4: F.b[3] = ldsB(wb + taf + ks * 2048, 256); break;
        case 5: F.a[1] = ldsA(sb + xa + xr[1]); break;
        case 6: F.a[2] = ldsA(sb + xa + xr[2]); break;
        case 7: F.a[3] = ldsA(sb + xa + xr[3]); break;
        case 8: F.be = ldsB2(wb + bea + ks * bes, wb + bea + ks * bes + bet); break;
        case 9: F.ah = ldsA(sb + EOFF + xa); break;
        default: F.e0 = ldsB2(wb + e0a + ks * e0s, wb + e0a + ks * e0s + e0t); break;
      }
    } else {
      switch (i) {
        case 0: F.a[0] = ldsA(sb + xa + xr[0]); break;
        case 1: F.b[0] = ldsB(wb + tb0 + ks * 8192, 1024); break;
        case 2: F.b[1] = ldsB(wb + (tb0 ^ 32) + ks * 8192, 1024); break;
        case 3: F.b[2] = ldsB(wb + tb2 + ks * 8192, 1024); break;
        case 4: F.b[3] = ldsB(wb + (tb2 ^ 32) + ks * 8192, 1024); break;
        case 5: F.a[1] = ldsA(sb + xa + xr[1]); break;
        case 6: F.a[2] = ldsA(sb + xa + xr[2]); break;
        case 7: F.a[3] = ldsA(sb + xa + xr[3]); break;
        default: F.bx = ldsB2(sb + tbx + ks * 1024, sb + tbx + ks * 1024 + dt32); break;
      }
    }
  };
  f32x4 acc[4][4], accx[2], acch = {0, 0, 0, 0}, cs0 = {0, 0, 0, 0}, cs1 = {0, 0, 0, 0}, csx = {0, 0, 0, 0};
  accx[0] = (f32x4){0, 0, 0, 0}; accx[1] = (f32x4){0, 0, 0, 0};
#pragma unroll
  for (int c = 0; c < 4; ++c)
#pragma unroll
    for (int rt = 0; rt < 4; ++rt) acc[c][rt] = (f32x4){0, 0, 0, 0};
  const bf16x8 ones = {(__bf16)1.0f, (__bf16)1.0f, (__bf16)1.0f, (__bf16)1.0f, (__bf16)1.0f, (__bf16)1.0f, (__bf16)1.0f, (__bf16)1.0f};
  auto mm = [&](const Frags& F, int gi) {
    if (gi < 4) {
      acc[0][gi] = mfma32(F.b[0], F.a[gi], acc[0][gi]);
      acc[1][gi] = mfma32(F.b[1], F.a[gi], acc[1][gi]);
      acc[2][gi] = TA ? mfma32(F.a[gi], F.b[2], acc[2][gi]) : mfma32(F.b[2], F.a[gi], acc[2][gi]);
      acc[3][gi] = mfma32(F.b[3], F.a[gi], acc[3][gi]);
    } else if (TA) {
      acch = mfma32(F.be, F.ah, acch);
      cs0 = mfma32(F.e0, ones, cs0); cs1 = mfma32(F.be, ones, cs1);
    } else {
      accx[0] = mfma32(F.bx, F.a[0], accx[0]); accx[1] = mfma32(F.bx, F.a[1], accx[1]);
      cs0 = mfma32(F.b[0], ones, cs0); cs1 = mfma32(F.b[1], ones, cs1); csx = mfma32(F.bx, ones, csx);
    }
  };
  constexpr int RPG = (NRD + 4) / 5;
  auto half = [&](const Frags& Fc, Frags& Fn, int so_n, int ks_n, int mode, int d0, int nd, int so_d, int bt) {
#pragma unroll
    for (int gi = 0; gi < 5; ++gi) {
#define PRIO 3
      if (gi == 0) { if (ks_n == 0) __builtin_amdgcn_s_setprio(3); else __builtin_amdgcn_s_setprio(1); }
      if (gi == PRIO) { if (ks_n == 0) __builtin_amdgcn_s_setprio(2); else __builtin_amdgcn_s_setprio(0); }
#pragma unroll
      for (int i = 0; i < RPG; ++i) { const int idx = gi * RPG + i; if (idx < NRD) rdf(Fn, idx, so_n, ks_n); }
      mm(Fc, gi);
#pragma unroll
      for (int k = 0; k < 5; ++k) {
        __builtin_amdgcn_sched_group_barrier(0x8, 1, 0);
        __builtin_amdgcn_sched_group_barrier(0x100, 1, 0);
        __builtin_amdgcn_sched_group_barrier(0x2, 2, 0);
      }
      __builtin_amdgcn_sched_group_barrier(0x100, 8, 0);
      SB0();
      if (mode == 0 && gi < nd) { dma(d0 + gi, so_d); SB0(); }
      if (mode == 1 && gi < nd && d0 + gi < 6) { dmab(3 * bt + ((d0 + gi) >> 1), (d0 + gi) & 1); SB0(); }
    }
  };
  Frags F0, F1;
  WAITV(8); BAR();
  STAMP(2);
#pragma unroll
  for (int i = 0; i < NRD; ++i) rdf(F0, i, 0, 0);
#pragma unroll
  for (int i = 0; i < 3; ++i) dma(i, 2 * SLOT);
  half(F0, F1, 0, 1, 0, 3, 4, 2 * SLOT, 0);
  pf(pfp + (size_t)(4u * pfs));
  asm volatile("" : "+v"(ev), "+v"(xrow));
  const unsigned tag = (unsigned)rfl((int)ev) + 1u;
  {
    float s1 = 0.f, s2 = 0.f;
#pragma unroll
    for (int i = 0; i < 4; ++i) {
      const float lo = __uint_as_float(xrow[i] << 16), hi = __uint_as_float(xrow[i] & 0xffff0000u);
      s1 += lo + hi; s2 = fmaf(lo, lo, fmaf(hi, hi, s2));
    }
    s1 = xg_sum(row_sum16(s1)); s2 = xg_sum(row_sum16(s2));
    const float m = s1 * (1.0f / 512.0f), var = fmaxf(s2 * (1.0f / 512.0f) - m * m, 0.f);
    const float rs = __builtin_amdgcn_rsqf(var + 1e-5f);
    const __amdgpu_buffer_rsrc_t str = __builtin_amdgcn_make_buffer_rsrc(p.ws + WS_STATS, 0, 2048 * 16, 0x00020000);
    if (lane == 0) st16(str, srow * 16, (u32x4){__float_as_uint(m), tag, __float_as_uint(rs), tag});
  }
  int sop = 0, so = SLOT;
#pragma unroll 1
  for (int t = 1; t < 6; ++t) {
    WAITV(9); WAITL0(); BAR();
    half(F1, F0, so, 0, 0, 0, 4, sop, 0);
    half(F0, F1, so, 1, 0, 4, 3, sop, 0);
    pf(t <= 3 ? pfp + (size_t)((unsigned)(t + 4) * pfs) : (t == 4 ? pfw : pfw2));
    sop = so; so = (so == 2 * SLOT) ? 0 : so + SLOT;
  }
  WAITV(9); WAITL0(); BAR();
  u32x4 stg;
  { int r = (w < 2) ? row0 + 64 * w + lane : (w == 2 ? row0 - 16 + (lane & 15) : row0 + lane); r = r < 0 ? 0 : r;
    asm volatile("global_load_dwordx4 %0, %1, %2 sc1" : "=v"(stg) : "v"((unsigned)(r * 16)), "s"(p.ws + WS_STATS) : "memory"); }
  half(F1, F0, 0, 0, 1, 0, 4, 0, 0);
  half(F0, F1, 0, 1, 1, 4, 3, 0, 0);
  WAITV(8); WAITL0(); BAR();
  half(F1, F0, SLOT, 0, 1, 0, 4, 0, 1);
  half(F0, F1, SLOT, 1, 1, 4, 3, 0, 1);
#pragma unroll
  for (int gi = 0; gi < 5; ++gi) mm(F1, gi);
  STAMP(4);
  {
    LAS f32x4* CSw = (LAS f32x4*)(lds + SC_CS);
    if (r16 == 0) {
      const int u0 = TA ? (mi ? 8 + fu : nj) : (mi * 8 + 2 * nj), u1 = TA ? (mi ? 8 + nj : 4 + nj) : (mi * 8 + 2 * nj + 1);
      CSw[u0 * 4 + g] = cs0; CSw[u1 * 4 + g] = cs1;
    }
    asm volatile("s_waitcnt vmcnt(12)" : "+v"(stg) :: "memory");
    if (w < 3) {
      int r = (w < 2) ? row0 + 64 * w + lane : row0 - 16 + (lane & 15); r = r < 0 ? 0 : r;
      if (!__all(stg[1] == tag && stg[3] == tag)) {
        gu64* gp = (gu64*)(p.ws + WS_STATS) + r * 2;
        for (unsigned spins = 0; spins < (1u << 20); ++spins) {
          const unsigned long long a = __hip_atomic_load(gp, RLX_AGENT), b = __hip_atomic_load(gp + 1, RLX_AGENT);
          stg = (u32x4){(unsigned)a, (unsigned)(a >> 32), (unsigned)b, (unsigned)(b >> 32)};
          if (__all(stg[1] == tag && stg[3] == tag)) break;
          __builtin_amdgcn_s_sleep(1);
        }
      }
      if (w < 2 || lane < 16) ((LAS f32x2*)(lds + SC_MR))[64 * w + lane] = (f32x2){__uint_as_float(stg[0]), __uint_as_float(stg[2])};
    }
  }
  WAITL0(); BAR();
  STAMP(5);
  LAS f32x4* CS = (LAS f32x4*)(lds + SC_CS);
  LAS f32x2* MR = (LAS f32x2*)(lds + SC_MR);
  const __amdgpu_buffer_rsrc_t ohr = __builtin_amdgcn_make_buffer_rsrc(p.ws + WS_OH, 0, 128 * NKS * 1024, 0x00020000);
  const unsigned ohl = lane * 16;
  if (!TA) {
    const f32x4 c0 = CS[(2 * nj) * 4 + g], c1 = CS[(2 * nj + 1) * 4 + g], c2 = CS[(8 + 2 * nj) * 4 + g], c3 = CS[(9 + 2 * nj) * 4 + g];
    const int tp = tm + 2 * nj;
    LAS char* fx = lds + SC_FX;
    { const f32x2 m0 = MR[64 * mi + 16 * nj + r16], m1 = MR[64 * mi + 16 * ((nj + 1) & 3) + r16];
      *(LAS s16x4*)(fx + ((mi * 2 + (nj & 1)) * 4 + nj) * 512 + lane * 8) = pack4(lnc(accx[0], m0, csx));
      *(LAS s16x4*)(fx + ((mi * 2 + (nj & 1)) * 4 + ((nj + 1) & 3)) * 512 + lane * 8) = pack4(lnc(accx[1], m1, csx)); }
    auto ffn = [&](const f32x4 (&G0)[4], const f32x4 (&U0)[4], const f32x4 (&G1)[4], const f32x4 (&U1)[4]) {
#pragma unroll
      for (int i = 0; i < 4; ++i) {
        const int rt = (i + nj) & 3;
        const f32x2 mr = MR[64 * mi + 16 * rt + r16];
        const u32x2 h0 = __builtin_bit_cast(u32x2, pack4(swiglu_ln(G0[i], U0[i], mr, c0, c2)));
        const u32x2 h1 = __builtin_bit_cast(u32x2, pack4(swiglu_ln(G1[i], U1[i], mr, c1, c3)));
        if (tp <= 85) st16(ohr, ohl + ((rg * 8 + 4 * mi + rt) * NKS + 16 + (tp >> 1)) * 1024, (u32x4){h0[0], h0[1], h1[0], h1[1]});
      }
    };
    if (mi) ffn(acc[2], acc[0], acc[3], acc[1]); else ffn(acc[0], acc[2], acc[1], acc[3]);
    WAITL0(); BAR();
    const f32x4 gx = unpack4(*(LAS s16x4*)(fx + ((mi * 2 + 0) * 4 + nj) * 512 + lane * 8)), ux = unpack4(*(LAS s16x4*)(fx + ((mi * 2 + 1) * 4 + nj) * 512 + lane * 8));
    st8(ohr, ohl + ((rg * 8 + w) * NKS + 16 + (te >> 1)) * 1024 + (te & 1) * 8, pack4(swiglu4(gx, ux)));
  } else {
    s16x4 Q[4], K[4], V[4]; u32x2 Fv[4];
    const f32x4 cq = CS[nj * 4 + g], ck = CS[(4 + nj) * 4 + g], cf = CS[(8 + fu) * 4 + g];
    const float cv = ((LAS float*)CS)[(8 + nj) * 16 + r16];
#pragma unroll
    for (int rt = 0; rt < 4; ++rt) {
      const f32x2 mr = MR[64 * mi + 16 * rt + r16];
      Q[rt] = pack4(lnc(acc[0][rt], mr, cq)); K[rt] = pack4(lnc(acc[1][rt], mr, ck)); Fv[rt] = __builtin_bit_cast(u32x2, pack4(lnc(acc[3][rt], mr, cf)));
      const f32x4 t01 = *(LAS f32x4*)(MR + 64 * mi + 16 * rt + 4 * g), t23 = *(LAS f32x4*)(MR + 64 * mi + 16 * rt + 4 * g + 2);
      const f32x4 mm4 = {t01[0], t01[2], t23[0], t23[2]}, rr4 = {t01[1], t01[3], t23[1], t23[3]};
      V[rt] = pack4((acc[2][rt] - mm4 * cv) * rr4);
    }
    LAS char* kvb = lds + SC_KV + nj * 1536;
    const s16x4 hv = pack4(lnc(acch, MR[128 + r16], CS[((mi ? 8 : 4) + nj) * 4 + g]));
    if (mi == 0) { *(LAS s16x4*)(kvb + 512 + lane * 8) = K[3]; *(LAS s16x4*)(kvb + 1024 + lane * 8) = V[3]; }
    else *(LAS s16x4*)(kvb + r16 * 32 + g * 8) = hv;
#pragma unroll
    for (int rt = 0; rt < 4; ++rt) *(LAS u32x2*)(lds + SC_FX + ((mi * 4 + rt) * 4 + nj) * 512 + lane * 8) = Fv[rt];
    f32x4 mkp, mkc;
#pragma unroll
    for (int r = 0; r < 4; ++r) {
      const int dp = r16 + 16 - (4 * g + r), dc = r16 - (4 * g + r);
      mkp[r] = (!(dp & 1) && dp <= 14) ? 0.f : -__builtin_inff();
      mkc[r] = (!(dc & 1) && dc >= 0 && dc <= 14) ? 0.f : -__builtin_inff();
    }
    const bool odd = r16 & 1;
    const unsigned psh = odd ? 16u : 0u;
    const int RT0 = rg * 8 + 4 * mi;
    const unsigned od = ohl + (RT0 * NKS + 2 * cg + (nj >> 1)) * 1024;
    const s16x4 ones4 = {0x3f80, 0x3f80, 0x3f80, 0x3f80};
    const bf16x8 ones8 = __builtin_bit_cast(bf16x8, __builtin_shufflevector(ones4, ones4, 0, 1, 2, 3, 4, 5, 6, 7));
    auto attn = [&](s16x4 kp, s16x4 vp, s16x4 kc, s16x4 vc, s16x4 qq, f32x4 mp, int rt) {
      const f32x4 z = {0, 0, 0, 0};
      const f32x4 sp = mfma16(kp, qq, mp), sc = mfma16(kc, qq, mkc);
      const float sc2 = 0.25f * 1.44269504089f;
      const float e0 = odd ? sp[1] : sp[0], e1 = odd ? sp[3] : sp[2], e2 = odd ? sc[1] : sc[0], e3 = odd ? sc[3] : sc[2];
      const float mx = xg_max(fmaxf(fmaxf(e0, e1), fmaxf(e2, e3))) * sc2;
      const float p0 = __builtin_amdgcn_exp2f(fmaf(e0, sc2, -mx)), p1 = __builtin_amdgcn_exp2f(fmaf(e1, sc2, -mx));
      const float p2 = __builtin_amdgcn_exp2f(fmaf(e2, sc2, -mx)), p3 = __builtin_amdgcn_exp2f(fmaf(e3, sc2, -mx));
      const u32x2 k01 = __builtin_bit_cast(u32x2, pack4((f32x4){p0, 0.f, p1, 0.f})), k23 = __builtin_bit_cast(u32x2, pack4((f32x4){p2, 0.f, p3, 0.f}));
      const u32x4 pw4 = {k01[0] << psh, k01[1] << psh, k23[0] << psh, k23[1] << psh};
      const bf16x8 pf = __builtin_bit_cast(bf16x8, pw4);
      const bf16x8 vf = __builtin_bit_cast(bf16x8, __builtin_shufflevector(vp, vc, 0, 1, 2, 3, 4, 5, 6, 7));
      const f32x4 o = mfma32(vf, pf, z), sm = mfma32(ones8, pf, z);
      return __builtin_bit_cast(u32x2, pack4(o * __builtin_amdgcn_rcpf(sm[0])));
    };
    u32x2 O[4];
#pragma unroll
    for (int rt = 1; rt < 4; ++rt) O[rt] = attn(K[rt - 1], V[rt - 1], K[rt], V[rt], Q[rt], mkp, rt);
    LAS char* ox = lds + SC_OX + w * 1024;
    *(LAS u32x2*)(ox + lane * 8) = (nj & 1) ? O[1] : O[3];
    if (nj & 1) *(LAS u32x2*)(ox + 512 + lane * 8) = O[2];
    WAITL0(); BAR();
    {
      LAS char* oo = lds + SC_OX + (w ^ 1) * 1024;
      const u32x2 p0 = *(LAS u32x2*)(oo + lane * 8);
      if (nj & 1) st16(ohr, od + 3 * NKS * 1024, (u32x4){p0[0], p0[1], O[3][0], O[3][1]});
      else {
        const u32x2 p1 = *(LAS u32x2*)(oo + 512 + lane * 8);
        st16(ohr, od + 1 * NKS * 1024, (u32x4){O[1][0], O[1][1], p0[0], p0[1]});
        st16(ohr, od + 2 * NKS * 1024, (u32x4){O[2][0], O[2][1], p1[0], p1[1]});
      }
    }
    {
      LAS char* fo = lds + SC_FX + (mi * 4 + nj) * 2048 + lane * 8;
      const s16x4 g0 = *(LAS s16x4*)fo, u0 = *(LAS s16x4*)(fo + 512), g1 = *(LAS s16x4*)(fo + 1024), u1 = *(LAS s16x4*)(fo + 1536);
      const u32x2 h0 = __builtin_bit_cast(u32x2, pack4(swiglu4(unpack4(g0), unpack4(u0)))), h1 = __builtin_bit_cast(u32x2, pack4(swiglu4(unpack4(g1), unpack4(u1))));
      st16(ohr, ohl + ((RT0 + nj) * NKS + 16 + cg) * 1024, (u32x4){h0[0], h0[1], h1[0], h1[1]});
    }
    s16x4 Kp, Vp;
    if (mi == 0) { Kp = hv; Vp = __builtin_amdgcn_ds_read_tr16_b64_v4i16((LAS s16x4*)(kvb + (4 * g + q) * 32 + pp * 8)); }
    else { Kp = *(LAS s16x4*)(kvb + 512 + lane * 8); Vp = *(LAS s16x4*)(kvb + 1024 + lane * 8); }
    const float ninf = -__builtin_inff();
    O[0] = attn(Kp, Vp, K[0], V[0], Q[0], RT0 == 0 ? (f32x4){ninf, ninf, ninf, ninf} : mkp, 0);
    st8(ohr, od + (nj & 1) * 8, __builtin_bit_cast(s16x4, O[0]));
  }
  STAMP(6);
  return tag;
}

constexpr int NR = (NKS + 3) / 4;
#define SC1 " sc1"
DI void gload16(u32x4& dst, const char* sbase, unsigned voff) { asm volatile("global_load_dwordx4 %0, %1, %2" SC1 : "=v"(dst) : "v"(voff), "s"(sbase) : "memory"); }
DI void gload8(u32x2& dst, const char* sbase, unsigned voff) { asm volatile("global_load_dwordx2 %0, %1, %2" SC1 : "=v"(dst) : "v"(voff), "s"(sbase) : "memory"); }
template <int N> DI void waitVn() { asm volatile("s_waitcnt vmcnt(%0)" :: "n"(N) : "memory"); }
#define P2AV 6
constexpr int P2A = P2AV;
constexpr int P2NA = P2A + 1;
constexpr int P2R = 4;
__host__ __device__ constexpr int bslot2(int r) { return r < 8 ? bslot(r) : r < 12 ? bslot(r - 8) : r == 12 ? SC_HS : bslot(r - 9); }
constexpr bool ksplit(int ks) { return false; }
constexpr bool psplit(int ke) { return ksplit(ke) || ksplit(ke + 1); }
constexpr int nA(int r) { return (psplit(4 * phys(r)) ? 2 : 1) + (psplit(4 * phys(r) + 2) ? 2 : 1); }
constexpr int p2issued(int r) { int n = 14; for (int q = 0; q < P2A; ++q) n += nA(q); for (int q = 0; q < r; ++q) n += (q + P2A < NR ? nA(q + P2A) : 0) + (q == 6 ? 4 : 0) + (q == 9 ? 2 : 0); return n; }
constexpr int p2posA(int r) { if (r < P2A) { int n = 14; for (int q = 0; q <= r; ++q) n += nA(q); return n; } return p2issued(r - P2A) + nA(r); }
constexpr int p2posB14() { return p2issued(6) + nA(6 + P2A) + 4; }
template <int R> struct P2W { static constexpr int n = p2issued(R) - ((R == 12 && p2posB14() > p2posA(R)) ? p2posB14() : p2posA(R)); };

DI void phase2(const Params& p, LAS char* lds, int rt64, int ct64, unsigned tag, bool boss) {
  const int tid = threadIdx.x, lane = tid & 63;
  const int w = rfl(tid >> 6), kh = w >> 2, m4 = w & 3;
  const int r16 = lane & 15, g = lane >> 4, q = r16 >> 2, pp = lane & 3;
  const unsigned ldsb = (unsigned)(uintptr_t)lds;
  constexpr int PH = 1; STAMP(0);
  const char* pa = p.ws + WS_OH + ((size_t)(rt64 * 4 + m4) * NKS + kh) * 1024;
  const unsigned va = lane * 16;
  unsigned vb;
  { const int row = 8 * m4 + (lane >> 3), pc = lane & 7; const int c = (pc >> 1) ^ ((row >> 1) & 3);
    vb = row * 1024 + ct64 * 128 + (2 * c + (pc & 1)) * 16; }
  const char* pb2 = p.w2 + (long)(kh - 16) * 32768;
  const char* pbo = p.wo + (long)kh * 32768;
  int bl[4];
#pragma unroll
  for (int c = 0; c < 4; ++c) bl[c] = (4 * g + q) * 128 + ((c ^ ((2 * g + (q >> 1)) & 3)) << 5) + pp * 8 + kh * 4096;
  auto readB = [&](bf16x8 (&dst)[2][4], int r) {
    LAS char* sb = lds + bslot2(r);
#pragma unroll
    for (int j = 0; j < 2; ++j)
#pragma unroll
      for (int c = 0; c < 4; ++c) dst[j][c] = ldsB(sb + j * 8192 + bl[c], 2048);
  };
  auto issueB = [&](int r) {
#pragma unroll
    for (int j = 0; j < 2; ++j) {
      const int ke = 4 * phys(r) + 2 * j;
      const bool over = (ke + 1 > NKS - 1);
      glds16s((ke < 16 ? pbo : pb2) + (over ? (long)ke - kh : (long)ke) * 32768, vb, ldsb + bslot2(r) + (2 * j) * 4096 + kh * 4096 + m4 * 1024);
    }
  };
  bf16x8 bp[P2R][2][4];
#pragma unroll
  for (int r = 0; r < P2R; ++r) readB(bp[r], r);
  WAITL0(); BAR();
#pragma unroll
  for (int r = 6; r <= 12; ++r) issueB(r);
  if (w == 0) {
    gu64* fl = (gu64*)(p.ws + WS_FLAGS) + (rt64 >> 1) * 16 + (lane & 15);
    const unsigned long long want = FMAGIC | tag;
    for (unsigned spins = 0; spins < (1u << 20); ++spins) {
      unsigned long long v;
      asm volatile("global_load_dwordx2 %0, %1, off sc1\n\ts_waitcnt vmcnt(0)" : "=v"(v) : "v"(fl) : "memory");
      if (__all(v == want)) break;
      __builtin_amdgcn_s_sleep(1);
    }
  }
  BAR();
  STAMP(1);
  u32x4 A4[P2NA][2]; u32x2 Al[P2NA][2], Ah[P2NA][2];
  const unsigned vs0 = lane * 8, vs1 = lane * 8 + 512, vn1 = lane * 16 + 8;
  auto issueA = [&](int r) {
#pragma unroll
    for (int j = 0; j < 2; ++j) {
      const int ke = 4 * phys(r) + 2 * j;
      const bool over = (ke + 1 > NKS - 1);
      const char* src = pa + (over ? (long)ke - kh : (long)ke) * 1024;
      if (!psplit(ke)) gload16(A4[r % P2NA][j], src, va);
      else if (ksplit(ke) && ksplit(ke + 1)) { gload8(Al[r % P2NA][j], src, vs0); gload8(Ah[r % P2NA][j], src, vs1); }
      else { const bool sp = ksplit(ke) ? (kh == 0) : (kh == 1); gload8(Al[r % P2NA][j], src, sp ? vs0 : va); gload8(Ah[r % P2NA][j], src, sp ? vs1 : vn1); }
    }
  };
  auto pinA = [&](int r) {
#pragma unroll
    for (int j = 0; j < 2; ++j) {
      if (psplit(4 * phys(r) + 2 * j)) asm volatile("" : "+v"(Al[r % P2NA][j]), "+v"(Ah[r % P2NA][j])); else asm volatile("" : "+v"(A4[r % P2NA][j]));
    }
  };
  auto getA = [&](int r, int j) {
    return psplit(4 * phys(r) + 2 * j) ? __builtin_bit_cast(bf16x8, __builtin_shufflevector(Al[r % P2NA][j], Ah[r % P2NA][j], 0, 1, 2, 3)) : __builtin_bit_cast(bf16x8, A4[r % P2NA][j]);
  };
#pragma unroll
  for (int r = 0; r < P2A; ++r) { issueA(r); if (r + 1 < P2A) BAR(); }
  f32x4 acc[4] = {{0, 0, 0, 0}, {0, 0, 0, 0}, {0, 0, 0, 0}, {0, 0, 0, 0}};
  const size_t ooff = (size_t)(rt64 * 64 + 16 * m4 + r16) * 1024 + (ct64 * 64 + 32 * kh + 4 * g) * 2;
  bf16x4 xv0, xv1;
  u32x4 fl0 = {0, 0, 0, 0}, fl1 = {0, 0, 0, 0};
  bf16x8 bf[2][2][4];
  auto round = [&](auto RC) {
    constexpr int r = decltype(RC)::value;
    waitVn<P2W<r>::n>();
    pinA(r);
    if (r == 5 || r == 6 || r == 12) BAR();
    if (r + P2A < NR) issueA(r + P2A);
    if (r == 6) { issueB(13); issueB(14); }
    if (r == 9) { asm volatile("global_load_dwordx2 %0, %2, %3\n\tglobal_load_dwordx2 %1, %2, %3 offset:32" : "=&v"(xv0), "=&v"(xv1) : "v"((unsigned)ooff), "s"(p.x) : "memory"); }
    if (r == 0) STAMP(2);
    if (r == 8) STAMP(3);
    if (r == 11 && boss) {
      const char* fb = p.ws + WS_FLAGS;
      asm volatile("global_load_dwordx4 %0, %2, %3 sc1\n\tglobal_load_dwordx4 %1, %2, %3 offset:1024 sc1" : "=&v"(fl0), "=&v"(fl1) : "v"(va), "s"(fb) : "memory");
    }
    SB0();
    if (r + 1 < NR && r + 1 >= P2R) readB(bf[(r + 1) & 1], r + 1);
#pragma unroll
    for (int j = 0; j < 2; ++j) {
      if (4 * phys(r) + 2 * j + 1 > NKS - 1 && kh) continue;
#pragma unroll
      for (int c = 0; c < 4; ++c) acc[c] = mfma32(r < P2R ? bp[r < P2R ? r : 0][j][c] : bf[r & 1][j][c], getA(r, j), acc[c]);
    }
#pragma unroll
    for (int k = 0; k < 8; ++k) {
      __builtin_amdgcn_sched_group_barrier(0x8, 1, 0);
      __builtin_amdgcn_sched_group_barrier(0x100, 2, 0);
    }
    SB0();
  };
  static_for<NR>(round);
  STAMP(4);
  {
    LAS f32x4* red = (LAS f32x4*)(lds + SC_RED);
    red[(w * 2 + 0) * 64 + lane] = kh ? acc[0] : acc[2];
    red[(w * 2 + 1) * 64 + lane] = kh ? acc[1] : acc[3];
    WAITL0(); BAR();
    const f32x4 o0 = red[((w ^ 4) * 2 + 0) * 64 + lane], o1 = red[((w ^ 4) * 2 + 1) * 64 + lane];
    const f32x4 s0 = (kh ? acc[2] : acc[0]) + o0, s1 = (kh ? acc[3] : acc[1]) + o1;
    asm volatile("s_waitcnt vmcnt(0)" : "+v"(xv0), "+v"(xv1) :: "memory");
    const f32x4 x0 = __builtin_convertvector(xv0, f32x4), x1 = __builtin_convertvector(xv1, f32x4);
    const u32x2 pa2 = __builtin_bit_cast(u32x2, pack4(s0 + x0)), pb2_ = __builtin_bit_cast(u32x2, pack4(s1 + x1));
    const auto d0 = __builtin_amdgcn_permlane16_swap(pa2[0], pb2_[0], false, false), d1 = __builtin_amdgcn_permlane16_swap(pa2[1], pb2_[1], false, false);
    const __amdgpu_buffer_rsrc_t outr = __builtin_amdgcn_make_buffer_rsrc(p.out, 0, 2048 * 1024, 0x00020000);
    st16(outr, (unsigned)((rt64 * 64 + 16 * m4 + r16) * 1024 + (ct64 * 64 + 32 * kh + (g & 1) * 16 + (g >> 1) * 8) * 2), (u32x4){d0[0], d1[0], d0[1], d1[1]});
  }
  STAMP(5);
  if (boss) {
    asm volatile("s_waitcnt vmcnt(0)" : "+v"(fl0), "+v"(fl1) :: "memory");
    const unsigned hi = (unsigned)(FMAGIC >> 32);
    bool ok = fl0[0] == tag && fl0[1] == hi && fl0[2] == tag && fl0[3] == hi && fl1[0] == tag && fl1[1] == hi && fl1[2] == tag && fl1[3] == hi;
    if (!__all(ok)) {
      gu64* fl = (gu64*)(p.ws + WS_FLAGS) + lane;
      const unsigned long long want = FMAGIC | tag;
      for (unsigned spins = 0; spins < (1u << 20); ++spins) {
        ok = __hip_atomic_load(fl, RLX_AGENT) == want && __hip_atomic_load(fl + 64, RLX_AGENT) == want &&
             __hip_atomic_load(fl + 128, RLX_AGENT) == want && __hip_atomic_load(fl + 192, RLX_AGENT) == want;
        if (__all(ok)) break;
        __builtin_amdgcn_s_sleep(1);
      }
    }
    if (lane == 0) __hip_atomic_store((gu32*)p.ws, tag, RLX_AGENT);
  }
}

__global__ __launch_bounds__(512, 2) void k_fused(Params p) {
  extern __shared__ __attribute__((aligned(16))) char shm[];
  LAS char* lds = (LAS char*)shm;
  const int b = blockIdx.x, xcd = b & 7, idx = b >> 3;
  const int rg = (xcd & 1) * 8 + (idx & 7), cg = (xcd >> 1) * 4 + (idx >> 3);
  const int rt64 = xcd * 4 + (idx & 3), ct64 = idx >> 2;
  {
    const char* code0 = (const char*)(__builtin_amdgcn_s_getpc() & ~(unsigned long long)255);
    const unsigned off = ((blockIdx.x >> 3) * 8 + (threadIdx.x >> 6)) * 128;
    asm volatile("s_mov_b32 m0, %2\n\ts_nop 0\n\tglobal_load_lds_dword %0, %1" :: "v"(off), "s"(code0), "s"((unsigned)(uintptr_t)lds + DUMMY) : "memory", "m0");
  }
  const char* code0 = nullptr;
  const unsigned tag = (cg < 8) ? phase1<true>(p, lds, rg, cg, ct64, idx, code0) : phase1<false>(p, lds, rg, cg - 8, ct64, idx, code0);
  asm volatile("s_waitcnt vmcnt(0)" ::: "memory");
  BAR();
  if (threadIdx.x == 0) __hip_atomic_store((gu64*)(p.ws + WS_FLAGS) + rg * 16 + cg, FMAGIC | tag, RLX_AGENT);
  { constexpr int PH = 0; STAMP(7); }
  phase2(p, lds, rt64, ct64, tag, b == 0 && rfl(threadIdx.x >> 6) == 0);
  { constexpr int PH = 1; STAMP(6); STAMP(7); }
}

extern "C" void kernel_launch(void* const* d_in, const int* in_sizes, int n_in, void* d_out, int out_size, void* d_ws, size_t ws_size,
                              hipStream_t stream) {
  Params p;
  p.x = (const char*)d_in[0]; p.wqkv = (const char*)d_in[1]; p.wo = (const char*)d_in[2];
  p.w1 = (const char*)d_in[4]; p.w3 = (const char*)d_in[5]; p.w2 = (const char*)d_in[6];
  p.ws = (char*)d_ws; p.out = (char*)d_out; p.stamps = (unsigned long long*)((char*)d_ws + (128u << 20));
  (void)hipFuncSetAttribute((const void*)k_fused, hipFuncAttributeMaxDynamicSharedMemorySize, P1_LDS);
  k_fused<<<256, 512, P1_LDS, stream>>>(p);
}
```

```cpp
#include <hip/hip_runtime.h>
#include <stdint.h>

#define LAS __attribute__((address_space(3)))
#define DI __device__ __forceinline__
typedef __bf16 bf16x8 __attribute__((ext_vector_type(8)));
typedef __bf16 bf16x4 __attribute__((ext_vector_type(4)));
typedef short s16x4 __attribute__((ext_vector_type(4)));
typedef float f32x4 __attribute__((ext_vector_type(4)));
typedef float f32x2 __attribute__((ext_vector_type(2)));
typedef unsigned u32x2 __attribute__((ext_vector_type(2)));
typedef unsigned u32x4 __attribute__((ext_vector_type(4)));

constexpr int NKS = 59;
constexpr int SLOT = 53248;
constexpr int WOFF = 16384, EOFF = 49152;
constexpr int DUMMY = 3 * SLOT;
constexpr int SC_CS = DUMMY + 1024;
constexpr int SC_MR = SC_CS + 1152;
constexpr int P1_LDS = 163840;
constexpr int SC_HS = 86016;
constexpr int SC_KV = SLOT;
constexpr int SC_FX = SC_KV + 6144;
constexpr int SC_OX = SC_FX + 16384;
constexpr int SC_RED = SLOT;
__host__ __device__ constexpr int bslot(int r) { return (r & 7) < 3 ? 2 * SLOT + (r & 7) * 16384 : (r & 7) < 6 ? ((r & 7) - 3) * 16384 : SLOT + ((r & 7) - 6) * 16384; }
__host__ __device__ constexpr int phys(int r) { return (r + 6) % 15; }
constexpr unsigned long long FMAGIC = 0x5AC3E17Bull << 32;
constexpr size_t WS_FLAGS = 4096, WS_STATS = 8192, WS_OH = 65536;
constexpr unsigned CODE_LINES = 248;

template <int V> struct IC { static constexpr int value = V; };
template <int N, int I = 0, class F> DI void static_for(F& f) { if constexpr (I < N) { f(IC<I>{}); static_for<N, I + 1>(f); } }
DI int rfl(int v) { return __builtin_amdgcn_readfirstlane(v); }
DI void glds16s(const char* sbase, unsigned voff, unsigned lds_dst) {
  asm volatile("s_mov_b32 m0, %2\n\ts_nop 0\n\tglobal_load_lds_dwordx4 %1, %0" :: "s"(sbase), "v"(voff), "s"(lds_dst) : "memory", "m0");
}
#define WAITV(n) asm volatile("s_waitcnt vmcnt(" #n ")" ::: "memory")
#define WAITL0() do { asm volatile("" ::: "memory"); __builtin_amdgcn_s_waitcnt(0xC07F); asm volatile("" ::: "memory"); } while (0)
#define BAR() do { asm volatile("" ::: "memory"); __builtin_amdgcn_s_barrier(); asm volatile("" ::: "memory"); } while (0)
#define SB0() __builtin_amdgcn_sched_barrier(0)

DI bf16x8 ldsA(LAS char* p) { return *(LAS bf16x8*)p; }
DI bf16x8 ldsB(LAS char* p, int toff) {
  s16x4 lo = __builtin_amdgcn_ds_read_tr16_b64_v4i16((LAS s16x4*)p);
  s16x4 hi = __builtin_amdgcn_ds_read_tr16_b64_v4i16((LAS s16x4*)(p + toff));
  return __builtin_bit_cast(bf16x8, __builtin_shufflevector(lo, hi, 0, 1, 2, 3, 4, 5, 6, 7));
}
DI f32x4 mfma32(bf16x8 a, bf16x8 b, f32x4 c) { return __builtin_amdgcn_mfma_f32_16x16x32_bf16(a, b, c, 0, 0, 0); }
DI f32x4 mfma16(s16x4 a, s16x4 b, f32x4 c) { return __builtin_amdgcn_mfma_f32_16x16x16bf16_1k(a, b, c, 0, 0, 0); }
DI s16x4 pack4(f32x4 v) { return __builtin_bit_cast(s16x4, __builtin_convertvector(v, bf16x4)); }
DI f32x4 unpack4(s16x4 v) { return __builtin_convertvector(__builtin_bit_cast(bf16x4, v), f32x4); }
DI float xg_sum(float v) {
  auto r = __builtin_amdgcn_permlane16_swap(__float_as_uint(v), __float_as_uint(v), false, false);
  v = __uint_as_float(r[0]) + __uint_as_float(r[1]);
  auto s = __builtin_amdgcn_permlane32_swap(__float_as_uint(v), __float_as_uint(v), false, false);
  return __uint_as_float(s[0]) + __uint_as_float(s[1]);
}
DI float xg_max(float v) {
  auto r = __builtin_amdgcn_permlane16_swap(__float_as_uint(v), __float_as_uint(v), false, false);
  v = fmaxf(__uint_as_float(r[0]), __uint_as_float(r[1]));
  auto s = __builtin_amdgcn_permlane32_swap(__float_as_uint(v), __float_as_uint(v), false, false);
  return fmaxf(__uint_as_float(s[0]), __uint_as_float(s[1]));
}
DI float row_sum16(float v) {
  v += __builtin_bit_cast(float, __builtin_amdgcn_update_dpp(0, __builtin_bit_cast(int, v), 0x128, 0xf, 0xf, false));
  v += __builtin_bit_cast(float, __builtin_amdgcn_update_dpp(0, __builtin_bit_cast(int, v), 0x124, 0xf, 0xf, false));
  v += __builtin_bit_cast(float, __builtin_amdgcn_update_dpp(0, __builtin_bit_cast(int, v), 0x122, 0xf, 0xf, false));
  v += __builtin_bit_cast(float, __builtin_amdgcn_update_dpp(0, __builtin_bit_cast(int, v), 0x121, 0xf, 0xf, false));
  return v;
}
DI f32x4 lnc(f32x4 a, f32x2 mr, f32x4 c) { return (a - c * mr[0]) * mr[1]; }
DI f32x4 swiglu_ln(f32x4 ag, f32x4 au, f32x2 mr, f32x4 cg, f32x4 cu) {
  const f32x4 g = ag - cg * mr[0], u = au - cu * mr[0];
  const f32x4 t = g * (mr[1] * -1.44269504089f);
  const float ir2 = __builtin_amdgcn_rcpf(mr[1] * mr[1]);
  f32x4 e, d;
#pragma unroll
  for (int r = 0; r < 4; ++r) e[r] = __builtin_amdgcn_exp2f(t[r]);
  e = e * ir2 + ir2;
#pragma unroll
  for (int r = 0; r < 4; ++r) d[r] = __builtin_amdgcn_rcpf(e[r]);
  return g * u * d;
}
DI f32x4 swiglu4(f32x4 g, f32x4 u) {
  const f32x4 t = g * -1.44269504089f;
  f32x4 d;
#pragma unroll
  for (int r = 0; r < 4; ++r) d[r] = __builtin_amdgcn_rcpf(1.0f + __builtin_amdgcn_exp2f(t[r]));
  return g * u * d;
}

struct Params {
  const char* x; const char* wqkv; const char* wo; const char* w1; const char* w3; const char* w2;
  char* ws; char* out;
};
typedef __attribute__((address_space(1))) unsigned gu32;
typedef __attribute__((address_space(1))) unsigned long long gu64;
#define RLX_AGENT __ATOMIC_RELAXED, __HIP_MEMORY_SCOPE_AGENT
DI void st16(__amdgpu_buffer_rsrc_t r, unsigned off, u32x4 v) { __builtin_amdgcn_raw_buffer_store_b128(v, r, off, 0, 16); }
DI void st8(__amdgpu_buffer_rsrc_t r, unsigned off, s16x4 v) { __builtin_amdgcn_raw_buffer_store_b64(__builtin_bit_cast(u32x2, v), r, off, 0, 16); }

struct Frags { bf16x8 a[4], ah, b[4], bx, be, e0; };

template <bool TA>
DI unsigned phase1(const Params& p, LAS char* lds, int rg, int cg, int ct64, int idx) {
  const int tid = threadIdx.x, lane = tid & 63;
  const int w = rfl(tid >> 6), mi = w >> 2, nj = w & 3;
  const int r16 = lane & 15, g = lane >> 4, q = r16 >> 2, pp = lane & 3;
  const unsigned ldsb = (unsigned)(uintptr_t)lds;
  const int row0 = rg * 128;
  const int srow = row0 + (TA ? cg : 8 + cg) * 8 + w;
  unsigned ev; u32x4 xrow;
  asm volatile("global_load_dword %0, %1, %2 sc1" : "=v"(ev) : "v"(0u), "s"(p.ws) : "memory");
  asm volatile("global_load_dwordx4 %0, %1, %2" : "=v"(xrow) : "v"((unsigned)(srow * 1024 + lane * 16)), "s"(p.x) : "memory");
  const int tm = TA ? 2 * cg : 16 + 18 * (cg >> 1) + ((cg & 1) ? 10 : 0);
  const int te = 16 + 18 * (cg >> 1) + 8 + (cg & 1);
  unsigned vx[2], vw[4], ve;
#pragma unroll
  for (int m = 0; m < 2; ++m) {
    const int row = 16 * w + 8 * m + (lane >> 3);
    vx[m] = (unsigned)((row0 + row) * 1024 + (((lane & 7) ^ ((row >> 1) & 7)) << 4));
  }
  const char* bw[4]; unsigned ldw[4], dw[4];
#pragma unroll
  for (int m = 0; m < 4; ++m) {
    if (!TA || m < 2) {
      const int pi = TA ? (2 * w + m) : (2 * w + (m & 1));
      const int kr = 4 * pi + (lane >> 4), pc = lane & 15;
      const int c = (pc >> 1) ^ ((kr & 3) | (((kr >> 3) & 1) << 2));
      if (TA) { bw[m] = p.wqkv; ldw[m] = 3072; vw[m] = kr * 3072 + ((c >> 2) * 512 + (4 * cg + (c & 3)) * 16) * 2 + (pc & 1) * 16; dw[m] = WOFF + pi * 1024; }
      else { bw[m] = (m >> 1) ? p.w3 : p.w1; ldw[m] = 2752; int t = tm + c; t = t > 85 ? 85 : t; vw[m] = kr * 2752 + t * 32 + (pc & 1) * 16; dw[m] = WOFF + (m >> 1) * 16384 + pi * 1024; }
    } else if (m == 2) {
      const int kr = 8 * w + (lane >> 3), pc = lane & 7;
      const int c = (pc >> 1) ^ (((kr >> 1) & 1) | (((kr >> 3) & 1) << 1));
      bw[m] = p.wqkv; ldw[m] = 3072; vw[m] = kr * 3072 + (1024 + (4 * cg + c) * 16) * 2 + (pc & 1) * 16; dw[m] = WOFF + 16384 + w * 1024;
    } else {
      const int kr = 16 * (w & 3) + (lane >> 2), pc = lane & 3;
      const int e = (pc >> 1) ^ ((kr >> 3) & 1);
      bw[m] = mi ? p.w3 : p.w1; ldw[m] = 2752; vw[m] = kr * 2752 + (tm + e) * 32 + (pc & 1) * 16; dw[m] = WOFF + 24576 + mi * 4096 + (w & 3) * 1024;
    }
  }
  const bool has_e = TA ? (w < 2) : (w < 4);
  const char* be_; unsigned lde, de;
  if (TA) {
    const int row = 8 * (w & 1) + (lane >> 3);
    int gr = row0 - 16 + row; gr = gr < 0 ? 0 : gr;
    be_ = p.x; lde = 2; ve = gr * 1024 + (((lane & 7) ^ ((row >> 1) & 7)) << 4); de = EOFF + w * 1024;
  } else {
    const int pos = 32 * (w & 1) + (lane >> 1), kr = pos ^ (((pos >> 3) & 1) << 2);
    be_ = (w & 2) ? p.w3 : p.w1; lde = 2752; ve = kr * 2752 + te * 32 + (lane & 1) * 16; de = EOFF + (w & 3) * 1024;
  }
  if (!has_e) { be_ = p.x; lde = 0; ve = 0; }
  unsigned vb;
  { const int row = 8 * nj + (lane >> 3), pc = lane & 7; const int c = (pc >> 1) ^ ((row >> 1) & 3);
    vb = row * 1024 + ct64 * 128 + (2 * c + (pc & 1)) * 16; }
  const unsigned emask = has_e ? 0xffffffffu : 0u;
  if (!has_e) de = DUMMY;
  const char* sx = p.x; const char* se = be_; const char* sw[4] = {bw[0], bw[1], bw[2], bw[3]};
  auto dma = [&](int i, int so) {
    if (i == 0) { glds16s(se, ve, ldsb + (so & emask) + de); se += 64u * lde; }
    else if (i <= 2) { glds16s(sx, vx[i - 1], ldsb + so + (2 * w + i - 1) * 1024); if (i == 2) sx += 128; }
    else { glds16s(sw[i - 3], vw[i - 3], ldsb + so + dw[i - 3]); sw[i - 3] += 64u * ldw[i - 3]; }
  };
  auto dmab = [&](int rr, int j) {
    const int ke = 4 * phys(rr) + 2 * j;
    const int ks2 = (ke + 1 > NKS - 1) ? ke : ke + mi;
    const char* src = (ke < 16) ? p.wo + ks2 * 32768 : p.w2 + (ks2 - 16) * 32768;
    glds16s(src, vb, ldsb + bslot(rr) + (2 * j + mi) * 4096 + nj * 1024);
  };
#pragma unroll
  for (int i = 0; i < 7; ++i) dma(i, 0);
#pragma unroll
  for (int i = 0; i < 7; ++i) dma(i, SLOT);
  const char* pfp; unsigned pfs; const char* pfw; const char* pfw2;
  {
    const int l5 = lane & 31;
    const int L = w * 10 + (l5 < 10 ? l5 : 9);
    if (L < 32) { pfp = p.x + (size_t)(row0 + 32 * (idx >> 3) + L) * 1024; pfs = 128; }
    else {
      const int L2 = L - 32, ri = L2 / 6, sg = L2 - 6 * ri;
      const int kr = 8 * (idx & 7) + ri;
      if (TA) { pfp = (sg < 3) ? p.wqkv + (size_t)kr * 3072 + sg * 1024 + cg * 128 : (sg == 3 ? p.w1 : p.w3) + (size_t)kr * 2752 + tm * 32; pfs = (sg < 3) ? 64u * 3072 : 64u * 2752; }
      else { int cb = (tm < te ? tm : te) * 32 + (sg % 3) * 128; cb = cb > 2748 ? 2748 : cb; pfp = (sg < 3 ? p.w1 : p.w3) + (size_t)kr * 2752 + cb; pfs = 64u * 2752; }
    }
    { const int l0 = (idx * 8 + w) * 27 + (lane < 14 ? lane : 13), l1 = (idx * 8 + w) * 27 + 14 + (lane < 13 ? lane : 12);
      auto lineaddr = [&](int l) { return l < 4096 ? p.wo + (size_t)l * 128 : l < 6144 ? p.w2 + (size_t)(l - 4096) * 128 : p.w2 + (size_t)1280 * 1024 + (size_t)(l - 6144) * 128; };
      pfw = lineaddr(l0); pfw2 = lineaddr(l1); }
  }
  auto pf = [&](const char* a) { asm volatile("s_mov_b32 m0, %1\n\ts_nop 0\n\tglobal_load_lds_dword %0, off" :: "v"(a), "s"(ldsb + DUMMY) : "memory", "m0"); };
  pf(pfp + (size_t)((lane < 32 ? 2u : 3u) * pfs));
  const int xa0 = r16 * 128 + ((g ^ (r16 >> 1)) << 4);
  const int fl = q | ((g & 1) << 2);
  const int w256 = (8 * g + q) * 256 + (fl << 5) + pp * 8;
  const int w128 = (8 * g + q) * 128 + ((((q >> 1) & 1) | ((g & 1) << 1)) << 5) + pp * 8;
  const int w64 = (8 * g + q) * 64 + ((g & 1) << 5) + pp * 8;
  const int w32 = (8 * g + ((g & 1) << 2) + q) * 32 + pp * 8;
  const int dt32 = (g & 1) ? -128 : 128;
  const int tb0 = (w256 ^ (nj << 6)) + (mi ? 16384 : 0), tb2 = (w256 ^ (nj << 6)) + (mi ? 0 : 16384);
  const int tbx = EOFF + (nj & 1) * 2048 + w32;
  const int taq = w256 ^ (nj << 5), tak = w256 ^ ((4 + nj) << 5), tav = 16384 + (w128 ^ (nj << 5)), taf = 24576 + (nj & 1) * 4096 + (w64 ^ ((nj >> 1) << 5));
  const int fu = 4 + ((nj >> 1) | ((nj & 1) << 1));
  int xr[4];
#pragma unroll
  for (int i = 0; i < 4; ++i) xr[i] = mi * 8192 + (TA ? i : ((i + nj) & 3)) * 2048;
  const int bea = mi ? tav : tak, bes = mi ? 4096 : 8192, bet = mi ? 512 : 1024;
  const int e0a = mi ? taf : taq, e0s = mi ? 2048 : 8192, e0t = mi ? 256 : 1024;
  constexpr int NRD = TA ? 11 : 9;
  auto ldsB2 = [&](LAS char* p0, LAS char* p1) {
    s16x4 lo = __builtin_amdgcn_ds_read_tr16_b64_v4i16((LAS s16x4*)p0), hi = __builtin_amdgcn_ds_read_tr16_b64_v4i16((LAS s16x4*)p1);
    return __builtin_bit_cast(bf16x8, __builtin_shufflevector(lo, hi, 0, 1, 2, 3, 4, 5, 6, 7));
  };
  auto rdf = [&](Frags& F, int i, int so, int ks) {
    LAS char* sb = lds + so;
    const int xa = xa0 ^ (ks * 64);
    LAS char* wb = sb + WOFF;
    if (TA) {
      switch (i) {
        case 0: F.a[0] = ldsA(sb + xa + xr[0]); break;
        case 1: F.b[0] = ldsB(wb + taq + ks * 8192, 1024); break;
        case 2: F.b[1] = ldsB(wb + tak + ks * 8192, 1024); break;
        case 3: F.b[2] = ldsB(wb + tav + ks * 4096, 512); break;
        case 4: F.b[3] = ldsB(wb + taf + ks * 2048, 256); break;
        case 5: F.a[1] = ldsA(sb + xa + xr[1]); break;
        case 6: F.a[2] = ldsA(sb + xa + xr[2]); break;
        case 7: F.a[3] = ldsA(sb + xa + xr[3]); break;
        case 8: F.be = ldsB2(wb + bea + ks * bes, wb + bea + ks * bes + bet); break;
        case 9: F.ah = ldsA(sb + EOFF + xa); break;
        default: F.e0 = ldsB2(wb + e0a + ks * e0s, wb + e0a + ks * e0s + e0t); break;
      }
    } else {
      switch (i) {
        case 0: F.a[0] = ldsA(sb + xa + xr[0]); break;
        case 1: F.b[0] = ldsB(wb + tb0 + ks * 8192, 1024); break;
        case 2: F.b[1] = ldsB(wb + (tb0 ^ 32) + ks * 8192, 1024); break;
        case 3: F.b[2] = ldsB(wb + tb2 + ks * 8192, 1024); break;
        case 4: F.b[3] = ldsB(wb + (tb2 ^ 32) + ks * 8192, 1024); break;
        case 5: F.a[1] = ldsA(sb + xa + xr[1]); break;
        case 6: F.a[2] = ldsA(sb + xa + xr[2]); break;
        case 7: F.a[3] = ldsA(sb + xa + xr[3]); break;
        default: F.bx = ldsB2(sb + tbx + ks * 1024, sb + tbx + ks * 1024 + dt32); break;
      }
    }
  };
  f32x4 acc[4][4], accx[2], acch = {0, 0, 0, 0}, cs0 = {0, 0, 0, 0}, cs1 = {0, 0, 0, 0}, csx = {0, 0, 0, 0};
  accx[0] = (f32x4){0, 0, 0, 0}; accx[1] = (f32x4){0, 0, 0, 0};
#pragma unroll
  for (int c = 0; c < 4; ++c)
#pragma unroll
    for (int rt = 0; rt < 4; ++rt) acc[c][rt] = (f32x4){0, 0, 0, 0};
  const bf16x8 ones = {(__bf16)1.0f, (__bf16)1.0f, (__bf16)1.0f, (__bf16)1.0f, (__bf16)1.0f, (__bf16)1.0f, (__bf16)1.0f, (__bf16)1.0f};
  auto mm = [&](const Frags& F, int gi) {
    if (gi < 4) {
      acc[0][gi] = mfma32(F.b[0], F.a[gi], acc[0][gi]);
      acc[1][gi] = mfma32(F.b[1], F.a[gi], acc[1][gi]);
      acc[2][gi] = TA ? mfma32(F.a[gi], F.b[2], acc[2][gi]) : mfma32(F.b[2], F.a[gi], acc[2][gi]);
      acc[3][gi] = mfma32(F.b[3], F.a[gi], acc[3][gi]);
    } else if (TA) {
      acch = mfma32(F.be, F.ah, acch);
      cs0 = mfma32(F.e0, ones, cs0); cs1 = mfma32(F.be, ones, cs1);
    } else {
      accx[0] = mfma32(F.bx, F.a[0], accx[0]); accx[1] = mfma32(F.bx, F.a[1], accx[1]);
      cs0 = mfma32(F.b[0], ones, cs0); cs1 = mfma32(F.b[1], ones, cs1); csx = mfma32(F.bx, ones, csx);
    }
  };
  constexpr int RPG = (NRD + 4) / 5;
  auto half = [&](const Frags& Fc, Frags& Fn, int so_n, int ks_n, int mode, int d0, int nd, int so_d, int bt) {
#pragma unroll
    for (int gi = 0; gi < 5; ++gi) {
      if (gi == 0) { if (ks_n == 0) __builtin_amdgcn_s_setprio(3); else __builtin_amdgcn_s_setprio(1); }
      if (gi == 3) { if (ks_n == 0) __builtin_amdgcn_s_setprio(2); else __builtin_amdgcn_s_setprio(0); }
#pragma unroll
      for (int i = 0; i < RPG; ++i) { const int idx = gi * RPG + i; if (idx < NRD) rdf(Fn, idx, so_n, ks_n); }
      mm(Fc, gi);
#pragma unroll
      for (int k = 0; k < 5; ++k) {
        __builtin_amdgcn_sched_group_barrier(0x8, 1, 0);
        __builtin_amdgcn_sched_group_barrier(0x100, 1, 0);
        __builtin_amdgcn_sched_group_barrier(0x2, 2, 0);
      }
      __builtin_amdgcn_sched_group_barrier(0x100, 8, 0);
      SB0();
      if (mode == 0 && gi < nd) { dma(d0 + gi, so_d); SB0(); }
      if (mode == 1 && gi < nd && d0 + gi < 6) { dmab(3 * bt + ((d0 + gi) >> 1), (d0 + gi) & 1); SB0(); }
    }
  };
  Frags F0, F1;
  WAITV(8); BAR();
#pragma unroll
  for (int i = 0; i < NRD; ++i) rdf(F0, i, 0, 0);
#pragma unroll
  for (int i = 0; i < 3; ++i) dma(i, 2 * SLOT);
  half(F0, F1, 0, 1, 0, 3, 4, 2 * SLOT, 0);
  pf(pfp + (size_t)(4u * pfs));
  asm volatile("" : "+v"(ev), "+v"(xrow));
  const unsigned tag = (unsigned)rfl((int)ev) + 1u;
  {
    const float piv = __uint_as_float((unsigned)rfl((int)xrow[0]) << 16);
    float s1 = 0.f, s2 = 0.f;
#pragma unroll
    for (int i = 0; i < 4; ++i) {
      const float lo = __uint_as_float(xrow[i] << 16) - piv, hi = __uint_as_float(xrow[i] & 0xffff0000u) - piv;
      s1 += lo + hi; s2 = fmaf(lo, lo, fmaf(hi, hi, s2));
    }
    const float dm = xg_sum(row_sum16(s1)) * (1.0f / 512.0f);
    const float var = fmaxf(xg_sum(row_sum16(s2)) * (1.0f / 512.0f) - dm * dm, 0.f), mu = piv + dm;
    auto rb = [](float v) { return (float)(__bf16)v; };
    const float m = rb(mu);
    const float rs = __builtin_amdgcn_rcpf(rb(__builtin_amdgcn_sqrtf(rb(rb(var) + 1e-5f))));
    const __amdgpu_buffer_rsrc_t str = __builtin_amdgcn_make_buffer_rsrc(p.ws + WS_STATS, 0, 2048 * 16, 0x00020000);
    if (lane == 0) st16(str, srow * 16, (u32x4){__float_as_uint(m), tag, __float_as_uint(rs), tag});
  }
  int sop = 0, so = SLOT;
#pragma unroll 1
  for (int t = 1; t < 6; ++t) {
    WAITV(9); WAITL0(); BAR();
    half(F1, F0, so, 0, 0, 0, 4, sop, 0);
    half(F0, F1, so, 1, 0, 4, 3, sop, 0);
    pf(t <= 3 ? pfp + (size_t)((unsigned)(t + 4) * pfs) : (t == 4 ? pfw : pfw2));
    sop = so; so = (so == 2 * SLOT) ? 0 : so + SLOT;
  }
  WAITV(9); WAITL0(); BAR();
  u32x4 stg;
  { int r = (w < 2) ? row0 + 64 * w + lane : (w == 2 ? row0 - 16 + (lane & 15) : row0 + lane); r = r < 0 ? 0 : r;
    asm volatile("global_load_dwordx4 %0, %1, %2 sc1" : "=v"(stg) : "v"((unsigned)(r * 16)), "s"(p.ws + WS_STATS) : "memory"); }
  half(F1, F0, 0, 0, 1, 0, 4, 0, 0);
  half(F0, F1, 0, 1, 1, 4, 3, 0, 0);
  WAITV(8); WAITL0(); BAR();
  half(F1, F0, SLOT, 0, 1, 0, 4, 0, 1);
  half(F0, F1, SLOT, 1, 1, 4, 3, 0, 1);
#pragma unroll
  for (int gi = 0; gi < 5; ++gi) mm(F1, gi);
  {
    LAS f32x4* CSw = (LAS f32x4*)(lds + SC_CS);
    if (r16 == 0) {
      const int u0 = TA ? (mi ? 8 + fu : nj) : (mi * 8 + 2 * nj), u1 = TA ? (mi ? 8 + nj : 4 + nj) : (mi * 8 + 2 * nj + 1);
      CSw[u0 * 4 + g] = cs0; CSw[u1 * 4 + g] = cs1;
    }
    asm volatile("s_waitcnt vmcnt(12)" : "+v"(stg) :: "memory");
    if (w < 3) {
      int r = (w < 2) ? row0 + 64 * w + lane : row0 - 16 + (lane & 15); r = r < 0 ? 0 : r;
      if (!__all(stg[1] == tag && stg[3] == tag)) {
        gu64* gp = (gu64*)(p.ws + WS_STATS) + r * 2;
        for (unsigned spins = 0; spins < (1u << 20); ++spins) {
          const unsigned long long a = __hip_atomic_load(gp, RLX_AGENT), b = __hip_atomic_load(gp + 1, RLX_AGENT);
          stg = (u32x4){(unsigned)a, (unsigned)(a >> 32), (unsigned)b, (unsigned)(b >> 32)};
          if (__all(stg[1] == tag && stg[3] == tag)) break;
          __builtin_amdgcn_s_sleep(1);
        }
      }
      if (w < 2 || lane < 16) ((LAS f32x2*)(lds + SC_MR))[64 * w + lane] = (f32x2){__uint_as_float(stg[0]), __uint_as_float(stg[2])};
    }
  }
  WAITL0(); BAR();
  LAS f32x4* CS = (LAS f32x4*)(lds + SC_CS);
  LAS f32x2* MR = (LAS f32x2*)(lds + SC_MR);
  const __amdgpu_buffer_rsrc_t ohr = __builtin_amdgcn_make_buffer_rsrc(p.ws + WS_OH, 0, 128 * NKS * 1024, 0x00020000);
  const unsigned ohl = lane * 16;
  if (!TA) {
    const f32x4 c0 = CS[(2 * nj) * 4 + g], c1 = CS[(2 * nj + 1) * 4 + g], c2 = CS[(8 + 2 * nj) * 4 + g], c3 = CS[(9 + 2 * nj) * 4 + g];
    const int tp = tm + 2 * nj;
    LAS char* fx = lds + SC_FX;
    { const f32x2 m0 = MR[64 * mi + 16 * nj + r16], m1 = MR[64 * mi + 16 * ((nj + 1) & 3) + r16];
      *(LAS s16x4*)(fx + ((mi * 2 + (nj & 1)) * 4 + nj) * 512 + lane * 8) = pack4(lnc(accx[0], m0, csx));
      *(LAS s16x4*)(fx + ((mi * 2 + (nj & 1)) * 4 + ((nj + 1) & 3)) * 512 + lane * 8) = pack4(lnc(accx[1], m1, csx)); }
    auto ffn = [&](const f32x4 (&G0)[4], const f32x4 (&U0)[4], const f32x4 (&G1)[4], const f32x4 (&U1)[4]) {
#pragma unroll
      for (int i = 0; i < 4; ++i) {
        const int rt = (i + nj) & 3;
        const f32x2 mr = MR[64 * mi + 16 * rt + r16];
        const u32x2 h0 = __builtin_bit_cast(u32x2, pack4(swiglu_ln(G0[i], U0[i], mr, c0, c2)));
        const u32x2 h1 = __builtin_bit_cast(u32x2, pack4(swiglu_ln(G1[i], U1[i], mr, c1, c3)));
        if (tp <= 85) st16(ohr, ohl + ((rg * 8 + 4 * mi + rt) * NKS + 16 + (tp >> 1)) * 1024, (u32x4){h0[0], h0[1], h1[0], h1[1]});
      }
    };
    if (mi) ffn(acc[2], acc[0], acc[3], acc[1]); else ffn(acc[0], acc[2], acc[1], acc[3]);
    WAITL0(); BAR();
    const f32x4 gx = unpack4(*(LAS s16x4*)(fx + ((mi * 2 + 0) * 4 + nj) * 512 + lane * 8)), ux = unpack4(*(LAS s16x4*)(fx + ((mi * 2 + 1) * 4 + nj) * 512 + lane * 8));
    st8(ohr, ohl + ((rg * 8 + w) * NKS + 16 + (te >> 1)) * 1024 + (te & 1) * 8, pack4(swiglu4(gx, ux)));
  } else {
    s16x4 Q[4], K[4], V[4]; u32x2 Fv[4];
    const f32x4 cq = CS[nj * 4 + g], ck = CS[(4 + nj) * 4 + g], cf = CS[(8 + fu) * 4 + g];
    const float cv = ((LAS float*)CS)[(8 + nj) * 16 + r16];
#pragma unroll
    for (int rt = 0; rt < 4; ++rt) {
      const f32x2 mr = MR[64 * mi + 16 * rt + r16];
      Q[rt] = pack4(lnc(acc[0][rt], mr, cq)); K[rt] = pack4(lnc(acc[1][rt], mr, ck)); Fv[rt] = __builtin_bit_cast(u32x2, pack4(lnc(acc[3][rt], mr, cf)));
      const f32x4 t01 = *(LAS f32x4*)(MR + 64 * mi + 16 * rt + 4 * g), t23 = *(LAS f32x4*)(MR + 64 * mi + 16 * rt + 4 * g + 2);
      const f32x4 mm4 = {t01[0], t01[2], t23[0], t23[2]}, rr4 = {t01[1], t01[3], t23[1], t23[3]};
      V[rt] = pack4((acc[2][rt] - mm4 * cv) * rr4);
    }
    LAS char* kvb = lds + SC_KV + nj * 1536;
    const s16x4 hv = pack4(lnc(acch, MR[128 + r16], CS[((mi ? 8 : 4) + nj) * 4 + g]));
    if (mi == 0) { *(LAS s16x4*)(kvb + 512 + lane * 8) = K[3]; *(LAS s16x4*)(kvb + 1024 + lane * 8) = V[3]; }
    else *(LAS s16x4*)(kvb + r16 * 32 + g * 8) = hv;
#pragma unroll
    for (int rt = 0; rt < 4; ++rt) *(LAS u32x2*)(lds + SC_FX + ((mi * 4 + rt) * 4 + nj) * 512 + lane * 8) = Fv[rt];
    f32x4 mkp, mkc;
#pragma unroll
    for (int r = 0; r < 4; ++r) {
      const int dp = r16 + 16 - (4 * g + r), dc = r16 - (4 * g + r);
      mkp[r] = (!(dp & 1) && dp <= 14) ? 0.f : -__builtin_inff();
      mkc[r] = (!(dc & 1) && dc >= 0 && dc <= 14) ? 0.f : -__builtin_inff();
    }
    const bool odd = r16 & 1;
    const unsigned psh = odd ? 16u : 0u;
    const int RT0 = rg * 8 + 4 * mi;
    const unsigned od = ohl + (RT0 * NKS + 2 * cg + (nj >> 1)) * 1024;
    const s16x4 ones4 = {0x3f80, 0x3f80, 0x3f80, 0x3f80};
    const bf16x8 ones8 = __builtin_bit_cast(bf16x8, __builtin_shufflevector(ones4, ones4, 0, 1, 2, 3, 4, 5, 6, 7));
    auto attn = [&](s16x4 kp, s16x4 vp, s16x4 kc, s16x4 vc, s16x4 qq, f32x4 mp, int rt) {
      const f32x4 z = {0, 0, 0, 0};
      const f32x4 sp = mfma16(kp, qq, mp), sc = mfma16(kc, qq, mkc);
      const float sc2 = 0.25f * 1.44269504089f;
      const float e0 = odd ? sp[1] : sp[0], e1 = odd ? sp[3] : sp[2], e2 = odd ? sc[1] : sc[0], e3 = odd ? sc[3] : sc[2];
      const float mx = xg_max(fmaxf(fmaxf(e0, e1), fmaxf(e2, e3))) * sc2;
      const float p0 = __builtin_amdgcn_exp2f(fmaf(e0, sc2, -mx)), p1 = __builtin_amdgcn_exp2f(fmaf(e1, sc2, -mx));
      const float p2 = __builtin_amdgcn_exp2f(fmaf(e2, sc2, -mx)), p3 = __builtin_amdgcn_exp2f(fmaf(e3, sc2, -mx));
      const u32x2 k01 = __builtin_bit_cast(u32x2, pack4((f32x4){p0, 0.f, p1, 0.f})), k23 = __builtin_bit_cast(u32x2, pack4((f32x4){p2, 0.f, p3, 0.f}));
      const u32x4 pw4 = {k01[0] << psh, k01[1] << psh, k23[0] << psh, k23[1] << psh};
      const bf16x8 pf = __builtin_bit_cast(bf16x8, pw4);
      const bf16x8 vf = __builtin_bit_cast(bf16x8, __builtin_shufflevector(vp, vc, 0, 1, 2, 3, 4, 5, 6, 7));
      const f32x4 o = mfma32(vf, pf, z), sm = mfma32(ones8, pf, z);
      return __builtin_bit_cast(u32x2, pack4(o * __builtin_amdgcn_rcpf(sm[0])));
    };
    u32x2 O[4];
#pragma unroll
    for (int rt = 1; rt < 4; ++rt) O[rt] = attn(K[rt - 1], V[rt - 1], K[rt], V[rt], Q[rt], mkp, rt);
    LAS char* ox = lds + SC_OX + w * 1024;
    *(LAS u32x2*)(ox + lane * 8) = (nj & 1) ? O[1] : O[3];
    if (nj & 1) *(LAS u32x2*)(ox + 512 + lane * 8) = O[2];
    WAITL0(); BAR();
    {
      LAS char* oo = lds + SC_OX + (w ^ 1) * 1024;
      const u32x2 p0 = *(LAS u32x2*)(oo + lane * 8);
      if (nj & 1) st16(ohr, od + 3 * NKS * 1024, (u32x4){p0[0], p0[1], O[3][0], O[3][1]});
      else {
        const u32x2 p1 = *(LAS u32x2*)(oo + 512 + lane * 8);
        st16(ohr, od + 1 * NKS * 1024, (u32x4){O[1][0], O[1][1], p0[0], p0[1]});
        st16(ohr, od + 2 * NKS * 1024, (u32x4){O[2][0], O[2][1], p1[0], p1[1]});
      }
    }
    {
      LAS char* fo = lds + SC_FX + (mi * 4 + nj) * 2048 + lane * 8;
      const s16x4 g0 = *(LAS s16x4*)fo, u0 = *(LAS s16x4*)(fo + 512), g1 = *(LAS s16x4*)(fo + 1024), u1 = *(LAS s16x4*)(fo + 1536);
      const u32x2 h0 = __builtin_bit_cast(u32x2, pack4(swiglu4(unpack4(g0), unpack4(u0)))), h1 = __builtin_bit_cast(u32x2, pack4(swiglu4(unpack4(g1), unpack4(u1))));
      st16(ohr, ohl + ((RT0 + nj) * NKS + 16 + cg) * 1024, (u32x4){h0[0], h0[1], h1[0], h1[1]});
    }
    s16x4 Kp, Vp;
    if (mi == 0) { Kp = hv; Vp = __builtin_amdgcn_ds_read_tr16_b64_v4i16((LAS s16x4*)(kvb + (4 * g + q) * 32 + pp * 8)); }
    else { Kp = *(LAS s16x4*)(kvb + 512 + lane * 8); Vp = *(LAS s16x4*)(kvb + 1024 + lane * 8); }
    const float ninf = -__builtin_inff();
    O[0] = attn(Kp, Vp, K[0], V[0], Q[0], RT0 == 0 ? (f32x4){ninf, ninf, ninf, ninf} : mkp, 0);
    st8(ohr, od + (nj & 1) * 8, __builtin_bit_cast(s16x4, O[0]));
  }
  return tag;
}

constexpr int NR = (NKS + 3) / 4;
DI void gload16(u32x4& dst, const char* sbase, unsigned voff) { asm volatile("global_load_dwordx4 %0, %1, %2 sc1" : "=v"(dst) : "v"(voff), "s"(sbase) : "memory"); }
template <int N> DI void waitVn() { asm volatile("s_waitcnt vmcnt(%0)" :: "n"(N) : "memory"); }
constexpr int P2A = 6;
constexpr int P2NA = P2A + 1;
constexpr int P2R = 4;
__host__ __device__ constexpr int bslot2(int r) { return r < 8 ? bslot(r) : r < 12 ? bslot(r - 8) : r == 12 ? SC_HS : bslot(r - 9); }
constexpr int nA(int) { return 2; }
constexpr int p2issued(int r) { int n = 14; for (int q = 0; q < P2A; ++q) n += nA(q); for (int q = 0; q < r; ++q) n += (q + P2A < NR ? nA(q + P2A) : 0) + (q == 6 ? 4 : 0) + (q == 9 ? 2 : 0); return n; }
constexpr int p2posA(int r) { if (r < P2A) { int n = 14; for (int q = 0; q <= r; ++q) n += nA(q); return n; } return p2issued(r - P2A) + nA(r); }
constexpr int p2posB14() { return p2issued(6) + nA(6 + P2A) + 4; }
template <int R> struct P2W { static constexpr int n = p2issued(R) - ((R == 12 && p2posB14() > p2posA(R)) ? p2posB14() : p2posA(R)); };

DI void phase2(const Params& p, LAS char* lds, int rt64, int ct64, unsigned tag, bool boss) {
  const int tid = threadIdx.x, lane = tid & 63;
  const int w = rfl(tid >> 6), kh = w >> 2, m4 = w & 3;
  const int r16 = lane & 15, g = lane >> 4, q = r16 >> 2, pp = lane & 3;
  const unsigned ldsb = (unsigned)(uintptr_t)lds;
  const char* pa = p.ws + WS_OH + ((size_t)(rt64 * 4 + m4) * NKS + kh) * 1024;
  const unsigned va = lane * 16;
  unsigned vb;
  { const int row = 8 * m4 + (lane >> 3), pc = lane & 7; const int c = (pc >> 1) ^ ((row >> 1) & 3);
    vb = row * 1024 + ct64 * 128 + (2 * c + (pc & 1)) * 16; }
  const char* pb2 = p.w2 + (long)(kh - 16) * 32768;
  const char* pbo = p.wo + (long)kh * 32768;
  int bl[4];
#pragma unroll
  for (int c = 0; c < 4; ++c) bl[c] = (4 * g + q) * 128 + ((c ^ ((2 * g + (q >> 1)) & 3)) << 5) + pp * 8 + kh * 4096;
  auto readB = [&](bf16x8 (&dst)[2][4], int r) {
    LAS char* sb = lds + bslot2(r);
#pragma unroll
    for (int j = 0; j < 2; ++j)
#pragma unroll
      for (int c = 0; c < 4; ++c) dst[j][c] = ldsB(sb + j * 8192 + bl[c], 2048);
  };
  auto issueB = [&](int r) {
#pragma unroll
    for (int j = 0; j < 2; ++j) {
      const int ke = 4 * phys(r) + 2 * j;
      const bool over = (ke + 1 > NKS - 1);
      glds16s((ke < 16 ? pbo : pb2) + (over ? (long)ke - kh : (long)ke) * 32768, vb, ldsb + bslot2(r) + (2 * j) * 4096 + kh * 4096 + m4 * 1024);
    }
  };
  bf16x8 bp[P2R][2][4];
#pragma unroll
  for (int r = 0; r < P2R; ++r) readB(bp[r], r);
  WAITL0(); BAR();
#pragma unroll
  for (int r = 6; r <= 12; ++r) issueB(r);
  if (w == 0) {
    gu64* fl = (gu64*)(p.ws + WS_FLAGS) + (rt64 >> 1) * 16 + (lane & 15);
    const unsigned long long want = FMAGIC | tag;
    for (unsigned spins = 0; spins < (1u << 20); ++spins) {
      unsigned long long v;
      asm volatile("global_load_dwordx2 %0, %1, off sc1\n\ts_waitcnt vmcnt(0)" : "=v"(v) : "v"(fl) : "memory");
      if (__all(v == want)) break;
      __builtin_amdgcn_s_sleep(1);
    }
  }
  BAR();
  u32x4 A4[P2NA][2];
  auto issueA = [&](int r) {
#pragma unroll
    for (int j = 0; j < 2; ++j) {
      const int ke = 4 * phys(r) + 2 * j;
      const bool over = (ke + 1 > NKS - 1);
      gload16(A4[r % P2NA][j], pa + (over ? (long)ke - kh : (long)ke) * 1024, va);
    }
  };
#pragma unroll
  for (int r = 0; r < P2A; ++r) { issueA(r); if (r + 1 < P2A) BAR(); }
  f32x4 acc[4] = {{0, 0, 0, 0}, {0, 0, 0, 0}, {0, 0, 0, 0}, {0, 0, 0, 0}};
  const size_t ooff = (size_t)(rt64 * 64 + 16 * m4 + r16) * 1024 + (ct64 * 64 + 32 * kh + 4 * g) * 2;
  bf16x4 xv0, xv1;
  u32x4 fl0 = {0, 0, 0, 0}, fl1 = {0, 0, 0, 0};
  bf16x8 bf[2][2][4];
  auto round = [&](auto RC) {
    constexpr int r = decltype(RC)::value;
    waitVn<P2W<r>::n>();
    asm volatile("" : "+v"(A4[r % P2NA][0]), "+v"(A4[r % P2NA][1]));
    if (r == 5 || r == 6 || r == 12) BAR();
    if (r + P2A < NR) issueA(r + P2A);
    if (r == 6) { issueB(13); issueB(14); }
    if (r == 9) { asm volatile("global_load_dwordx2 %0, %2, %3\n\tglobal_load_dwordx2 %1, %2, %3 offset:32" : "=&v"(xv0), "=&v"(xv1) : "v"((unsigned)ooff), "s"(p.x) : "memory"); }
    if (r == 11 && boss) {
      const char* fb = p.ws + WS_FLAGS;
      asm volatile("global_load_dwordx4 %0, %2, %3 sc1\n\tglobal_load_dwordx4 %1, %2, %3 offset:1024 sc1" : "=&v"(fl0), "=&v"(fl1) : "v"(va), "s"(fb) : "memory");
    }
    SB0();
    if (r + 1 < NR && r + 1 >= P2R) readB(bf[(r + 1) & 1], r + 1);
#pragma unroll
    for (int j = 0; j < 2; ++j) {
      if (4 * phys(r) + 2 * j + 1 > NKS - 1 && kh) continue;
#pragma unroll
      for (int c = 0; c < 4; ++c) acc[c] = mfma32(r < P2R ? bp[r < P2R ? r : 0][j][c] : bf[r & 1][j][c], __builtin_bit_cast(bf16x8, A4[r % P2NA][j]), acc[c]);
    }
#pragma unroll
    for (int k = 0; k < 8; ++k) {
      __builtin_amdgcn_sched_group_barrier(0x8, 1, 0);
      __builtin_amdgcn_sched_group_barrier(0x100, 2, 0);
    }
    SB0();
  };
  static_for<NR>(round);
  {
    LAS f32x4* red = (LAS f32x4*)(lds + SC_RED);
    red[(w * 2 + 0) * 64 + lane] = kh ? acc[0] : acc[2];
    red[(w * 2 + 1) * 64 + lane] = kh ? acc[1] : acc[3];
    WAITL0(); BAR();
    const f32x4 o0 = red[((w ^ 4) * 2 + 0) * 64 + lane], o1 = red[((w ^ 4) * 2 + 1) * 64 + lane];
    const f32x4 s0 = (kh ? acc[2] : acc[0]) + o0, s1 = (kh ? acc[3] : acc[1]) + o1;
    asm volatile("s_waitcnt vmcnt(0)" : "+v"(xv0), "+v"(xv1) :: "memory");
    const f32x4 x0 = __builtin_convertvector(xv0, f32x4), x1 = __builtin_convertvector(xv1, f32x4);
    const u32x2 pa2 = __builtin_bit_cast(u32x2, pack4(s0 + x0)), pb2_ = __builtin_bit_cast(u32x2, pack4(s1 + x1));
    const auto d0 = __builtin_amdgcn_permlane16_swap(pa2[0], pb2_[0], false, false), d1 = __builtin_amdgcn_permlane16_swap(pa2[1], pb2_[1], false, false);
    const __amdgpu_buffer_rsrc_t outr = __builtin_amdgcn_make_buffer_rsrc(p.out, 0, 2048 * 1024, 0x00020000);
    st16(outr, (unsigned)((rt64 * 64 + 16 * m4 + r16) * 1024 + (ct64 * 64 + 32 * kh + (g & 1) * 16 + (g >> 1) * 8) * 2), (u32x4){d0[0], d1[0], d0[1], d1[1]});
  }
  if (boss) {
    asm volatile("s_waitcnt vmcnt(0)" : "+v"(fl0), "+v"(fl1) :: "memory");
    const unsigned hi = (unsigned)(FMAGIC >> 32);
    bool ok = fl0[0] == tag && fl0[1] == hi && fl0[2] == tag && fl0[3] == hi && fl1[0] == tag && fl1[1] == hi && fl1[2] == tag && fl1[3] == hi;
    if (!__all(ok)) {
      gu64* fl = (gu64*)(p.ws + WS_FLAGS) + lane;
      const unsigned long long want = FMAGIC | tag;
      for (unsigned spins = 0; spins < (1u << 20); ++spins) {
        ok = __hip_atomic_load(fl, RLX_AGENT) == want && __hip_atomic_load(fl + 64, RLX_AGENT) == want &&
             __hip_atomic_load(fl + 128, RLX_AGENT) == want && __hip_atomic_load(fl + 192, RLX_AGENT) == want;
        if (__all(ok)) break;
        __builtin_amdgcn_s_sleep(1);
      }
    }
    if (lane == 0) __hip_atomic_store((gu32*)p.ws, tag, RLX_AGENT);
  }
}

__global__ __launch_bounds__(512, 2) void k_fused(Params p) {
  extern __shared__ __attribute__((aligned(16))) char shm[];
  LAS char* lds = (LAS char*)shm;
  const int b = blockIdx.x, xcd = b & 7, idx = b >> 3;
  const int rg = (xcd & 1) * 8 + (idx & 7), cg = (xcd >> 1) * 4 + (idx >> 3);
  const int rt64 = xcd * 4 + (idx & 3), ct64 = idx >> 2;
  {
    const char* code0 = (const char*)(__builtin_amdgcn_s_getpc() & ~(unsigned long long)255);
    const unsigned ln = (blockIdx.x >> 3) * 8 + (threadIdx.x >> 6);
    asm volatile("s_mov_b32 m0, %2\n\ts_nop 0\n\tglobal_load_lds_dword %0, %1" :: "v"((ln < CODE_LINES ? ln : CODE_LINES - 1) * 128), "s"(code0), "s"((unsigned)(uintptr_t)lds + DUMMY) : "memory", "m0");
  }
  const unsigned tag = (cg < 8) ? phase1<true>(p, lds, rg, cg, ct64, idx) : phase1<false>(p, lds, rg, cg - 8, ct64, idx);
  asm volatile("s_waitcnt vmcnt(0)" ::: "memory");
  BAR();
  if (threadIdx.x == 0) __hip_atomic_store((gu64*)(p.ws + WS_FLAGS) + rg * 16 + cg, FMAGIC | tag, RLX_AGENT);
  phase2(p, lds, rt64, ct64, tag, b == 0 && rfl(threadIdx.x >> 6) == 0);
}

extern "C" void kernel_launch(void* const* d_in, const int* in_sizes, int n_in, void* d_out, int out_size, void* d_ws, size_t ws_size,
                              hipStream_t stream) {
  Params p;
  p.x = (const char*)d_in[0]; p.wqkv = (const char*)d_in[1]; p.wo = (const char*)d_in[2];
  p.w1 = (const char*)d_in[4]; p.w3 = (const char*)d_in[5]; p.w2 = (const char*)d_in[6];
  p.ws = (char*)d_ws; p.out = (char*)d_out;
  (void)hipFuncSetAttribute((const void*)k_fused, hipFuncAttributeMaxDynamicSharedMemorySize, P1_LDS);
  k_fused<<<256, 512, P1_LDS, stream>>>(p);
}
```
